```python
import torch

def _softmax(x, axis=-1):
    return torch.softmax(x, dim=axis)
def _gelu(x):
    return torch.nn.functional.gelu(x, approximate="tanh")
def _silu(x):
    return torch.nn.functional.silu(x)
def _sigmoid(x):
    return torch.sigmoid(x)
def _layernorm(x, axis=-1, eps=1e-5):
    m = x.mean(dim=axis, keepdim=True)
    v = x.var(dim=axis, keepdim=True, unbiased=False)
    return (x - m) / torch.sqrt(v + eps)
def _scatter_add(tgt, idx, src, axis=0):
    return tgt.clone().index_add_(axis, idx, src)
def _segment_sum(src, seg, n):
    out = torch.zeros((n,) + src.shape[1:], dtype=src.dtype)
    return out.index_add_(0, seg, src)
def _pad(x, pw):
    flat = tuple(v for pair in reversed(pw) for v in pair)
    return torch.nn.functional.pad(x, flat)

kernel_name = 'interleave_layers__sliding_local_dilated__dense_activation_squared_relu__prompt'

def setup_inputs():

    _w = (lambda a: (lambda t: t.to(torch.bfloat16) if t.is_floating_point() else t)(torch.as_tensor(a)))
    def _A_setup():
        B, S, D = 1, 2048, 640
        return {
            "x": _w((lambda *s: torch.randn(*s, dtype=torch.bfloat16) * 0.05)(B, S, D)),
            "wqkv": _w((lambda *s: torch.randn(*s, dtype=torch.bfloat16) * 0.05)(D, 3 * D)),
            "wo": _w((lambda *s: torch.randn(*s, dtype=torch.bfloat16) * 0.05)(D, D)),
        }
    _A_in = _A_setup()

    def _B_setup():
        B, S, D, F = 1, 2048, 640, 1728
        return {
            "x": _w((lambda *s: torch.randn(*s, dtype=torch.bfloat16) * 0.05)(B, S, D)),
            "w1": _w((lambda *s: torch.randn(*s, dtype=torch.bfloat16) * 0.05)(D, F)),
            "b1": _w((lambda *s: torch.randn(*s, dtype=torch.bfloat16) * 0.2)(F)),
            "w2": _w((lambda *s: torch.randn(*s, dtype=torch.bfloat16) * 0.05)(F, D)),
            "b2": _w((lambda *s: torch.randn(*s, dtype=torch.bfloat16) * 0.8)(D)),
        }
    _B_in = _B_setup()

    return {
        "A_x": _A_in["x"],
        "A_wqkv": _A_in["wqkv"],
        "A_wo": _A_in["wo"],
        "B_x": _B_in["x"],
        "B_w1": _B_in["w1"],
        "B_b1": _B_in["b1"],
        "B_w2": _B_in["w2"],
        "B_b2": _B_in["b2"],
    }

def reference(A_x, A_wqkv, A_wo, B_x, B_w1, B_b1, B_w2, B_b2):
    def _A_ref(x, wqkv, wo):
        B, S, D = x.shape
        H = 32; Dh = D // H
        W, dil = 8, 2
        qkv = (lambda x, *d: x.permute(*d))((x @ wqkv).reshape(B, S, 3, H, Dh), 2, 0, 3, 1, 4)
        q, k, v = qkv[0], qkv[1], qkv[2]
        scale = Dh ** -0.5
        pos = torch.arange(S)
        off = torch.arange(W) * dil
        idx = pos[:, None] - off[None, :]
        mask = idx >= 0
        idx_c = torch.where(mask, idx, 0).reshape(-1)
        kg = k[:, :, idx_c, :].reshape(B, H, S, W, Dh)
        vg = v[:, :, idx_c, :].reshape(B, H, S, W, Dh)
        logits = torch.einsum("bhsd,bhswd->bhsw", q, kg) * scale
        logits = torch.where(mask[None, None, :, :], logits, -1e9)
        attn = _softmax(logits, axis=-1)
        o = torch.einsum("bhsw,bhswd->bhsd", attn, vg)
        return (lambda x, *d: x.permute(*d))(o, 0, 2, 1, 3).reshape(B, S, D) @ wo

    def _B_ref(x, w1, b1, w2, b2):
        h = torch.einsum("bsd,df->bsf", x, w1) + b1
        zero = torch.zeros_like(h)
        r = torch.where(h > zero, h, zero)
        h = r * r
        out = torch.einsum("bsf,fd->bsd", h, w2) + b2
        return out

    x = A_x
    x = x + _A_ref(_layernorm(x, axis=-1), A_wqkv, A_wo)
    x = x + _B_ref(_layernorm(x, axis=-1), B_w1, B_b1, B_w2, B_b2)
    x = x + _A_ref(_layernorm(x, axis=-1), A_wqkv, A_wo)
    x = x + _B_ref(_layernorm(x, axis=-1), B_w1, B_b1, B_w2, B_b2)
    return x
```

```cpp
#include <hip/hip_runtime.h>
#include <hip/hip_bf16.h>
#include <stdint.h>

typedef unsigned v4u __attribute__((ext_vector_type(4)));
typedef unsigned v2u __attribute__((ext_vector_type(2)));
typedef float f32x4 __attribute__((ext_vector_type(4)));
typedef float f32x2 __attribute__((ext_vector_type(2)));
typedef __bf16 bf16x8 __attribute__((ext_vector_type(8)));
typedef __bf16 bf16x2 __attribute__((ext_vector_type(2)));
typedef short s16x4 __attribute__((ext_vector_type(4)));
#if defined(__HIP_DEVICE_COMPILE__)
#define NOPK __attribute__((target("no-packed-fp32-ops")))
#else
#define NOPK
#endif
#define GAS __attribute__((address_space(1)))
#define LAS __attribute__((address_space(3)))

constexpr int SEQ = 2048, DM = 640, FF = 1728;
constexpr int NTT = 128;
constexpr size_t WQKV_CU = 153600;
constexpr size_t W1_CU = 143360;
constexpr size_t WO_WV = 25600;
constexpr size_t W2_WV = 69120;
constexpr size_t OFF_FLAGS = 0;
constexpr size_t OFF_WQKV = 65536;
constexpr size_t OFF_W1 = OFF_WQKV + 16 * WQKV_CU;
constexpr size_t OFF_WO = OFF_W1 + 16 * W1_CU;
constexpr size_t OFF_W2 = OFF_WO + 32 * WO_WV;
constexpr size_t OFF_CSQ = OFF_W2 + 32 * W2_WV;
constexpr size_t OFF_CS1 = OFF_CSQ + 16 * 128 * 4;
constexpr size_t OFF_X = OFF_CS1 + 16384;
constexpr size_t XBUF = (size_t)NTT * 20 * 1024;
constexpr size_t OFF_ST = OFF_X + 4 * XBUF;
constexpr size_t STBUF = (size_t)NTT * 8 * 128;
constexpr size_t OFF_O = OFF_ST + 4 * STBUF;
constexpr size_t OFF_H = OFF_O + 2 * XBUF;
constexpr size_t HBUF = (size_t)64 * 4 * 27 * 1024;
constexpr size_t OFF_RES = OFF_H + 2 * HBUF;
constexpr size_t WS_END = OFF_RES + (size_t)256 * 256 * 80;

__host__ __device__ constexpr int xcol(int ks, int g, int j) {
  return (ks % 5) < 4 ? 80 * (2 * (ks / 5) + ((ks % 5) >> 1)) + 16 * (2 * ((ks % 5) & 1) + (j >> 2)) + 4 * g + (j & 3)
                      : 80 * (2 * (ks / 5) + (g >> 1)) + 64 + 8 * (j >> 2) + 4 * (g & 1) + (j & 3);
}
__host__ __device__ constexpr int ocol(int ks, int g, int j) {
  return (ks % 5) < 4 ? 40 * (4 * (ks / 5) + (ks % 5)) + 16 * (j >> 2) + 4 * g + (j & 3)
                      : 40 * (4 * (ks / 5) + g) + 32 + j;
}
__host__ __device__ constexpr int hrow(int w, int st, int g, int j) {
  return st < 12 ? 432 * w + 112 * (st / 3) + 16 * (2 * (st % 3) + (j >> 2)) + 4 * g + (j & 3)
       : st == 12 ? 432 * w + 112 * (j >> 2) + 96 + 4 * g + (j & 3)
                  : 432 * w + 224 + 96 + 4 * g + (j & 3);
}
__host__ __device__ constexpr int qkvcol(int c, int tile, int i) {
  return tile == 0 ? 40 * c + i
       : tile == 1 ? 640 + 40 * c + i
       : tile == 2 ? 40 * c + 20 + i
       : tile == 3 ? 640 + 40 * c + 20 + i
       : tile == 4 ? ((i >> 3) * 640 + 40 * c + 20 * ((i >> 2) & 1) + 16 + (i & 3))
                   : 1280 + 40 * c + 16 * (tile - 5) + i;
}

struct Params {
  const uint16_t *x, *wqkv, *wo, *w1, *b1, *w2, *b2;
  uint16_t *out;
  unsigned char *ws;
};

__device__ __forceinline__ void wg_sync() {
  __builtin_amdgcn_fence(__ATOMIC_RELEASE, "workgroup");
  __builtin_amdgcn_s_barrier();
  __builtin_amdgcn_fence(__ATOMIC_ACQUIRE, "workgroup");
}
__device__ __forceinline__ float shfl(float v, int src) {
  return __builtin_bit_cast(float, __builtin_amdgcn_ds_bpermute(src << 2, __builtin_bit_cast(int, v)));
}
#define AI __attribute__((always_inline))
__device__ __forceinline__ float u2f(unsigned u) { return __builtin_bit_cast(float, u); }
__device__ __forceinline__ float bf2f(unsigned b) { return __builtin_bit_cast(float, b << 16); }
__device__ __forceinline__ float bflo(unsigned u) { return __builtin_bit_cast(float, u << 16); }
__device__ __forceinline__ float bfhi(unsigned u) { return __builtin_bit_cast(float, u & 0xffff0000u); }
__device__ __forceinline__ unsigned pk2(float a, float b) {
  f32x2 v = {a, b};
  return __builtin_bit_cast(unsigned, __builtin_convertvector(v, bf16x2));
}
__device__ __forceinline__ v2u pk4(f32x4 v) { return v2u{pk2(v[0], v[1]), pk2(v[2], v[3])}; }
__device__ __forceinline__ f32x4 mfma32(v4u a, v4u b, f32x4 c) {
  return __builtin_amdgcn_mfma_f32_16x16x32_bf16(__builtin_bit_cast(bf16x8, a), __builtin_bit_cast(bf16x8, b), c, 0, 0, 0);
}
__device__ __forceinline__ f32x4 mfma16(v2u a, v2u b, f32x4 c) {
  return __builtin_amdgcn_mfma_f32_16x16x16bf16_1k(__builtin_bit_cast(s16x4, a), __builtin_bit_cast(s16x4, b), c, 0, 0, 0);
}
__device__ __forceinline__ float xrow_max(float x) {
  auto s = __builtin_amdgcn_permlane16_swap(__builtin_bit_cast(unsigned, x), __builtin_bit_cast(unsigned, x), false, false);
  x = __builtin_fmaxf(u2f(s[0]), u2f(s[1]));
  auto t = __builtin_amdgcn_permlane32_swap(__builtin_bit_cast(unsigned, x), __builtin_bit_cast(unsigned, x), false, false);
  return __builtin_fmaxf(u2f(t[0]), u2f(t[1]));
}
__device__ __forceinline__ float xrow_sum(float x) {
  auto s = __builtin_amdgcn_permlane16_swap(__builtin_bit_cast(unsigned, x), __builtin_bit_cast(unsigned, x), false, false);
  x = u2f(s[0]) + u2f(s[1]);
  auto t = __builtin_amdgcn_permlane32_swap(__builtin_bit_cast(unsigned, x), __builtin_bit_cast(unsigned, x), false, false);
  return u2f(t[0]) + u2f(t[1]);
}
__device__ __forceinline__ int tok_row(int p) { return 2 * (p & 1023) + (p >> 10); }

constexpr int NG_QKV = 16 * 20 * 480, NG_W1 = 16 * 20 * 448, NG_WO = 32 * 1600, NG_W2 = 32 * 13 * 320, NG_W2H = 32 * 320;
constexpr int NG_X = NTT * 20 * 64;
constexpr int G1 = NG_QKV, G2 = G1 + NG_W1, G3 = G2 + NG_WO, G4 = G3 + NG_W2, G5 = G4 + NG_W2H, G6 = G5 + NG_X;
constexpr int G7 = G6 + 16 * 128, G8 = G7 + 16 * 112, G9 = G8 + NTT * 8 * 16;

__global__ void k_prep(Params p) {
  const int id = blockIdx.x * 256 + threadIdx.x;
  if (id < G1) {
    const int c = id / 9600, ks = (id / 480) % 20, q = id % 480;
    int tile, g, i;
    if (q < 448) { tile = q >> 6; g = (q >> 4) & 3; i = q & 15; } else { tile = 7; g = (q - 448) >> 3; i = q & 7; }
    const int n = qkvcol(c, tile, i);
    unsigned e[8];
    for (int j = 0; j < 8; ++j) e[j] = p.wqkv[(size_t)xcol(ks, g, j) * 1920 + n];
    *(v4u *)(p.ws + OFF_WQKV + (size_t)id * 16) = v4u{e[0] | e[1] << 16, e[2] | e[3] << 16, e[4] | e[5] << 16, e[6] | e[7] << 16};
  } else if (id < G2) {
    const int u = id - G1;
    const int c = u / 8960, ks = (u / 448) % 20, q = u % 448, tile = q >> 6, g = (q >> 4) & 3, i = q & 15;
    const bool ok = !((c & 3) == 3 && tile == 6);
    const int n = 432 * (c >> 2) + 112 * (c & 3) + 16 * tile + i;
    unsigned e[8];
    for (int j = 0; j < 8; ++j) e[j] = ok ? p.w1[(size_t)xcol(ks, g, j) * FF + n] : 0;
    *(v4u *)(p.ws + OFF_W1 + (size_t)u * 16) = v4u{e[0] | e[1] << 16, e[2] | e[3] << 16, e[4] | e[5] << 16, e[6] | e[7] << 16};
  } else if (id < G3) {
    const int u = id - G2;
    const int ct = u / 6400, ks = (u / 320) % 20, tile = (u >> 6) % 5, g = (u >> 4) & 3, i = u & 15;
    const int n = 80 * ct + 16 * tile + i;
    unsigned e[8];
    for (int j = 0; j < 8; ++j) e[j] = p.wo[(size_t)ocol(ks, g, j) * DM + n];
    *(v4u *)(p.ws + OFF_WO + (size_t)u * 16) = v4u{e[0] | e[1] << 16, e[2] | e[3] << 16, e[4] | e[5] << 16, e[6] | e[7] << 16};
  } else if (id < G4) {
    const int u = id - G3;
    const int cw = u / 4160, st = (u / 320) % 13, tile = (u >> 6) % 5, g = (u >> 4) & 3, i = u & 15;
    const int n = 80 * (cw >> 2) + 16 * tile + i;
    unsigned e[8];
    for (int j = 0; j < 8; ++j) e[j] = p.w2[(size_t)hrow(cw & 3, st, g, j) * DM + n];
    *(v4u *)(p.ws + OFF_W2 + (size_t)cw * W2_WV + (size_t)(u % 4160) * 16) = v4u{e[0] | e[1] << 16, e[2] | e[3] << 16, e[4] | e[5] << 16, e[6] | e[7] << 16};
  } else if (id < G5) {
    const int u = id - G4;
    const int cw = u / 320, tile = (u >> 6) % 5, g = (u >> 4) & 3, i = u & 15;
    const int n = 80 * (cw >> 2) + 16 * tile + i;
    unsigned e[4];
    for (int j = 0; j < 4; ++j) e[j] = p.w2[(size_t)hrow(cw & 3, 13, g, j) * DM + n];
    *(v2u *)(p.ws + OFF_W2 + (size_t)cw * W2_WV + 66560 + (size_t)(u % 320) * 8) = v2u{e[0] | e[1] << 16, e[2] | e[3] << 16};
  } else if (id < G6) {
    const int u = id - G5;
    const int tt = u / 1280, ks = (u >> 6) % 20, g = (u >> 4) & 3, t = u & 15;
    const uint16_t *row = p.x + (size_t)tok_row(16 * tt + t) * DM;
    const v2u a = *(const v2u *)(row + xcol(ks, g, 0)), b = *(const v2u *)(row + xcol(ks, g, 4));
    *(v4u *)(p.ws + OFF_X + (size_t)u * 16) = v4u{a.x, a.y, b.x, b.y};
  } else if (id < G7) {
    const int u = id - G6, c = u >> 7, tile = (u >> 4) & 7, i = u & 15;
    float s = 0.f;
    if (tile < 7 || i < 8) {
      const int n = qkvcol(c, tile, i);
      for (int k = 0; k < DM; ++k) s += bf2f(p.wqkv[(size_t)k * 1920 + n]);
    }
    ((float *)(p.ws + OFF_CSQ))[u] = s;
  } else if (id < G8) {
    const int u = id - G7, c = u / 112, r = u % 112;
    float s = 0.f, b = 0.f;
    if (!((c & 3) == 3 && r >= 96)) {
      const int n = 432 * (c >> 2) + 112 * (c & 3) + r;
      for (int k = 0; k < DM; ++k) s += bf2f(p.w1[(size_t)k * FF + n]);
      b = bf2f(p.b1[n]);
    }
    ((float *)(p.ws + OFF_CS1))[u] = s;
    ((float *)(p.ws + OFF_CS1))[16 * 112 + u] = b;
  } else if (id < G9) {
    const int u = id - G8, tt = u >> 7, ct = (u >> 4) & 7, t = u & 15;
    const uint16_t *row = p.x + (size_t)tok_row(16 * tt + t) * DM + 80 * ct;
    float s = 0.f;
    for (int k = 0; k < 80; ++k) s += bf2f(row[k]);
    const float m = s * (1.f / 80.f);
    float q = 0.f;
    for (int k = 0; k < 80; ++k) { const float d = bf2f(row[k]) - m; q += d * d; }
    *(f32x2 *)(p.ws + OFF_ST + (size_t)u * 8) = f32x2{s, q};
  }
}

__device__ __forceinline__ void wg_map(int b, int &pn, int &c) {
  const int x = b & 7, r = b >> 3;
  pn = 4 * (x >> 1) + (r >> 3);
  c = 8 * (x & 1) + (r & 7);
}

__device__ __forceinline__ void load_stats(const unsigned char *st, int tt, int lane, float &mean, float &rstd) {
  const int g = lane >> 4, t = lane & 15;
  const f32x2 a = *(const f32x2 *)(st + ((size_t)(tt * 8 + 2 * g) * 16 + t) * 8);
  const f32x2 b = *(const f32x2 *)(st + ((size_t)(tt * 8 + 2 * g + 1) * 16 + t) * 8);
  mean = xrow_sum(a.x + b.x) * (1.f / 640.f);
  const float da = a.x * (1.f / 80.f) - mean, db = b.x * (1.f / 80.f) - mean;
  const float m2 = xrow_sum(a.y + b.y + 80.f * (da * da + db * db));
  rstd = __builtin_amdgcn_rsqf(m2 * (1.f / 640.f) + 1e-5f);
}

struct KV { v2u k0, k1, es, v0, v1, v2; };

__global__ NOPK void __launch_bounds__(256, 1) k_p1(Params p, int layer) {
  extern __shared__ __attribute__((aligned(16))) unsigned char smem[];
  LAS unsigned char *L = (LAS unsigned char *)smem;
  const int tid = threadIdx.x, lane = tid & 63, w = __builtin_amdgcn_readfirstlane(tid >> 6);
  const int g = lane >> 4, t = lane & 15;
  int pn, c;
  wg_map(blockIdx.x, pn, c);
  const unsigned char *wsrc = p.ws + OFF_WQKV + (size_t)c * WQKV_CU;
  for (int i = w; i < 150; i += 4)
    __builtin_amdgcn_global_load_lds((const GAS unsigned *)(wsrc + i * 1024 + lane * 16), (LAS unsigned *)(L + i * 1024), 16, 0, 0);
  const unsigned char *xb = p.ws + OFF_X + (size_t)(2 * layer) * XBUF;
  const unsigned char *st = p.ws + OFF_ST + (size_t)(2 * layer) * STBUF;
  const int T0 = 8 * pn + 2 * w;
  const bool has_halo = (pn & 7) != 0;
  const int Th = has_halo ? 8 * pn - 1 : 8 * pn;
  v4u xa[2][20], xh[5];
#pragma unroll
  for (int i = 0; i < 20; ++i) {
    const int ks = (((i / 5) + w) & 3) * 5 + i % 5;
    xa[0][i] = *(const v4u *)(xb + ((size_t)T0 * 20 + ks) * 1024 + lane * 16);
    xa[1][i] = *(const v4u *)(xb + ((size_t)(T0 + 1) * 20 + ks) * 1024 + lane * 16);
    if (i < 5) xh[i] = *(const v4u *)(xb + ((size_t)Th * 20 + ks) * 1024 + lane * 16);
  }
  float mean[3], rstd[3];
  load_stats(st, T0, lane, mean[0], rstd[0]);
  load_stats(st, T0 + 1, lane, mean[1], rstd[1]);
  load_stats(st, Th, lane, mean[2], rstd[2]);
  const float *csq = (const float *)(p.ws + OFF_CSQ) + c * 128;
  f32x4 cs[5];
  float csv[3];
#pragma unroll
  for (int i = 0; i < 5; ++i) cs[i] = *(const f32x4 *)(csq + 16 * i + 4 * g);
#pragma unroll
  for (int i = 0; i < 3; ++i) csv[i] = csq[80 + 16 * i + t];
  f32x4 meanR[3], rstdR[3];
#pragma unroll
  for (int x = 0; x < 3; ++x)
#pragma unroll
    for (int r = 0; r < 4; ++r) {
      meanR[x][r] = shfl(mean[x], 4 * g + r);
      rstdR[x][r] = shfl(rstd[x], 4 * g + r);
    }
  asm volatile("s_waitcnt vmcnt(0)" ::: "memory");
  wg_sync();

  f32x4 acc[2][8], hacc[6];
#pragma unroll
  for (int i = 0; i < 8; ++i) { acc[0][i] = f32x4{0, 0, 0, 0}; acc[1][i] = f32x4{0, 0, 0, 0}; }
#pragma unroll
  for (int i = 0; i < 6; ++i) hacc[i] = f32x4{0, 0, 0, 0};
#pragma unroll
  for (int i = 0; i < 20; ++i) {
    const int kb = (((i / 5) + w) & 3) * 38400 + (i % 5) * 7680;
    v4u wf[8];
#pragma unroll
    for (int n = 0; n < 7; ++n) wf[n] = *(LAS v4u *)(L + kb + n * 1024 + lane * 16);
    wf[7] = *(LAS v4u *)(L + kb + 7168 + (g * 8 + (t & 7)) * 16);
#pragma unroll
    for (int n = 0; n < 8; ++n) {
#pragma unroll
      for (int x = 0; x < 2; ++x)
        acc[x][n] = n < 5 ? mfma32(wf[n], xa[x][i], acc[x][n]) : mfma32(xa[x][i], wf[n], acc[x][n]);
    }
    if (i < 5) {
      hacc[0] = mfma32(wf[1], xh[i], hacc[0]);
      hacc[1] = mfma32(wf[3], xh[i], hacc[1]);
      hacc[2] = mfma32(wf[4], xh[i], hacc[2]);
      hacc[3] = mfma32(xh[i], wf[5], hacc[3]);
      hacc[4] = mfma32(xh[i], wf[6], hacc[4]);
      hacc[5] = mfma32(xh[i], wf[7], hacc[5]);
    }
  }
  const float qs = 0.22360679774997896f * 1.4426950408889634f;
  auto fixT = [&](f32x4 a, f32x4 csx, float m, float rs) AI { return (a - m * csx) * rs; };
  auto fixV = [&](f32x4 a, float csx, f32x4 m, f32x4 rs) AI { return (a - m * csx) * rs; };
  auto swap_hi = [&](v2u e) AI {
    auto rx = __builtin_amdgcn_permlane32_swap(e.x, e.x, false, false);
    auto ry = __builtin_amdgcn_permlane32_swap(e.y, e.y, false, false);
    return v2u{rx[1], ry[1]};
  };
  v2u q0[2], q1[2], eq[2];
  KV kv[2];
#pragma unroll
  for (int x = 0; x < 2; ++x) {
    q0[x] = pk4(fixT(acc[x][0], cs[0], mean[x], rstd[x] * qs));
    kv[x].k0 = pk4(fixT(acc[x][1], cs[1], mean[x], rstd[x]));
    q1[x] = pk4(fixT(acc[x][2], cs[2], mean[x], rstd[x] * qs));
    kv[x].k1 = pk4(fixT(acc[x][3], cs[3], mean[x], rstd[x]));
    eq[x] = pk4(fixT(acc[x][4], cs[4], mean[x], g < 2 ? rstd[x] * qs : rstd[x]));
    kv[x].es = swap_hi(eq[x]);
    kv[x].v0 = pk4(fixV(acc[x][5], csv[0], meanR[x], rstdR[x]));
    kv[x].v1 = pk4(fixV(acc[x][6], csv[1], meanR[x], rstdR[x]));
    kv[x].v2 = pk4(fixV(acc[x][7], csv[2], meanR[x], rstdR[x]));
  }
  wg_sync();
  {
    LAS v4u *ex = (LAS v4u *)(L + w * 3072);
    ex[lane] = v4u{kv[1].k0.x, kv[1].k0.y, kv[1].k1.x, kv[1].k1.y};
    ex[64 + lane] = v4u{kv[1].es.x, kv[1].es.y, kv[1].v0.x, kv[1].v0.y};
    ex[128 + lane] = v4u{kv[1].v1.x, kv[1].v1.y, kv[1].v2.x, kv[1].v2.y};
    LAS f32x4 *hp = (LAS f32x4 *)(L + 16384 + w * 6144);
    if (w != 0) {
#pragma unroll
      for (int i = 0; i < 6; ++i) hp[i * 64 + lane] = hacc[i];
    }
  }
  wg_sync();
  KV pv;
  bool prev_ok = true;
  if (w != 0) {
    LAS v4u *ex = (LAS v4u *)(L + (w - 1) * 3072);
    const v4u a = ex[lane], b = ex[64 + lane], d = ex[128 + lane];
    pv.k0 = v2u{a.x, a.y}; pv.k1 = v2u{a.z, a.w}; pv.es = v2u{b.x, b.y};
    pv.v0 = v2u{b.z, b.w}; pv.v1 = v2u{d.x, d.y}; pv.v2 = v2u{d.z, d.w};
  } else {
#pragma unroll
    for (int ww = 1; ww < 4; ++ww) {
      LAS f32x4 *hp = (LAS f32x4 *)(L + 16384 + ww * 6144);
#pragma unroll
      for (int i = 0; i < 6; ++i) hacc[i] += hp[i * 64 + lane];
    }
    pv.k0 = pk4(fixT(hacc[0], cs[1], mean[2], rstd[2]));
    pv.k1 = pk4(fixT(hacc[1], cs[3], mean[2], rstd[2]));
    pv.es = swap_hi(pk4(fixT(hacc[2], cs[4], mean[2], rstd[2])));
    pv.v0 = pk4(fixV(hacc[3], csv[0], meanR[2], rstdR[2]));
    pv.v1 = pk4(fixV(hacc[4], csv[1], meanR[2], rstdR[2]));
    pv.v2 = pk4(fixV(hacc[5], csv[2], meanR[2], rstdR[2]));
    prev_ok = has_halo;
  }
  v2u m0[2], m1[2], m2[2];
#pragma unroll
  for (int x = 0; x < 2; ++x) {
    const KV &P = x == 0 ? pv : kv[0];
    const KV &C = kv[x];
    const bool pok = x == 0 ? prev_ok : true;
    f32x4 o[2][2];
#pragma unroll
    for (int h = 0; h < 2; ++h) {
      const v2u qq = h == 0 ? q0[x] : q1[x];
      const v4u qop = v4u{qq.x, qq.y, g == h ? eq[x].x : 0u, g == h ? eq[x].y : 0u};
      const v2u kp = h == 0 ? P.k0 : P.k1, kc = h == 0 ? C.k0 : C.k1;
      f32x4 sp = mfma32(v4u{kp.x, kp.y, P.es.x, P.es.y}, qop, f32x4{0, 0, 0, 0});
      f32x4 sc = mfma32(v4u{kc.x, kc.y, C.es.x, C.es.y}, qop, f32x4{0, 0, 0, 0});
      float mx = -INFINITY;
#pragma unroll
      for (int r = 0; r < 4; ++r) {
        const int j = 4 * g + r;
        sp[r] = (pok && j >= t + 9) ? sp[r] : -INFINITY;
        sc[r] = (j <= t && j >= t - 7) ? sc[r] : -INFINITY;
        mx = __builtin_fmaxf(mx, __builtin_fmaxf(sp[r], sc[r]));
      }
      mx = xrow_max(mx);
      float l = 0.f;
#pragma unroll
      for (int r = 0; r < 4; ++r) {
        sp[r] = __builtin_amdgcn_exp2f(sp[r] - mx);
        sc[r] = __builtin_amdgcn_exp2f(sc[r] - mx);
        l += sp[r] + sc[r];
      }
      l = xrow_sum(l);
      const float il = __builtin_amdgcn_rcpf(l);
      const v2u pp = pk4(sp), pc = pk4(sc);
      const v4u pop = v4u{pp.x, pp.y, pc.x, pc.y};
      const v2u va_p = h == 0 ? P.v0 : P.v1, va_c = h == 0 ? C.v0 : C.v1;
      const v2u vb_p = h == 0 ? P.v1 : P.v2, vb_c = h == 0 ? C.v1 : C.v2;
      o[h][0] = mfma32(v4u{va_p.x, va_p.y, va_c.x, va_c.y}, pop, f32x4{0, 0, 0, 0}) * il;
      o[h][1] = mfma32(v4u{vb_p.x, vb_p.y, vb_c.x, vb_c.y}, pop, f32x4{0, 0, 0, 0}) * il;
    }
    m0[x] = pk4(o[0][0]);
    m1[x] = pk4(g == 0 ? o[0][1] : o[1][0]);
    m2[x] = pk4(o[1][1]);
  }
  unsigned char *ob = p.ws + OFF_O + (size_t)layer * XBUF;
  const int ksf = 5 * (c >> 2) + (c & 3), ksq = 5 * (c >> 2) + 4;
#pragma unroll
  for (int x = 0; x < 2; ++x)
    *(v4u *)(ob + ((size_t)(T0 + x) * 20 + ksf) * 1024 + lane * 16) = v4u{m0[x].x, m0[x].y, m1[x].x, m1[x].y};
  {
    auto rx = __builtin_amdgcn_permlane16_swap(m2[0].x, m2[1].x, false, false);
    auto ry = __builtin_amdgcn_permlane16_swap(m2[0].y, m2[1].y, false, false);
    if (lane < 32)
      *(v4u *)(ob + ((size_t)(T0 + g) * 20 + ksq) * 1024 + ((c & 3) * 16 + t) * 16) = v4u{rx[0], ry[0], rx[1], ry[1]};
  }
}

__global__ NOPK void __launch_bounds__(256, 1) k_p4(Params p, int layer) {
  extern __shared__ __attribute__((aligned(16))) unsigned char smem[];
  LAS unsigned char *L = (LAS unsigned char *)smem;
  const int tid = threadIdx.x, lane = tid & 63, w = __builtin_amdgcn_readfirstlane(tid >> 6);
  const int g = lane >> 4;
  int pn, c;
  wg_map(blockIdx.x, pn, c);
  const unsigned char *wsrc = p.ws + OFF_W1 + (size_t)c * W1_CU;
  for (int i = w; i < 140; i += 4)
    __builtin_amdgcn_global_load_lds((const GAS unsigned *)(wsrc + i * 1024 + lane * 16), (LAS unsigned *)(L + i * 1024), 16, 0, 0);
  const unsigned char *xb = p.ws + OFF_X + (size_t)(2 * layer + 1) * XBUF;
  const unsigned char *st = p.ws + OFF_ST + (size_t)(2 * layer + 1) * STBUF;
  const int T0 = 8 * pn + 2 * w;
  v4u xa[2][20];
#pragma unroll
  for (int i = 0; i < 20; ++i) {
    xa[0][i] = *(const v4u *)(xb + ((size_t)T0 * 20 + i) * 1024 + lane * 16);
    xa[1][i] = *(const v4u *)(xb + ((size_t)(T0 + 1) * 20 + i) * 1024 + lane * 16);
  }
  float mean[2], rstd[2];
  load_stats(st, T0, lane, mean[0], rstd[0]);
  load_stats(st, T0 + 1, lane, mean[1], rstd[1]);
  const float *cs1 = (const float *)(p.ws + OFF_CS1) + c * 112;
  f32x4 cs[7], bb[7];
#pragma unroll
  for (int i = 0; i < 7; ++i) {
    cs[i] = *(const f32x4 *)(cs1 + 16 * i + 4 * g);
    bb[i] = *(const f32x4 *)(cs1 + 16 * 112 + 16 * i + 4 * g);
  }
  asm volatile("s_waitcnt vmcnt(0)" ::: "memory");
  wg_sync();
  f32x4 acc[2][7];
#pragma unroll
  for (int i = 0; i < 7; ++i) { acc[0][i] = f32x4{0, 0, 0, 0}; acc[1][i] = f32x4{0, 0, 0, 0}; }
#pragma unroll
  for (int i = 0; i < 20; ++i) {
    v4u wf[7];
#pragma unroll
    for (int n = 0; n < 7; ++n) wf[n] = *(LAS v4u *)(L + i * 7168 + n * 1024 + lane * 16);
#pragma unroll
    for (int n = 0; n < 7; ++n)
#pragma unroll
      for (int x = 0; x < 2; ++x) acc[x][n] = mfma32(wf[n], xa[x][i], acc[x][n]);
  }
  v2u hp[2][7];
#pragma unroll
  for (int x = 0; x < 2; ++x)
#pragma unroll
    for (int n = 0; n < 7; ++n) {
      f32x4 v = (acc[x][n] - mean[x] * cs[n]) * rstd[x] + bb[n];
#pragma unroll
      for (int r = 0; r < 4; ++r) { const float u = __builtin_fmaxf(v[r], 0.f); v[r] = u * u; }
      hp[x][n] = pk4(v);
    }
  unsigned char *hb = p.ws + OFF_H + (size_t)layer * HBUF + ((size_t)(T0 >> 1) * 4 + (c >> 2)) * 27648;
  const int q = c & 3;
#pragma unroll
  for (int b = 0; b < 3; ++b)
#pragma unroll
    for (int x = 0; x < 2; ++x)
      *(v4u *)(hb + ((3 * q + b) * 2 + x) * 1024 + lane * 16) = v4u{hp[x][2 * b].x, hp[x][2 * b].y, hp[x][2 * b + 1].x, hp[x][2 * b + 1].y};
  if (q < 3) *(v4u *)(hb + (24 + q) * 1024 + lane * 16) = v4u{hp[0][6].x, hp[0][6].y, hp[1][6].x, hp[1][6].y};
}

__device__ __forceinline__ void resid_publish(const Params &p, int xbuf, int tt, int ct, int lane, f32x4 (&v)[5], bool final_out) {
  const int g = lane >> 4, t = lane & 15;
  v2u pk[5];
#pragma unroll
  for (int i = 0; i < 5; ++i) pk[i] = pk4(v[i]);
  if (final_out) {
    uint16_t *row = p.out + (size_t)tok_row(16 * tt + t) * DM + 80 * ct + 4 * g;
#pragma unroll
    for (int i = 0; i < 5; ++i) *(v2u *)(row + 16 * i) = pk[i];
    return;
  }
  unsigned char *xb = p.ws + OFF_X + (size_t)xbuf * XBUF + (size_t)tt * 20480;
  const int ks0 = 5 * (ct >> 1) + 2 * (ct & 1);
  *(v4u *)(xb + ks0 * 1024 + lane * 16) = v4u{pk[0].x, pk[0].y, pk[1].x, pk[1].y};
  *(v4u *)(xb + (ks0 + 1) * 1024 + lane * 16) = v4u{pk[2].x, pk[2].y, pk[3].x, pk[3].y};
  {
    auto rx = __builtin_amdgcn_permlane32_swap(pk[4].x, pk[4].x, false, false);
    auto ry = __builtin_amdgcn_permlane32_swap(pk[4].y, pk[4].y, false, false);
    if (lane < 32) *(v4u *)(xb + (5 * (ct >> 1) + 4) * 1024 + ((ct & 1) * 32 + lane) * 16) = v4u{rx[0], ry[0], rx[1], ry[1]};
  }
  float s = 0.f;
  float r[20];
#pragma unroll
  for (int i = 0; i < 5; ++i) {
    r[4 * i] = bflo(pk[i].x); r[4 * i + 1] = bfhi(pk[i].x); r[4 * i + 2] = bflo(pk[i].y); r[4 * i + 3] = bfhi(pk[i].y);
    s += (r[4 * i] + r[4 * i + 1]) + (r[4 * i + 2] + r[4 * i + 3]);
  }
  s = xrow_sum(s);
  const float m = s * (1.f / 80.f);
  float q = 0.f;
#pragma unroll
  for (int i = 0; i < 20; ++i) { const float d = r[i] - m; q += d * d; }
  q = xrow_sum(q);
  if (lane < 16) *(f32x2 *)(p.ws + OFF_ST + (size_t)xbuf * STBUF + ((size_t)(tt * 8 + ct) * 16 + t) * 8) = f32x2{s, q};
}

__global__ NOPK void __launch_bounds__(256, 1) k_p3(Params p, int layer) {
  extern __shared__ __attribute__((aligned(16))) unsigned char smem[];
  LAS unsigned char *L = (LAS unsigned char *)smem;
  const int tid = threadIdx.x, lane = tid & 63, w = __builtin_amdgcn_readfirstlane(tid >> 6);
  const int g = lane >> 4, t = lane & 15;
  int pn, c;
  wg_map(blockIdx.x, pn, c);
  const int ct = c >> 1, hf = c & 1;
  const int TB = 8 * pn + 4 * hf;
  const unsigned char *wsrc = p.ws + OFF_WO + (size_t)(ct * 4 + w) * WO_WV + lane * 16;
  v4u wf[5][5];
#pragma unroll
  for (int s = 0; s < 5; ++s)
#pragma unroll
    for (int n = 0; n < 5; ++n) wf[s][n] = *(const v4u *)(wsrc + (s * 5 + n) * 1024);
  const unsigned char *ob = p.ws + OFF_O + (size_t)layer * XBUF;
  v4u xa[4][5];
#pragma unroll
  for (int i = 0; i < 4; ++i) {
    const int tt = TB + ((w + 1 + i) & 3);
#pragma unroll
    for (int s = 0; s < 5; ++s) xa[i][s] = *(const v4u *)(ob + ((size_t)tt * 20 + 5 * w + s) * 1024 + lane * 16);
  }
  f32x4 res[5];
  f32x4 *rp = (f32x4 *)(p.ws + OFF_RES + (size_t)blockIdx.x * 20480) + tid;
  if (layer == 0) {
    const uint16_t *row = p.x + (size_t)tok_row(16 * (TB + w) + t) * DM + 80 * ct + 4 * g;
#pragma unroll
    for (int i = 0; i < 5; ++i) {
      const v2u u = *(const v2u *)(row + 16 * i);
      res[i] = f32x4{bflo(u.x), bfhi(u.x), bflo(u.y), bfhi(u.y)};
    }
  } else {
#pragma unroll
    for (int i = 0; i < 5; ++i) res[i] = rp[i * 256];
  }
  f32x4 acc[5];
#pragma unroll
  for (int i = 0; i < 4; ++i) {
#pragma unroll
    for (int n = 0; n < 5; ++n) acc[n] = f32x4{0, 0, 0, 0};
    if (i == 3) wg_sync();
#pragma unroll
    for (int s = 0; s < 5; ++s)
#pragma unroll
      for (int n = 0; n < 5; ++n) acc[n] = mfma32(wf[s][n], xa[i][s], acc[n]);
    if (i < 3) {
      LAS f32x4 *sl = (LAS f32x4 *)(L + ((((w + 1 + i) & 3) * 3 + i) * 5) * 1024);
#pragma unroll
      for (int n = 0; n < 5; ++n) sl[n * 64 + lane] = acc[n];
    }
  }
#pragma unroll
  for (int i = 0; i < 3; ++i) {
    LAS f32x4 *sl = (LAS f32x4 *)(L + ((w * 3 + i) * 5) * 1024);
#pragma unroll
    for (int n = 0; n < 5; ++n) acc[n] += sl[n * 64 + lane];
  }
#pragma unroll
  for (int n = 0; n < 5; ++n) { res[n] += acc[n]; rp[n * 256] = res[n]; }
  resid_publish(p, 2 * layer + 1, TB + w, ct, lane, res, false);
}

__global__ NOPK void __launch_bounds__(256, 1) k_p5(Params p, int layer) {
  extern __shared__ __attribute__((aligned(16))) unsigned char smem[];
  LAS unsigned char *L = (LAS unsigned char *)smem;
  const int tid = threadIdx.x, lane = tid & 63, w = __builtin_amdgcn_readfirstlane(tid >> 6);
  const int g = lane >> 4;
  int pn, c;
  wg_map(blockIdx.x, pn, c);
  const int ct = c >> 1, hf = c & 1;
  const int TB = 8 * pn + 4 * hf;
  const unsigned char *wsrc = p.ws + OFF_W2 + (size_t)(ct * 4 + w) * W2_WV;
  v4u wf[13][5];
  v2u wh[5];
#pragma unroll
  for (int s = 0; s < 13; ++s)
#pragma unroll
    for (int n = 0; n < 5; ++n) wf[s][n] = *(const v4u *)(wsrc + (s * 5 + n) * 1024 + lane * 16);
#pragma unroll
  for (int n = 0; n < 5; ++n) wh[n] = *(const v2u *)(wsrc + 66560 + n * 512 + lane * 8);
  const unsigned char *hbase = p.ws + OFF_H + (size_t)layer * HBUF;
  f32x4 res[5];
  f32x4 *rp = (f32x4 *)(p.ws + OFF_RES + (size_t)blockIdx.x * 20480) + tid;
#pragma unroll
  for (int i = 0; i < 5; ++i) {
    const v2u u = *(const v2u *)(p.b2 + 80 * ct + 16 * i + 4 * g);
    res[i] = rp[i * 256] + f32x4{bflo(u.x), bfhi(u.x), bflo(u.y), bfhi(u.y)};
  }
  f32x4 acc[5];
#pragma unroll
  for (int i = 0; i < 4; ++i) {
    const int tt = TB + ((w + 1 + i) & 3);
    const unsigned char *hb = hbase + ((size_t)(tt >> 1) * 4 + w) * 27648 + lane * 16;
    const int x = tt & 1;
    v4u xa[12];
#pragma unroll
    for (int s = 0; s < 12; ++s) xa[s] = *(const v4u *)(hb + (s * 2 + x) * 1024);
    v2u lo[3];
#pragma unroll
    for (int s = 0; s < 3; ++s) lo[s] = *(const v2u *)(hb + (24 + s) * 1024 + x * 8);
#pragma unroll
    for (int n = 0; n < 5; ++n) acc[n] = f32x4{0, 0, 0, 0};
    if (i == 3) wg_sync();
#pragma unroll
    for (int s = 0; s < 12; ++s)
#pragma unroll
      for (int n = 0; n < 5; ++n) acc[n] = mfma32(wf[s][n], xa[s], acc[n]);
#pragma unroll
    for (int n = 0; n < 5; ++n) acc[n] = mfma32(wf[12][n], v4u{lo[0].x, lo[0].y, lo[1].x, lo[1].y}, acc[n]);
#pragma unroll
    for (int n = 0; n < 5; ++n) acc[n] = mfma32(v4u{wh[n].x, wh[n].y, 0u, 0u}, v4u{lo[2].x, lo[2].y, lo[2].x, lo[2].y}, acc[n]);
    if (i < 3) {
      LAS f32x4 *sl = (LAS f32x4 *)(L + ((((w + 1 + i) & 3) * 3 + i) * 5) * 1024);
#pragma unroll
      for (int n = 0; n < 5; ++n) sl[n * 64 + lane] = acc[n];
    }
  }
#pragma unroll
  for (int i = 0; i < 3; ++i) {
    LAS f32x4 *sl = (LAS f32x4 *)(L + ((w * 3 + i) * 5) * 1024);
#pragma unroll
    for (int n = 0; n < 5; ++n) acc[n] += sl[n * 64 + lane];
  }
#pragma unroll
  for (int n = 0; n < 5; ++n) { res[n] += acc[n]; rp[n * 256] = res[n]; }
  resid_publish(p, 2 * layer + 2, TB + w, ct, lane, res, layer == 1);
}

extern "C" void kernel_launch(void *const *d_in, const int *in_sizes, int n_in, void *d_out, int out_size, void *d_ws,
                              size_t ws_size, hipStream_t stream) {
  Params p;
  p.x = (const uint16_t *)d_in[0];
  p.wqkv = (const uint16_t *)d_in[1];
  p.wo = (const uint16_t *)d_in[2];
  p.w1 = (const uint16_t *)d_in[4];
  p.b1 = (const uint16_t *)d_in[5];
  p.w2 = (const uint16_t *)d_in[6];
  p.b2 = (const uint16_t *)d_in[7];
  p.out = (uint16_t *)d_out;
  p.ws = (unsigned char *)d_ws;
  (void)hipFuncSetAttribute((const void *)k_p1, hipFuncAttributeMaxDynamicSharedMemorySize, 163840);
  (void)hipFuncSetAttribute((const void *)k_p4, hipFuncAttributeMaxDynamicSharedMemorySize, 163840);
  k_prep<<<(G9 + 255) / 256, 256, 0, stream>>>(p);
  for (int layer = 0; layer < 2; ++layer) {
    k_p1<<<256, 256, 153600, stream>>>(p, layer);
    k_p3<<<256, 256, 61440, stream>>>(p, layer);
    k_p4<<<256, 256, 143360, stream>>>(p, layer);
    k_p5<<<256, 256, 61440, stream>>>(p, layer);
  }
}
```

```cpp
#include <hip/hip_runtime.h>
#include <stdint.h>

typedef unsigned v4u __attribute__((ext_vector_type(4)));
typedef unsigned v2u __attribute__((ext_vector_type(2)));
typedef float f32x4 __attribute__((ext_vector_type(4)));
typedef float f32x2 __attribute__((ext_vector_type(2)));
typedef float f32x16 __attribute__((ext_vector_type(16)));
typedef __bf16 bf16x8 __attribute__((ext_vector_type(8)));
typedef __bf16 bf16x2 __attribute__((ext_vector_type(2)));
typedef short s16x4 __attribute__((ext_vector_type(4)));
typedef short s16x2 __attribute__((ext_vector_type(2)));
typedef __amdgpu_buffer_rsrc_t rsrc_t;
#if defined(__HIP_DEVICE_COMPILE__)
#define NOPK __attribute__((target("no-packed-fp32-ops")))
#else
#define NOPK
#endif
#define LAS __attribute__((address_space(3)))
#define AI __attribute__((always_inline))
#define DI __device__ __forceinline__
#define UNLIKELY(x) __builtin_expect(!!(x), 0)
#define WLK 28
#define SPLITX 1
template <int V> struct IC { static constexpr int value = V; };
template <int N, int I = 0, class F> DI void static_for(F &f) {
  if constexpr (I < N) { f(IC<I>{}); static_for<N, I + 1>(f); }
}

constexpr unsigned XT = 20480, HT = 55296, GRB = 8u << 20;
constexpr unsigned OFF_FL = 0;
constexpr unsigned OFF_KVF = OFF_FL + 64 * 2048;
constexpr unsigned OFF_REC = OFF_KVF + 2 * 2048;
constexpr unsigned OFF_KV = OFF_REC + 64 * 2048;
constexpr unsigned OFF_XF = OFF_KV + 2 * 16 * 4096;
constexpr unsigned OFF_ACT = OFF_XF + 64 * XT;
constexpr unsigned OFF_E = GRB - 8192, OFF_ACK = GRB - 4096;
static_assert(OFF_ACT + 64 * HT + 2048 <= OFF_E, "ws map");
constexpr unsigned WS_END = 8 * GRB;
constexpr unsigned OOB = 0x40000000u;
constexpr unsigned L_QKV = 122880, QP = 272, QSL = 32 * QP, L_HALO = L_QKV + 3 * QSL, L_HST = L_HALO + 16 * QP;
constexpr unsigned L_STG = 155648;
constexpr unsigned L_PART = 102400, PSZ = 5440, L_B2 = L_PART + 8 * PSZ;
constexpr unsigned L_CTL = 159744, L_TS = 160256, L_DUMMY = 162304, L_TOTAL = 163840;
constexpr unsigned C_TAG = L_CTL;
constexpr unsigned C_R = L_CTL + 16, C_MD = L_CTL + 64, C_XF = L_CTL + 96, C_ZERO = L_CTL + 128, C_WL = L_CTL + 176;
constexpr unsigned C_PF = L_CTL + 64;
static_assert(L_HST + 3 * 512 <= L_STG, "lds map");
constexpr int SPIN_CAP = 1 << 18;

struct Params {
  const void *x, *wqkv, *wo, *w1, *b1, *w2, *b2;
  void *out, *ws;
};

DI void cfence() { asm volatile("" ::: "memory"); }
DI void bar() { asm volatile("s_waitcnt lgkmcnt(0)\n\ts_barrier" ::: "memory"); }
DI void drain() { asm volatile("s_waitcnt vmcnt(0)" ::: "memory"); }
template <int N> DI void vmw() { asm volatile("s_waitcnt vmcnt(%0)" ::"n"(N) : "memory"); }
template <int N> DI void vmw(v4u &d) { asm volatile("s_waitcnt vmcnt(%1)" : "+v"(d) : "n"(N) : "memory"); }
template <int N> DI void vwz(f32x4 (&a)[5], const v4u *w, v4u h) {
  asm volatile("s_waitcnt vmcnt(%11)\n\t"
               "v_mfma_f32_16x16x32_bf16 %0, %5, %10, 0\n\tv_mfma_f32_16x16x32_bf16 %1, %6, %10, 0\n\t"
               "v_mfma_f32_16x16x32_bf16 %2, %7, %10, 0\n\tv_mfma_f32_16x16x32_bf16 %3, %8, %10, 0\n\t"
               "v_mfma_f32_16x16x32_bf16 %4, %9, %10, 0"
               : "=&v"(a[0]), "=&v"(a[1]), "=&v"(a[2]), "=&v"(a[3]), "=&v"(a[4])
               : "v"(w[0]), "v"(w[1]), "v"(w[2]), "v"(w[3]), "v"(w[4]), "v"(h), "n"(N) : "memory");
}
template <int N> DI void vwa(f32x4 (&a)[5], const v4u *w, v4u h) {
  asm volatile("s_waitcnt vmcnt(%11)\n\t"
               "v_mfma_f32_16x16x32_bf16 %0, %5, %10, %0\n\tv_mfma_f32_16x16x32_bf16 %1, %6, %10, %1\n\t"
               "v_mfma_f32_16x16x32_bf16 %2, %7, %10, %2\n\tv_mfma_f32_16x16x32_bf16 %3, %8, %10, %3\n\t"
               "v_mfma_f32_16x16x32_bf16 %4, %9, %10, %4"
               : "+v"(a[0]), "+v"(a[1]), "+v"(a[2]), "+v"(a[3]), "+v"(a[4])
               : "v"(w[0]), "v"(w[1]), "v"(w[2]), "v"(w[3]), "v"(w[4]), "v"(h), "n"(N) : "memory");
}
DI void mfin(f32x4 (&a)[5]) {
  asm volatile("s_nop 7\n\ts_nop 3" : "+v"(a[0]), "+v"(a[1]), "+v"(a[2]), "+v"(a[3]), "+v"(a[4])::"memory");
}
DI void awds5(unsigned p, f32x4 (&a)[5]) {
  asm volatile("ds_write_b128 %0, %1\n\tds_write_b128 %0, %2 offset:1088\n\tds_write_b128 %0, %3 offset:2176\n\t"
               "ds_write_b128 %0, %4 offset:3264\n\tds_write_b128 %0, %5 offset:4352"
               ::"v"(p), "v"(a[0]), "v"(a[1]), "v"(a[2]), "v"(a[3]), "v"(a[4]) : "memory");
}
template <int N> DI uint64_t vwne(v2u s, v2u t) {
  uint64_t m;
  asm volatile("s_waitcnt vmcnt(%3)\n\tv_cmp_ne_u64 %0, %2, %1" : "=s"(m) : "v"(s), "s"(t), "n"(N) : "memory");
  return m;
}
DI void sbar() { __builtin_amdgcn_sched_barrier(0); }
DI float u2f(unsigned u) { return __builtin_bit_cast(float, u); }
DI unsigned f2u(float f) { return __builtin_bit_cast(unsigned, f); }
DI float bflo(unsigned u) { return u2f(u << 16); }
DI float bfhi(unsigned u) { return u2f(u & 0xffff0000u); }
DI unsigned rfl(unsigned v) { return __builtin_amdgcn_readfirstlane(v); }
DI unsigned pk2(float a, float b) {
  f32x2 v = {a, b};
  return __builtin_bit_cast(unsigned, __builtin_convertvector(v, bf16x2));
}
DI v2u pk4(f32x4 v) { return v2u{pk2(v[0], v[1]), pk2(v[2], v[3])}; }
DI f32x4 mfma16(v4u a, v4u b, f32x4 c) {
  return __builtin_amdgcn_mfma_f32_16x16x32_bf16(__builtin_bit_cast(bf16x8, a), __builtin_bit_cast(bf16x8, b), c, 0, 0, 0);
}
DI f32x16 mfma32(v4u a, v4u b, f32x16 c) {
  return __builtin_amdgcn_mfma_f32_32x32x16_bf16(__builtin_bit_cast(bf16x8, a), __builtin_bit_cast(bf16x8, b), c, 0, 0, 0);
}
DI float dot1(unsigned a, float c) {
  return __builtin_amdgcn_fdot2_f32_bf16(__builtin_bit_cast(bf16x2, a), __builtin_bit_cast(bf16x2, 0x3f803f80u), c, false);
}
DI float fmx(float a, float b) { return __builtin_elementwise_maximum(a, b); }
DI float half_sum(float x) {
  auto t = __builtin_amdgcn_permlane32_swap(f2u(x), f2u(x), false, false);
  return u2f(t[0]) + u2f(t[1]);
}
template <int CTRL> DI float dpp(float x) {
  return u2f((unsigned)__builtin_amdgcn_update_dpp(0, (int)f2u(x), CTRL, 0xf, 0xf, false));
}
DI float sum8(float x) {
  x += dpp<0xB1>(x); x += dpp<0x4E>(x); x += dpp<0x141>(x);
  return x;
}
DI float sum16(float x) { x = sum8(x); return x + dpp<0x128>(x); }
DI float sum32(float x) {
  x = sum8(x);
  x += dpp<0x128>(x);
  auto s = __builtin_amdgcn_permlane16_swap(f2u(x), f2u(x), false, false);
  return u2f(s[0]) + u2f(s[1]);
}
DI v4u ld128(rsrc_t r, unsigned vo, unsigned so) { return __builtin_amdgcn_raw_buffer_load_b128(r, vo, so, 0); }
DI v4u ld128c(rsrc_t r, unsigned vo, unsigned so) { return __builtin_amdgcn_raw_buffer_load_b128(r, vo, so, 16); }
DI v2u ld64c(rsrc_t r, unsigned vo, unsigned so) { return __builtin_amdgcn_raw_buffer_load_b64(r, vo, so, 16); }
DI void st128c(v4u v, rsrc_t r, unsigned vo, unsigned so) {
  asm volatile("" : "+v"(v));
  __builtin_amdgcn_raw_buffer_store_b128(v, r, vo, so, 16);
  asm volatile("s_nop 1" ::"v"(v) : "memory");
}
DI void st64c(v2u v, rsrc_t r, unsigned vo, unsigned so) { __builtin_amdgcn_raw_buffer_store_b64(v, r, vo, so, 16); }
template <int OFF> DI void ald128c(v4u &d, rsrc_t r, unsigned vo, unsigned so) {
  asm volatile("buffer_load_dwordx4 %0, %1, %2, %3 offen offset:%4 sc1" : "+v"(d) : "v"(vo), "s"(r), "s"(so), "n"(OFF) : "memory");
}
DI void ald128(v4u &d, rsrc_t r, unsigned vo, unsigned so) {
  asm volatile("buffer_load_dwordx4 %0, %1, %2, %3 offen" : "=&v"(d) : "v"(vo), "s"(r), "s"(so) : "memory");
}
DI void ald4x128c(v4u (&d)[4], rsrc_t r, unsigned vo, unsigned so) {
  asm volatile("s_nop 4\n\t"
               "buffer_load_dwordx4 %0, %4, %5, %6 offen sc1\n\t"
               "buffer_load_dwordx4 %1, %4, %5, %6 offen offset:1024 sc1\n\t"
               "buffer_load_dwordx4 %2, %4, %5, %6 offen offset:2048 sc1\n\t"
               "buffer_load_dwordx4 %3, %4, %5, %6 offen offset:3072 sc1"
               : "+v"(d[0]), "+v"(d[1]), "+v"(d[2]), "+v"(d[3]) : "v"(vo), "s"(r), "s"(so) : "memory");
}
DI void ald64c(v2u &d, rsrc_t r, unsigned vo, unsigned so) {
  asm volatile("s_nop 4\n\tbuffer_load_dwordx2 %0, %1, %2, %3 offen sc1" : "+v"(d) : "v"(vo), "s"(r), "s"(so) : "memory");
}
DI void ast64c(v2u d, rsrc_t r, unsigned vo, unsigned so) {
  asm volatile("s_nop 4\n\tbuffer_store_dwordx2 %0, %1, %2, %3 offen sc1" ::"v"(d), "v"(vo), "s"(r), "s"(so) : "memory");
}
DI void ast128c(v4u d, rsrc_t r, unsigned vo, unsigned so) {
  asm volatile("s_nop 4\n\tbuffer_store_dwordx4 %0, %1, %2, %3 offen sc1\n\ts_nop 1" ::"v"(d), "v"(vo), "s"(r), "s"(so) : "memory");
}
DI void snop4() { asm volatile("s_nop 4" ::: "memory"); }
DI void glds(rsrc_t r, unsigned vo, unsigned so, unsigned dst) {
  unsigned keep;
  asm volatile("s_mov_b32 %0, m0\n\ts_mov_b32 m0, %1\n\ts_nop 2\n\tbuffer_load_dwordx4 %2, %3, %4 offen lds\n\ts_mov_b32 m0, %0"
               : "=&s"(keep) : "s"(dst), "v"(vo), "s"(r), "s"(so) : "memory");
}
DI void glds4c(rsrc_t r, unsigned vo, unsigned so, unsigned dst) {
  unsigned keep;
  asm volatile("s_mov_b32 %0, m0\n\ts_mov_b32 m0, %1\n\ts_nop 2\n\t"
               "buffer_load_dwordx4 %2, %3, %4 offen sc1 lds\n\t"
               "buffer_load_dwordx4 %2, %3, %4 offen offset:1024 sc1 lds\n\t"
               "buffer_load_dwordx4 %2, %3, %4 offen offset:2048 sc1 lds\n\t"
               "buffer_load_dwordx4 %2, %3, %4 offen offset:3072 sc1 lds\n\t"
               "s_mov_b32 m0, %0"
               : "=&s"(keep) : "s"(dst), "v"(vo), "s"(r), "s"(so) : "memory");
}
DI void glds5(rsrc_t r, const unsigned (&vo)[5], unsigned so, unsigned dst) {
  unsigned keep;
  asm volatile("s_mov_b32 %0, m0\n\ts_mov_b32 m0, %1\n\ts_nop 2\n\t"
               "buffer_load_dwordx4 %2, %7, %8 offen lds\n\t"
               "buffer_load_dwordx4 %3, %7, %8 offen offset:1024 lds\n\t"
               "buffer_load_dwordx4 %4, %7, %8 offen offset:2048 lds\n\t"
               "buffer_load_dwordx4 %5, %7, %8 offen offset:3072 lds\n\t"
               "s_add_u32 m0, m0, 0x1000\n\ts_nop 1\n\t"
               "buffer_load_dwordx4 %6, %7, %8 offen lds\n\t"
               "s_mov_b32 m0, %0"
               : "=&s"(keep) : "s"(dst), "v"(vo[0]), "v"(vo[1]), "v"(vo[2]), "v"(vo[3]), "v"(vo[4]), "s"(r), "s"(so) : "memory", "scc");
}
DI v4u lds128(unsigned a) { return *(LAS v4u *)(uintptr_t)a; }
DI f32x4 ldsf4(unsigned a) { return *(LAS f32x4 *)(uintptr_t)a; }
DI void lds_st128(unsigned a, v4u v) { *(LAS v4u *)(uintptr_t)a = v; }
DI void lds_stf4(unsigned a, f32x4 v) { *(LAS f32x4 *)(uintptr_t)a = v; }
DI void lds_st32(unsigned a, unsigned v) { *(LAS unsigned *)(uintptr_t)a = v; }
DI v2u ldstr(unsigned a) {
  return __builtin_bit_cast(v2u, __builtin_amdgcn_ds_read_tr16_b64_v4i16((LAS s16x4 *)(uintptr_t)a));
}
template <int OFF> DI void ards128(v4u &d, unsigned a) {
  asm volatile("ds_read_b128 %0, %1 offset:%2" : "+v"(d) : "v"(a), "n"(OFF) : "memory");
}
DI void ards64(v2u &d, unsigned a) { asm volatile("ds_read_b64 %0, %1" : "+v"(d) : "v"(a) : "memory"); }
template <int OFF> DI void awds128(unsigned a, v4u d) {
  asm volatile("ds_write_b128 %0, %1 offset:%2" ::"v"(a), "v"(d), "n"(OFF) : "memory");
}
template <int OFF> DI void awds32(unsigned a, unsigned d) {
  asm volatile("ds_write_b32 %0, %1 offset:%2" ::"v"(a), "v"(d), "n"(OFF) : "memory");
}
template <int N> DI void lgw(v4u &d) { asm volatile("s_waitcnt lgkmcnt(%1)" : "+v"(d) : "n"(N) : "memory"); }
DI unsigned sel(bool c, unsigned a, unsigned b) { return c ? a : b; }
DI unsigned umin(unsigned a, unsigned b) { return a < b ? a : b; }
DI void signal(unsigned a, unsigned v) {
  cfence();
  *(volatile LAS unsigned *)(uintptr_t)a = v;
  cfence();
}
DI void signal2(unsigned a, unsigned v) {
  cfence();
  *(volatile LAS v2u *)(uintptr_t)a = v2u{v, v};
  cfence();
}
DI unsigned peek4(unsigned a) {
  const v4u v = *(volatile LAS v4u *)(uintptr_t)a;
  return rfl(umin(umin(v.x, v.y), umin(v.z, v.w)));
}
DI void wait4(unsigned a, unsigned target, int &budget) {
  cfence();
  if (UNLIKELY(peek4(a) < target)) {
    do { --budget; __builtin_amdgcn_s_sleep(1); } while (peek4(a) < target && budget > 0);
  }
  cfence();
}
DI void wait2(unsigned a, unsigned target, int &budget) {
  cfence();
  for (;;) {
    const v2u v = *(volatile LAS v2u *)(uintptr_t)a;
    if (rfl(umin(v.x, v.y)) >= target || budget <= 0) break;
    --budget;
    __builtin_amdgcn_s_sleep(1);
  }
  cfence();
}
DI void wait1(unsigned a, unsigned target, int &budget) {
  cfence();
  if (UNLIKELY(rfl(*(volatile LAS unsigned *)(uintptr_t)a) < target)) {
    do { --budget; __builtin_amdgcn_s_sleep(1); } while (rfl(*(volatile LAS unsigned *)(uintptr_t)a) < target && budget > 0);
  }
  cfence();
}
DI v2u tg(v2u t) {
  unsigned a = t.x, b = t.y;
  asm volatile("" : "+s"(a), "+s"(b));
  return v2u{a, b};
}
template <int N> DI void wmfma0(f32x16 &d, v4u a, v4u b) {
  asm volatile("s_waitcnt lgkmcnt(%3)\n\tv_mfma_f32_32x32x16_bf16 %0, %1, %2, 0" : "=&v"(d) : "v"(a), "v"(b), "n"(N) : "memory");
}
template <int N> DI void wmfma(f32x16 &d, v4u a, v4u b) {
  asm volatile("s_waitcnt lgkmcnt(%3)\n\tv_mfma_f32_32x32x16_bf16 %0, %1, %2, %0" : "+v"(d) : "v"(a), "v"(b), "n"(N) : "memory");
}
template <int N> DI void wmfmac(f32x16 &d, v4u a, v4u b, f32x16 c) {
  asm volatile("s_waitcnt lgkmcnt(%4)\n\tv_mfma_f32_32x32x16_bf16 %0, %1, %2, %3" : "=&v"(d) : "v"(a), "v"(b), "v"(c), "n"(N) : "memory");
}
DI bool any_true(bool p) { return __builtin_amdgcn_ballot_w64(p) != 0; }
DI bool all_true(bool p) { return __builtin_amdgcn_ballot_w64(!p) == 0; }
DI void poll64(rsrc_t r, unsigned vo, unsigned so, v2u tag, int &budget) {
  for (;;) {
    cfence();
    const v2u v = ld64c(r, vo, so);
    if (all_true(v.x == tag.x && v.y == tag.y) || budget <= 0) break;
    budget -= 8;
    __builtin_amdgcn_s_sleep(2);
  }
  cfence();
}
#define TS(i)
template <int K> struct XO { static constexpr int v = (K / 5) * 2560 + (K % 5) * 32; };
constexpr int m_w1(int set) { return set == 0 ? 6 : 10; }
constexpr int m_w2(int set) { return set == 0 ? 10 : 18; }
constexpr int m_extra(int set, int s) {
  s = (s + 41) % 41;
  return (s == 14) + (s == 34) + (s == 36) + (s == m_w1(set)) + 2 * (s == m_w2(set));
}
constexpr int m_nout(int set, int s) {
  return 3 + m_extra(set, s - 4) + m_extra(set, s - 3) + m_extra(set, s - 2) + m_extra(set, s - 1);
}

__global__ NOPK void __launch_bounds__(512, 2) k_fwd(Params p) {
  const int tid = threadIdx.x;
  const int w = rfl(tid >> 6);
  const int bid = blockIdx.x;
  const bool isY = bid & 1;
  const int grp = 2 * ((bid & 7) >> 1) + (bid >> 7), rk = (bid >> 3) & 15;
  const rsrc_t rws = __builtin_amdgcn_make_buffer_rsrc(p.ws, 0, (int)WS_END, 0x00020000);
  const unsigned gb = grp * GRB;
  const unsigned row0 = (unsigned)(512 * (grp & 3) + (grp >> 2)) * 1280u;
  int budget = SPIN_CAP;
  TS(0);
  v2u e0 = {0u, 0u};
  if (w == 7) e0 = ld64c(rws, 0, OFF_E);
  if (tid < 64) *(volatile LAS unsigned *)(uintptr_t)(L_CTL + tid * 4) = (tid >= 4 && tid < 16 && (tid & 2)) ? 7u : 0u;
  bar();
  if (w == 7) {
    const unsigned lo = rfl(e0.x) + 1u;
    const unsigned hi = rfl(e0.y) + (lo == 0u ? 1u : 0u);
    *(volatile LAS v4u *)(uintptr_t)C_TAG = v4u{lo, hi, 1u, 0u};
    if ((tid & 63) == 0) st64c(v2u{lo, hi}, rws, bid * 8, OFF_ACK);
  }
  auto get_tag = [&]() AI {
    wait1(C_TAG + 8, 1u, budget);
    const v2u v = *(volatile LAS v2u *)(uintptr_t)C_TAG;
    return v2u{rfl(v.x), rfl(v.y)};
  };

  if (!isY) {
    const int c = rk, set = w >> 2, wq = w & 3;
    const int n1 = 4 * c + wq;
    const bool live = set == 0 || n1 < 54;
    const int step = c >= 14 ? 2 : 1;
    v2u tag = {0u, 0u};
    auto load_units = [&](int og0, int og1, int v, int base, int lane) AI {
      v4u pr[4];
      asm volatile("" : "=v"(pr[0]), "=v"(pr[1]), "=v"(pr[2]), "=v"(pr[3]));
      auto rso = [&](int og) AI { return gb + OFF_REC + (v * 16 + 2 * (7 - (og & 7))) * 2048; };
      auto okP = [&]() AI {
        asm volatile("s_waitcnt vmcnt(0)" : "+v"(pr[0]), "+v"(pr[1]), "+v"(pr[2]), "+v"(pr[3])::"memory");
        const unsigned a = (pr[0].y ^ tag.x) | (pr[1].y ^ tag.x) | (pr[2].y ^ tag.x) | (pr[3].y ^ tag.x);
        const unsigned b = (pr[0].w ^ tag.y) | (pr[1].w ^ tag.y) | (pr[2].w ^ tag.y) | (pr[3].w ^ tag.y);
        return all_true((a | b) == 0u) || budget <= 0;
      };
      ald4x128c(pr, rws, lane * 16, rso(og0));
      for (int og = og0; og < og1; og += 2) {
        const int u = 7 - (og & 7);
        const unsigned slot = og % 3, xr = C_R + 16 * slot, dst = slot * 40960;
        const unsigned xfa = C_XF + ((og - 3 >= base ? v : v - step) & 1) * 16;
        if (!v) {
          if (og >= 3) wait4(xfa, og - 2, budget);
          const rsrc_t rx = __builtin_amdgcn_make_buffer_rsrc((char *)p.x - 4096, 0, 2048 * 1280 + 4096, 0x00020000);
          unsigned vx[5];
#pragma unroll
          for (int m = 0; m < 5; ++m) {
            const unsigned idx = m * 64 + lane, h = idx >= 160, r = idx - 160 * h, tk = (r * 205) >> 11, pos = r - 10 * tk;
            vx[m] = tk * 2560 + h * 160 + ((pos ^ (tk >> 3)) * 16) + 4096 - (m & 3) * 1024;
          }
#pragma unroll
          for (int k = 0; k < 8; ++k) {
            const int T = (k >> 1) & 1, a = (k >> 2) * 2 + (k & 1);
            glds5(rx, vx, row0 + (2 * u + T) * 40960 + a * 320, dst + T * XT + a * 5120);
          }
        }
        for (;;) {
          const bool ok = okP();
          signal(v ? L_DUMMY : xr, 2 * og + 1);
          if (ok) break;
          budget -= 8;
          __builtin_amdgcn_s_sleep(1);
          ald4x128c(pr, rws, lane * 16, rso(og));
        }
        TS(34 + 2 * (8 * v + 7 - u));
        if (v) {
          wait4(xfa, og - 2, budget);
          const unsigned sx = gb + OFF_XF + (v * 16 + 2 * u) * XT;
#pragma unroll
          for (int k = 0; k < 10; ++k) {
            const unsigned o = k < 6 ? (k / 3) * XT + (k % 3) * 4096 : (k & 1) * XT + (k / 2) * 4096;
            glds4c(rws, lane * 16, sx + o, dst + o);
          }
        }
        float s[4], q[4];
#pragma unroll
        for (int k = 0; k < 4; ++k) {
          const float sk = u2f(pr[k].x);
          s[k] = sum8(sk);
          const float d = sk * (1.f / 80.f) - s[k] * (1.f / 640.f);
          q[k] = sum8(u2f(pr[k].z) + 80.f * d * d);
        }
        const int sel = lane & 3;
        const float S = sel == 0 ? s[0] : sel == 1 ? s[1] : sel == 2 ? s[2] : s[3];
        const float Q = sel == 0 ? q[0] : sel == 1 ? q[1] : sel == 2 ? q[2] : q[3];
        const float nm = S * (-1.f / 640.f), var = Q * (1.f / 640.f) + 1e-5f;
        const float rstd = __builtin_amdgcn_rsqf(var), sd = var * rstd;
        const unsigned mh = pk2(nm, sd);
        lds_st128(L_HST + slot * 512 + (8 * sel + (lane >> 3)) * 16,
                  v4u{pk2(nm, nm - bflo(mh)), mh, pk2(sd - bfhi(mh), 0.f), f2u(rstd) & ~0x80u});
        ald4x128c(pr, rws, lane * 16, og + 2 < og1 ? rso(og + 2) : OOB);
        vmw<15>();
        signal(xr, 2 * og + 1);
        vmw<4>();
        signal(xr, 2 * og + 2);
        TS(35 + 2 * (8 * v + 7 - u));
      }
      asm volatile("s_waitcnt vmcnt(0)" : "+v"(pr[0]), "+v"(pr[1]), "+v"(pr[2]), "+v"(pr[3])::"memory");
    };
    if (set == 1) {
      tag = get_tag();
      if (wq >= 2) load_units(wq - 2, 3, 0, 0, tid & 63);
      wait1(C_WL + wq * 4, 1u, budget);
    }
    v4u wr[40], csf;
    unsigned braw;
    {
      const int lane = tid & 63;
      const rsrc_t rw = set == 0 ? __builtin_amdgcn_make_buffer_rsrc((void *)p.wqkv, 0, 640 * 1920 * 2, 0x00020000)
                                 : __builtin_amdgcn_make_buffer_rsrc((void *)p.w1, 0, live ? 640 * 1728 * 2 : 0, 0x00020000);
      const rsrc_t rb = __builtin_amdgcn_make_buffer_rsrc((void *)p.b1, 0, 1728 * 2, 0x00020000);
      const unsigned pitch = set == 0 ? 3840u : 3456u;
      unsigned col;
      if (set == 0) {
        const int cc = 4 * wq + (lane & 3);
        col = cc == 15 ? OOB / 2 : 640 * (cc / 5) + 40 * c + 8 * (cc % 5);
      } else {
        col = 32 * n1 + 8 * (lane & 3);
      }
      const unsigned vo = (lane >> 2) * pitch + col * 2;
      const int i = lane & 31, q4 = (i >> 2) & 3;
      const unsigned bc = 32 * n1 + (i & 16) + 8 * (q4 & 1) + 4 * (q4 >> 1) + (i & 3);
      asm volatile("s_nop 4\n\tbuffer_load_ushort %0, %1, %2, 0 offen" : "=&v"(braw) : "v"(bc * 2), "s"(rb) : "memory");
#pragma unroll
      for (int k = 0; k < 40; ++k) ald128(wr[k], rw, vo, k * 16 * pitch);
      asm volatile("" : "=v"(csf));
    }
    if (set == 0) tag = get_tag();

    for (int ph = 0, base = 0; ph < 4; ph += step, base += 8) {
      const int layer = ph >> 1;
      const bool isM = set == (ph & 1);
      int lane = tid & 63;
      asm volatile("" : "+v"(lane));
      if (isM && ph < 2) {
        const unsigned stg = (set == 0 ? L_HALO : L_STG) + wq * 1024;
        const int G = lane >> 4, r4 = (lane >> 2) & 3, c4 = lane & 3;
        const unsigned tra = stg + (8 * (G >> 1) + r4) * 64 + 32 * (G & 1) + 16 * (c4 & 1) + 8 * (c4 >> 1);
        float cs[4] = {0.f, 0.f, 0.f, 0.f};
        auto tr = [&](auto kc) AI {
          constexpr int k = decltype(kc)::value;
          if constexpr (k < 40) {
            vmw<39 - k>(wr[k]);
            lds_st128(stg + lane * 16, wr[k]);
            cfence();
            const v2u lo = ldstr(tra), hi = ldstr(tra + 256);
            cfence();
            wr[k] = v4u{lo.x, lo.y, hi.x, hi.y};
            if constexpr (k == WLK) signal(C_WL + wq * 4, 1u);
          }
          if constexpr (k >= 2) {
            const v4u z = wr[k - 2];
            cs[0] = dot1(z.x, cs[0]); cs[1] = dot1(z.y, cs[1]); cs[2] = dot1(z.z, cs[2]); cs[3] = dot1(z.w, cs[3]);
          }
          sbar();
        };
        static_for<42>(tr);
        asm volatile("" : "+v"(braw));
        float bias = bflo(braw);
        if (set == 0) bias = (wq == 3 && (lane & 20) == 20) ? 1.f : 0.f;
        const float c0 = half_sum((cs[0] + cs[1]) + (cs[2] + cs[3]));
        const unsigned h2 = pk2(c0, c0);
        const float cl = c0 - bflo(h2);
        csf = v4u{h2, pk2(cl, bias), pk2(bias, 0.f), 0u};
        if (lane >= 32) csf = v4u{0u, 0u, 0u, 0u};
        TS(1);
      }
      if (wq >= 2) {
        const int og0 = isM ? base : ph == 0 ? 6 - wq : base + wq - 1;
        const int og1 = isM ? (wq == 2 && ph != 0 ? base + 1 : base) : base + 8;
        if (og0 < og1) load_units(og0, og1, ph, base, lane);
      }
      if (isM) {
        auto mloop = [&](auto setc) AI {
          constexpr int SET = decltype(setc)::value;
          const unsigned xl = ((lane >> 4) & 1) * XT + (lane & 15) * 160 + (((lane >> 5) ^ (lane >> 3)) & 1) * 16;
          const unsigned ql = L_QKV + (lane & 31) * QP + 64 * wq + (lane >> 5) * 16;
          const unsigned hl = L_HST + (lane & 31) * 16, dl = L_DUMMY + lane * 16;
          v4u b[5], bx, rec;
          f32x16 out;
          asm volatile("" : "=v"(b[0]), "=v"(b[1]), "=v"(b[2]), "=v"(b[3]), "=v"(b[4]), "=v"(bx), "=v"(rec), "=v"(out));
          unsigned slot = base % 3;
          wait1(C_R + 16 * slot, 2 * base + 1, budget);
          unsigned xa = xl + slot * 40960, ra = C_R + 16 * slot, qa = dl, mda = dl, mdv = 0, ha = 0;
          ards128<XO<0>::v>(b[0], xa); ards128<XO<1>::v>(b[1], xa); ards128<XO<2>::v>(b[2], xa); ards128<XO<3>::v>(b[3], xa);
          __builtin_amdgcn_s_setprio(2);
          for (unsigned og = base;; ++og) {
            const unsigned nslot = slot == 2 ? 0 : slot + 1;
            TS(2 + 2 * (8 * layer + (og & 7)));
            f32x16 acc;
            unsigned o[8], u;
            float t0, t1;
            const bool last = (og & 7) == 7;
            unsigned lim;
            auto relu2 = [&](unsigned x) AI {
              return __builtin_bit_cast(unsigned, __builtin_elementwise_max(__builtin_bit_cast(s16x2, x), s16x2{0, 0}));
            };
            auto mm = [&](auto kc) AI {
              constexpr int s = decltype(kc)::value;
              if constexpr (s == 0) wmfma0<m_nout(SET, s)>(acc, wr[0], b[0]);
              else wmfma<m_nout(SET, s)>(acc, wr[s], b[s % 5]);
              if constexpr (s <= 35) ards128<XO<(s + 4) % 40>::v>(b[(s + 4) % 5], xa);
              else if constexpr (s == 36) ards128<0>(bx, ha);
              else ards128<XO<(s + 3) % 40>::v>(b[(s + 3) % 5], xa);
              if constexpr (s == 14 || s == 34) ards128<0>(rec, ra);
              if constexpr (s == 2) asm volatile("" : "+v"(out));
              sbar();
              if constexpr (SET == 0) {
                if constexpr (s == m_w1(SET)) awds128<0>(qa, v4u{o[0], o[1], o[2], o[3]});
                if constexpr (s >= 2 && s < 10) {
                  constexpr int j = s - 2;
                  o[j] = pk2(out[2 * j] * u2f(bx.w), out[2 * j + 1] * u2f(bx.w));
                  asm volatile("" : "+v"(o[j]));
                }
              } else if constexpr (s >= 2 && s <= 18) {
                constexpr int j = (s - 2) >> 1;
                if constexpr ((s & 1) == 0) {
                  if constexpr (j > 0) { o[j - 1] = relu2(u); sbar(); }
                  if constexpr (j < 8) {
                    t0 = out[2 * j] * u2f(bx.w); t1 = out[2 * j + 1] * u2f(bx.w);
                    asm volatile("" : "+v"(t0), "+v"(t1));
                  }
                  if constexpr (j > 0) asm volatile("" : "+v"(o[j - 1]));
                  if constexpr (s == m_w1(SET)) awds128<0>(qa, v4u{o[0], o[1], o[2], o[3]});
                } else {
                  u = pk2(t0 * __builtin_fabsf(t0), t1 * __builtin_fabsf(t1));
                  asm volatile("" : "+v"(u));
                }
              }
              if constexpr (s == m_w2(SET)) { awds128<32>(qa, v4u{o[4], o[5], o[6], o[7]}); awds32<0>(mda, mdv); }
              if constexpr (s == 19) {
                asm volatile("" : "+v"(rec));
                if (UNLIKELY(any_true(rec.x < 2u * og + 2u))) wait1(C_R + 16 * slot, 2 * og + 2, budget);
              }
              if constexpr (s == 21) {
                ha = hl + slot * 512; ra = C_R + 16 * nslot; mdv = og + 1;
                asm volatile("" : "+v"(ha), "+v"(ra), "+v"(mdv));
              }
              if constexpr (s == 22) {
                qa = ql + slot * QSL; mda = C_MD + 4 * w;
                asm volatile("" : "+v"(qa), "+v"(mda));
              }
              if constexpr (s == 23) {
                lim = last ? ~0u : 2u * og + 3u;
                asm volatile("" : "+s"(lim));
              }
              if constexpr (s == 36) {
                awds32<32>(mda, mdv);
                xa = xl + nslot * 40960;
                asm volatile("" : "+v"(xa));
              }
              sbar();
            };
            static_for<40>(mm);
            asm volatile("" : "+v"(rec));
            uint64_t slow = __builtin_amdgcn_ballot_w64(umin(rec.x, umin(rec.z, rec.w)) < lim);
            asm volatile("" : "+s"(slow));
            TS(3 + 2 * (8 * layer + (og & 7)));
            wmfmac<m_nout(SET, 40)>(out, csf, bx, acc);
            if (UNLIKELY(slow != 0)) {
              asm volatile("s_nop 7\n\ts_nop 7\n\ts_nop 3" : "+v"(out) : "v"(acc));
              wait2(C_R + 16 * nslot + 8, 2 * og + 3, budget);
              const float rs = u2f(bx.w);
#pragma unroll
              for (int j = 0; j < 8; ++j) {
                float a = out[2 * j] * rs, bb = out[2 * j + 1] * rs;
                if (SET == 1) { a *= __builtin_fabsf(a); bb *= __builtin_fabsf(bb); }
                o[j] = pk2(a, bb);
                if (SET == 1) o[j] = relu2(o[j]);
              }
              awds128<0>(qa, v4u{o[0], o[1], o[2], o[3]});
              awds128<32>(qa, v4u{o[4], o[5], o[6], o[7]});
              awds32<0>(mda, mdv);
              if (last) break;
              qa = dl; mda = dl;
              wait1(C_R + 16 * nslot, 2 * og + 3, budget);
              ards128<XO<0>::v>(b[0], xa); ards128<XO<1>::v>(b[1], xa); ards128<XO<2>::v>(b[2], xa);
            }
            ards128<XO<3>::v>(b[3], xa);
            slot = nslot;
          }
          __builtin_amdgcn_s_setprio(0);
          asm volatile("s_waitcnt lgkmcnt(0)" : "+v"(b[0]), "+v"(b[1]), "+v"(b[2])::"memory");
        };
        if (!live) {
          signal(C_MD + w * 4, base + 8);
          signal(C_XF + w * 4, base + 8);
        } else if (set == 0) {
          mloop(IC<0>{});
        } else {
          mloop(IC<1>{});
        }
        if (bid == 120 && w == 3 && ph == 0) {
          for (;;) {
            cfence();
            bool ok = true;
#pragma unroll
            for (int k = 0; k < 2; ++k) {
              const v4u a = ld128c(rws, lane * 16 + k * 1024, OFF_ACK);
              ok = ok && a.x == tag.x && a.y == tag.y && a.z == tag.x && a.w == tag.y;
            }
            if (all_true(ok)) { if (lane == 0) st64c(tg(tag), rws, 0, OFF_E); break; }
            if (budget <= 0) break;
            budget -= 8;
            __builtin_amdgcn_s_sleep(2);
          }
        }
      }
      if (wq >= 2) continue;
      asm volatile("" : "+v"(lane));
      if (ph & 1) {
        if (isM) continue;
        const unsigned ra = L_QKV + (lane & 15) * QP + (lane >> 4) * 16;
        const unsigned v0 = lane * 16, v1 = c == 13 ? OOB : v0;
        for (int i = wq; i < 8; i += 2) {
          const unsigned og = base + i, slot = og % 3, u = 7 - i;
          wait4(C_MD + 16, og + 1, budget);
          TS(66 + 2 * (8 * layer + i));
          v4u d[8];
#pragma unroll
          for (int k = 0; k < 8; ++k) d[k] = lds128(ra + slot * QSL + (k >> 2) * 16 * QP + (k & 3) * 64);
          signal2(C_R + 16 * ((og + 1) % 3) + 8, 2 * og + 9);
          const unsigned so = gb + OFF_ACT + (ph * 16 + 2 * u) * HT + c * 4096;
#pragma unroll
          for (int k = 0; k < 8; ++k) st128c(d[k], rws, ((k & 3) < 2 ? v0 : v1) + (k & 3) * 1024, so + (k >> 2) * HT);
          drain();
          if (lane < 2) st64c(tg(tag), rws, lane * 2048, gb + OFF_FL + (ph * 16 + 2 * u) * 2048 + c * 128);
          TS(67 + 2 * (8 * layer + i));
        }
      } else {
        const int g = lane >> 4, t = lane & 15, c4 = lane & 3;
        const bool hi = wq == 0;
        const bool has_halo = (grp & 3) != 0, give_halo = (grp & 3) != 3;
        const int m0 = set ? 0 : 7 - wq, m1 = set ? 7 - wq : 8;
        f32x4 cP, cQ;
#pragma unroll
        for (int r = 0; r < 4; ++r) {
          const int j = 4 * g + r;
          cP[r] = j >= t + 9 ? 0.f : -INFINITY;
          cQ[r] = (j <= t && j >= t - 7) ? 0.f : -INFINITY;
        }
        const unsigned mp0 = (4 * g > t ? 0xffffu : 0u) | (4 * g + 1 > t ? 0xffff0000u : 0u);
        const unsigned mp1 = (4 * g + 2 > t ? 0xffffu : 0u) | (4 * g + 3 > t ? 0xffff0000u : 0u);
        const unsigned mqa = g == 2 ? 0u : ~0u, mqb = g == 0 ? 0u : ~0u;
        const unsigned kl = t * QP + 16 * g;
        const unsigned vrow = (4 * g + ((lane >> 2) & 3)) * QP;
        const unsigned v0 = vrow + 160 + c4 * 8, v1a = vrow + (c4 ? 240 : 192), v1b = vrow + (c4 ? 240 : 232);
        const unsigned so1 = lane < 16 ? 4096 + (c & 3) * 256 + lane * 16 : OOB;
        bool pend = false;
        unsigned pfl = OOB;
        for (int m = m0; m < m1; ++m) {
          const unsigned og = base + m, u = 7 - m;
          const unsigned need = og + (hi || m == 7 ? 1 : 2);
          if (pend && (m == 7 || peek4(C_MD) < need)) { drain(); st64c(tg(tag), rws, pfl, 0); pend = false; }
          if (!hi && m == 7) {
            if (has_halo) {
              const unsigned id = layer * 16 + c;
              poll64(rws, 0, gb - GRB + OFF_KVF + id * 128, tag, budget);
              v4u d[4];
#pragma unroll
              for (int k = 0; k < 4; ++k) d[k] = ld128c(rws, lane * 16 + k * 1024, gb - GRB + OFF_KV + id * 4096);
#pragma unroll
              for (int k = 0; k < 4; ++k) lds_st128(L_HALO + (4 * k + g) * QP + t * 16, d[k]);
            } else {
              cP = f32x4{-INFINITY, -INFINITY, -INFINITY, -INFINITY};
            }
          }
          wait4(C_MD, need, budget);
          TS(34 + 2 * (8 * layer + m));
          unsigned rc, rp;
          if (hi) { rp = L_QKV + (og % 3) * QSL; rc = rp + 16 * QP; }
          else {
            rc = L_QKV + (og % 3) * QSL;
            rp = m < 7 ? L_QKV + ((og + 1) % 3) * QSL + 16 * QP : (has_halo ? (unsigned)L_HALO : rc);
          }
          const unsigned ac = rc + kl, ap = rp + kl, aq = g == 3 ? (unsigned)C_ZERO : ac;
          v4u qa = lds128(aq), kpa = lds128(ap + 80), kca = lds128(ac + 80);
          v4u qb = lds128(aq + 32), kpb = lds128(ap + 112), kcb = lds128(ac + 112);
          v2u vp[4], vc[4];
          vp[0] = ldstr(rp + v0); vc[0] = ldstr(rc + v0); vp[1] = ldstr(rp + v1a); vc[1] = ldstr(rc + v1a);
          vp[2] = ldstr(rp + v0 + 40); vc[2] = ldstr(rc + v0 + 40); vp[3] = ldstr(rp + v1b); vc[3] = ldstr(rc + v1b);
          qa.z &= mqa; qa.w &= mqa; qb.x &= mqb; qb.y &= mqb;
          const f32x4 spa = mfma16(kpa, qa, cP), sqa = mfma16(kca, qa, cQ);
          const f32x4 spb = mfma16(kpb, qb, cP), sqb = mfma16(kcb, qb, cQ);
          f32x4 sa, sb;
#pragma unroll
          for (int r = 0; r < 4; ++r) { sa[r] = fmx(spa[r], sqa[r]); sb[r] = fmx(spb[r], sqb[r]); }
          const float sc = 0.22360679774997896f * 1.4426950408889634f;
          float na, nb;
          {
            const float xa = fmx(fmx(sa[0], sa[1]), fmx(sa[2], sa[3])), xb = fmx(fmx(sb[0], sb[1]), fmx(sb[2], sb[3]));
            auto s1 = __builtin_amdgcn_permlane16_swap(f2u(xa), f2u(xb), false, false);
            const float y = fmx(u2f(s1[0]), u2f(s1[1]));
            auto s2 = __builtin_amdgcn_permlane32_swap(f2u(y), f2u(y), false, false);
            const float z = fmx(u2f(s2[0]), u2f(s2[1])) * -sc;
            auto s3 = __builtin_amdgcn_permlane16_swap(f2u(z), f2u(z), false, false);
            na = u2f(s3[0]); nb = u2f(s3[1]);
          }
#pragma unroll
          for (int r = 0; r < 4; ++r) {
            sa[r] = __builtin_amdgcn_exp2f(__builtin_fmaf(sa[r], sc, na));
            sb[r] = __builtin_amdgcn_exp2f(__builtin_fmaf(sb[r], sc, nb));
          }
          const v2u pa = pk4(sa), pb = pk4(sb);
          const unsigned a0 = pa.x & mp0, a1 = pa.y & mp1, b0 = pb.x & mp0, b1 = pb.y & mp1;
          const v4u opa = {a0, a1, pa.x ^ a0, pa.y ^ a1}, opb = {b0, b1, pb.x ^ b0, pb.y ^ b1};
          const f32x4 z4 = {0.f, 0.f, 0.f, 0.f};
          const f32x4 oa1 = mfma16(v4u{vp[1].x, vp[1].y, vc[1].x, vc[1].y}, opa, z4);
          const f32x4 ob1 = mfma16(v4u{vp[3].x, vp[3].y, vc[3].x, vc[3].y}, opb, z4);
          const f32x4 oa0 = mfma16(v4u{vp[0].x, vp[0].y, vc[0].x, vc[0].y}, opa, z4);
          const f32x4 ob0 = mfma16(v4u{vp[2].x, vp[2].y, vc[2].x, vc[2].y}, opb, z4);
          auto la = __builtin_amdgcn_permlane16_swap(f2u(oa1[0]), f2u(oa1[0]), false, false);
          auto lb = __builtin_amdgcn_permlane16_swap(f2u(ob1[0]), f2u(ob1[0]), false, false);
          const float ia = __builtin_amdgcn_rcpf(u2f(la[1])), ib = __builtin_amdgcn_rcpf(u2f(lb[1]));
          const v2u e0 = pk4(oa0 * ia), e1 = pk4(ob0 * ib), e2 = pk4(oa1 * ia), e3 = pk4(ob1 * ib);
          const unsigned T = ph * 16 + 2 * u + (hi ? 1 : 0);
          const unsigned so = gb + OFF_ACT + T * HT + 5 * (c >> 2) * 1024;
          st128c(v4u{e0.x, e0.y, e1.x, e1.y}, rws, lane * 16 + (c & 3) * 1024, so);
          st128c(v4u{e2.x, e2.y, e3.x, e3.y}, rws, so1, so);
          if (pend) { vmw<2>(); st64c(tg(tag), rws, pfl, 0); }
          pend = true;
          pfl = lane == 0 ? gb + OFF_FL + T * 2048 + c * 128 : OOB;
          if (hi && m == 0 && give_halo) {
            const unsigned id = layer * 16 + c;
#pragma unroll
            for (int k = 0; k < 4; ++k) st128c(lds128(rc + (4 * k + g) * QP + t * 16), rws, lane * 16 + k * 1024, gb + OFF_KV + id * 4096);
            if (lane == 1) pfl = gb + OFF_KVF + id * 128;
          }
          if (!hi && m == 6) wait1(C_R + 16 * (og % 3) + 12, 2 * og + 7, budget);
          signal(C_R + 16 * ((og + 1) % 3) + 8 + wq * 4, 2 * og + 9);
          TS(35 + 2 * (8 * layer + m));
        }
        drain();
        st64c(tg(tag), rws, pfl, 0);
      }
    }
  } else {
    const int ct = rk & 7, sy = rk >> 3, Q = w & 3;
    const bool isL = w >= 4;
    const int lane = tid & 63, g = lane >> 4, t = lane & 15;
    const rsrc_t rx0 = __builtin_amdgcn_make_buffer_rsrc((void *)p.x, 0, 2048 * 1280, 0x00020000);
    v2u tag;
    unsigned vrow[5];
#pragma unroll
    for (int m = 0; m < 5; ++m) { const int idx = m * 64 + lane; vrow[m] = (idx / 10) * 1280 + (idx % 10) * 16 + 160 * ct; }
    {
      const int tl = 2 * (7 - w) + sy;
      const bool act = t < 10;
      v4u d[4];
#pragma unroll
      for (int k = 0; k < 4; ++k) d[k] = ld128(rx0, act ? g * 2560 + 160 * ct + 16 * t : OOB, row0 + tl * 40960 + k * 10240);
      const rsrc_t rb2 = __builtin_amdgcn_make_buffer_rsrc((void *)p.b2, 0, 640 * 2, 0x00020000);
      const unsigned short b2v = __builtin_amdgcn_raw_buffer_load_b16(rb2, tid >= 80 && tid < 160 ? 160 * ct + 2 * (tid - 80) : OOB, 0, 0);
      {
        const rsrc_t rw = __builtin_amdgcn_make_buffer_rsrc((void *)p.wo, 0, 640 * 640 * 2, 0x00020000);
        for (int blk = w; blk < 20; blk += 8) {
#pragma unroll
          for (int m = 0; m < 5; ++m) glds(rw, vrow[m], blk * 40960, blk * 5120 + m * 1024);
        }
      }
      tag = get_tag();
#pragma unroll
      for (int k = 0; k < 4; ++k) {
        const v4u pk = d[k];
        const float r[8] = {bflo(pk.x), bfhi(pk.x), bflo(pk.y), bfhi(pk.y), bflo(pk.z), bfhi(pk.z), bflo(pk.w), bfhi(pk.w)};
        const float s = sum16(((r[0] + r[1]) + (r[2] + r[3])) + ((r[4] + r[5]) + (r[6] + r[7])));
        const float mu = s * (1.f / 80.f);
        float q = 0.f;
#pragma unroll
        for (int i = 0; i < 8; ++i) { const float dd = r[i] - mu; q += dd * dd; }
        q = sum16(act ? q : 0.f);
        const v2u tv = tg(tag);
        st128c(v4u{f2u(s), tv.x, f2u(q), tv.y}, rws, t == 0 ? (4 * k + g) * 128 + ct * 16 : OOB, gb + OFF_REC + tl * 2048);
        st128c(pk, rws, act ? ct * 2560 + (4 * k + g) * 160 + ((t ^ (k >> 1)) * 16) : OOB, gb + OFF_XF + tl * XT);
      }
      if (tid < 160) lds_st32(L_B2 + tid * 4, (unsigned)b2v << 16);
    }
    TS(1);
    v4u w2[7][5];
    {
      const rsrc_t rw = __builtin_amdgcn_make_buffer_rsrc((void *)p.w2, 0, 1728 * 640 * 2, 0x00020000);
      const unsigned stg = L_PART + w * 5120;
      const unsigned tra = stg + (8 * g + ((lane >> 2) & 3)) * 160 + (lane & 3) * 8;
#pragma unroll
      for (int k = 0; k < 7; ++k)
#pragma unroll
        for (int m = 0; m < 5; ++m) w2[k][m] = ld128(rw, vrow[m] + (w < 7 || k < 4 ? 7 * w + k : k == 6 ? 53 : 54) * 40960, 0);
      sbar();
#pragma unroll
      for (int k = 0; k < 7; ++k) {
#pragma unroll
        for (int m = 0; m < 5; ++m) lds_st128(stg + m * 1024 + lane * 16, w2[k][m]);
        cfence();
#pragma unroll
        for (int j = 0; j < 5; ++j) {
          const v2u lo = ldstr(tra + j * 32), hi = ldstr(tra + j * 32 + 640);
          w2[k][j] = v4u{lo.x, lo.y, hi.x, hi.y};
        }
        cfence();
        sbar();
      }
    }
    TS(2);
    drain();
    bar();
    TS(3);
    const unsigned r4 = (lane >> 2) & 3, c4 = lane & 3;
    const unsigned wa0 = (160 * Q + (isL ? 120 : 0) + 4 * g + r4) * 160 + c4 * 8;
    const unsigned wa1 = isL ? (160 * Q + 40 * g + 16 + r4) * 160 + c4 * 8 : wa0 + 6400;
    const unsigned pv3 = (4 * Q + c4) * 128, pv5 = umin((7 * w + umin(lane, 6)) >> 2, 13) * 128;
    const unsigned pw = L_PART + Q * PSZ + g * 272 + t * 16;
    const int e = lane & 31, ec = umin(e, 19), tok = 2 * w + (lane >> 5);
    const bool act = e < 20;
    const unsigned ea = L_PART + ec * 272 + tok * 16, ba = L_B2 + ec * 16;
    const unsigned xw = ct * 2560 + tok * 160 + (((ec >> 1) ^ (tok >> 3)) * 16) + (ec & 1) * 8;
    const unsigned xm = tok * 2560 + ct * 160 + ec * 8;
    const unsigned drop = act && !(e & 1) ? 0u : OOB;
    const unsigned hv = lane * 16;
    const unsigned xs = xw + drop, ra = ct * 16 + tok * 128;
    const unsigned fo3 = (5 * Q + (isL ? 3 : 0)) * 1024, fo5 = 7 * w * 1024, fx3 = isL ? OOB : 0u, fx5 = w == 7 ? 1024u : 3072u;
    v4u h[7];
    v2u pv = ~tag, rn = {0u, 0u}, rp = {0u, 0u}, rq = {0u, 0u};
    unsigned recS = 0, recQ = 0, recA = OOB;
    bool have = false, nd = false, pend = false;
    asm volatile("" : "=v"(h[0]), "=v"(h[1]), "=v"(h[2]), "=v"(h[3]), "=v"(h[4]), "=v"(h[5]), "=v"(h[6]));
    auto job = [&](unsigned r) AI { return ((2 * r) ^ 14) + sy; };
    auto poll = [&](unsigned r) AI { ald64c(pv, rws, sel(r & 8, pv5, pv3), gb + OFF_FL + job(r) * 2048); };
    auto pok = [&](auto nc) AI {
      return vwne<decltype(nc)::value>(pv, tag) == 0 || budget <= 0;
    };
    auto flush = [&]() AI {
      if (pend) {
        const v2u tv = tg(tag);
        ast128c(v4u{recS, tv.x, recQ, tv.y}, rws, recA, 0);
        recA = OOB;
        pend = false;
      }
    };
    auto fetch = [&](unsigned r) AI {
      const bool p5 = r & 8;
      const unsigned sa = gb + OFF_ACT + job(r) * HT + sel(p5, fo5, fo3);
      const unsigned sb = sa + sel(p5, 0, fx3), sc = sel(p5, sa, OOB), sd = sc + fx5;
      asm volatile("s_nop 4\n\t"
                   "buffer_load_dwordx4 %0, %7, %8, %9 offen sc1\n\t"
                   "buffer_load_dwordx4 %1, %7, %8, %9 offen offset:1024 sc1\n\t"
                   "buffer_load_dwordx4 %2, %7, %8, %10 offen offset:2048 sc1\n\t"
                   "buffer_load_dwordx4 %3, %7, %8, %11 offen offset:3072 sc1\n\t"
                   "buffer_load_dwordx4 %4, %7, %8, %12 offen offset:1024 sc1\n\t"
                   "buffer_load_dwordx4 %5, %7, %8, %12 offen offset:2048 sc1\n\t"
                   "buffer_load_dwordx4 %6, %7, %8, %12 offen offset:3072 sc1"
                   : "+v"(h[0]), "+v"(h[1]), "+v"(h[2]), "+v"(h[3]), "+v"(h[4]), "+v"(h[5]), "+v"(h[6])
                   : "v"(hv), "s"(rws), "s"(sa), "s"(sb), "s"(sc), "s"(sd) : "memory");
      nd = false;
    };
    auto finish = [&](unsigned j) AI {
      const unsigned a = ea + (j & 1) * 4 * PSZ, n = job(j);
      const f32x4 p0 = ldsf4(a), p1 = ldsf4(a + PSZ), p2 = ldsf4(a + 2 * PSZ), p3 = ldsf4(a + 3 * PSZ);
      const f32x4 bb = ldsf4(ba + (j & 8) * 40);
      const f32x4 y = ((p0 + p1) + (p2 + p3)) + (bb + f32x4{bflo(rp.x), bfhi(rp.x), bflo(rp.y), bfhi(rp.y)});
      const v2u pk = pk4(y);
      const v4u d = {pk.x, pk.y, f2u(dpp<0xF5>(u2f(pk.x))), f2u(dpp<0xF5>(u2f(pk.y)))};
      nd = true;
      if (j >= 24) {
        ast128c(d, __builtin_amdgcn_make_buffer_rsrc(p.out, 0, 2048 * 1280, 0x00020000), xm + drop, row0 + (n & 15) * 40960);
      } else {
        ast128c(d, rws, xs, gb + OFF_XF + (n + 16) * XT);
        const float r0 = bflo(pk.x), r1 = bfhi(pk.x), r2 = bflo(pk.y), r3 = bfhi(pk.y);
        const float sm = sum32(act ? (r0 + r1) + (r2 + r3) : 0.f);
        const float mu = sm * (1.f / 80.f);
        const float d0 = r0 - mu, d1 = r1 - mu, d2 = r2 - mu, d3 = r3 - mu;
        float q = (d0 * d0 + d1 * d1) + (d2 * d2 + d3 * d3);
        asm volatile("" : "+v"(q));
        q = sum32(act ? q : 0.f);
        unsigned na = gb + OFF_REC + (n + 16) * 2048 + ra;
        asm volatile("" : "+v"(q), "+v"(na));
        const bool me = e == (int)(j & 1);
        recS = me ? f2u(sm) : recS; recQ = me ? f2u(q) : recQ; recA = me ? na : recA;
        pend = true;
      }
      TS(6 + 3 * j);
    };
    for (unsigned r = 0;; ++r) {
      rp = rq;
      const bool efirst = isL || !have;
      if (!have) poll(r);
      if (efirst && r) finish(r - 1);
      if (r == 32) break;
      if (!have) {
        bool ok = pok(IC<1>{});
        while (!ok) {
          budget -= 8;
          poll(r);
          ok = pok(IC<0>{});
          flush();
        }
        fetch(r);
      }
      if (!nd) ald64c(rn, rws, hv, OOB);
      poll(r + 1);
      ald64c(rn, rws, xw, gb + OFF_XF + job(r) * XT);
      f32x4 acc[5];
      if (r & 8) {
        vwz<9>(acc, w2[0], h[0]);
        TS(4 + 3 * r);
        vwa<8>(acc, w2[1], h[1]); vwa<7>(acc, w2[2], h[2]); vwa<6>(acc, w2[3], h[3]);
        vwa<5>(acc, w2[4], h[4]); vwa<4>(acc, w2[5], h[5]); vwa<3>(acc, w2[6], h[6]);
      } else {
        v4u wf[10];
#pragma unroll
        for (int j = 0; j < 10; ++j) {
          const unsigned a = sel(j < 5, wa0, wa1) + (j % 5) * 32;
          const v2u lo = ldstr(a), hi = ldstr(a + 3200);
          wf[j] = v4u{lo.x, lo.y, hi.x, hi.y};
        }
        vwz<9>(acc, wf, h[0]);
        TS(4 + 3 * r);
#pragma unroll
        for (int j = 0; j < 5; ++j) {
          const v2u lo = ldstr(wa0 + 12800 + j * 32), hi = ldstr(wa0 + 16000 + j * 32);
          wf[j] = v4u{lo.x, lo.y, hi.x, hi.y};
        }
        vwa<8>(acc, wf + 5, h[1]);
        if (!isL) vwa<7>(acc, wf, h[2]);
      }
      mfin(acc);
      const unsigned a = pw + (r & 1) * 4 * PSZ;
      if (isL) {
        wait1(C_PF + 4 * Q, r + 1, budget);
        f32x4 o[5];
#pragma unroll
        for (int j = 0; j < 5; ++j) o[j] = ldsf4(a + j * 1088);
        sbar();
#pragma unroll
        for (int j = 0; j < 5; ++j) o[j] += acc[j];
        awds5(a, o);
        bar();
      } else {
        awds5(a, acc);
        signal(C_PF + 4 * Q, r + 1);
      }
      TS(5 + 3 * r);
      have = pok(IC<0>{}) && r < 31;
      asm volatile("v_mov_b64 %0, %1" : "=v"(rq) : "v"(rn) : "memory");
      flush();
      if (have) fetch(r + 1);
      if (!efirst) finish(r - 1);
      if (!isL) bar();
    }
    drain();
  }
}

extern "C" void kernel_launch(void *const *d_in, const int *in_sizes, int n_in, void *d_out, int out_size, void *d_ws,
                              size_t ws_size, hipStream_t stream) {
  Params p;
  p.x = d_in[0]; p.wqkv = d_in[1]; p.wo = d_in[2]; p.w1 = d_in[4]; p.b1 = d_in[5]; p.w2 = d_in[6]; p.b2 = d_in[7];
  p.out = d_out; p.ws = d_ws;
  (void)hipFuncSetAttribute((const void *)k_fwd, hipFuncAttributeMaxDynamicSharedMemorySize, L_TOTAL);
  k_fwd<<<256, 512, L_TOTAL, stream>>>(p);
}
```

```cpp
#include <hip/hip_runtime.h>
#include <stdint.h>

typedef unsigned v4u __attribute__((ext_vector_type(4)));
typedef unsigned v2u __attribute__((ext_vector_type(2)));
typedef float f32x4 __attribute__((ext_vector_type(4)));
typedef float f32x2 __attribute__((ext_vector_type(2)));
typedef float f32x32 __attribute__((ext_vector_type(32)));
typedef float f32x16 __attribute__((ext_vector_type(16)));
typedef __bf16 bf16x8 __attribute__((ext_vector_type(8)));
typedef __bf16 bf16x2 __attribute__((ext_vector_type(2)));
typedef short s16x4 __attribute__((ext_vector_type(4)));
typedef short s16x2 __attribute__((ext_vector_type(2)));
typedef __amdgpu_buffer_rsrc_t rsrc_t;
#define LAS __attribute__((address_space(3)))
#define AI __attribute__((always_inline))
#define DI __device__ __forceinline__
#define UNLIKELY(x) __builtin_expect(!!(x), 0)
#define WLK 28
#define SPLITX 1
template <int V> struct IC { static constexpr int value = V; };
template <int N, int I = 0, class F> DI void static_for(F &f) {
  if constexpr (I < N) { f(IC<I>{}); static_for<N, I + 1>(f); }
}

constexpr unsigned XT = 20480, HT = 55296, GRB = 8u << 20;
constexpr unsigned OFF_FL = 0;
constexpr unsigned OFF_KVF = OFF_FL + 64 * 2048;
constexpr unsigned OFF_REC = OFF_KVF + 2 * 2048;
constexpr unsigned OFF_KV = OFF_REC + 64 * 2048;
constexpr unsigned OFF_ONE = OFF_KV + 2 * 16 * 4096;
constexpr unsigned OFF_XF = OFF_ONE + 16 * 2048;
constexpr unsigned OFF_ACT = OFF_XF + 64 * XT;
constexpr unsigned OFF_E = GRB - 8192, OFF_ACK = GRB - 4096;
static_assert(OFF_ACT + 64 * HT + 2048 <= OFF_E, "ws map");
constexpr unsigned WS_END = 8 * GRB;
constexpr unsigned OOB = 0x40000000u;
constexpr unsigned L_QKV = 122880, QP = 272, QSL = 32 * QP, L_HALO = L_QKV + 3 * QSL, L_HST = L_HALO + 16 * QP;
constexpr unsigned L_STG = 155648;
constexpr unsigned L_PART = 102400, PSZ = 5120, L_RDY = L_PART + 8 * PSZ;
constexpr unsigned L_CTL = 159744, L_TS = 160256, L_DUMMY = 162304, L_TOTAL = 163840;
constexpr unsigned C_TAG = L_CTL;
constexpr unsigned C_R = L_CTL + 16, C_MD = L_CTL + 64, C_XF = L_CTL + 96, C_ZERO = L_CTL + 128, C_WL = L_CTL + 176;
constexpr unsigned C_PF = L_CTL + 64, C_FD = L_CTL + 96, C_GO = L_CTL + 100;
static_assert(L_HST + 3 * 512 <= L_STG, "lds map");
constexpr int SPIN_CAP = 1 << 18;

struct Params {
  const void *x, *wqkv, *wo, *w1, *b1, *w2, *b2;
  void *out, *ws;
};

DI void cfence() { asm volatile("" ::: "memory"); }
DI void bar() { asm volatile("s_waitcnt lgkmcnt(0)\n\ts_barrier" ::: "memory"); }
DI void drain() { asm volatile("s_waitcnt vmcnt(0)" ::: "memory"); }
template <int N> DI void vmw() { asm volatile("s_waitcnt vmcnt(%0)" ::"n"(N) : "memory"); }
template <int N> DI void vmw(v4u &d) { asm volatile("s_waitcnt vmcnt(%1)" : "+v"(d) : "n"(N) : "memory"); }
#define MF5(C0, C1, C2, C3, C4)                                                                             \
  "v_mfma_f32_16x16x32_bf16 %0, %6, %5, " C0 "\n\tv_mfma_f32_16x16x32_bf16 %1, %7, %5, " C1 "\n\t"           \
  "v_mfma_f32_16x16x32_bf16 %2, %8, %5, " C2 "\n\tv_mfma_f32_16x16x32_bf16 %3, %9, %5, " C3 "\n\t"           \
  "v_mfma_f32_16x16x32_bf16 %4, %10, %5, " C4 "\n\t"
#define RELOAD "s_mov_b64 exec, %14\n\tbuffer_load_dwordx4 %5, %11, %12, %13 offen offset:%16 sc1\n\ts_mov_b64 exec, -1"
template <int N, int OFF, bool FIRST> DI void ygrp(f32x4 (&a)[5], const v4u *w, v4u &h, unsigned vo, rsrc_t r, unsigned so, uint64_t m) {
  if constexpr (FIRST) {
    asm volatile("s_waitcnt vmcnt(%15)\n\t" MF5("0", "0", "0", "0", "0") RELOAD
                 : "=&v"(a[0]), "=&v"(a[1]), "=&v"(a[2]), "=&v"(a[3]), "=&v"(a[4]), "+v"(h)
                 : "v"(w[0]), "v"(w[1]), "v"(w[2]), "v"(w[3]), "v"(w[4]), "v"(vo), "s"(r), "s"(so), "s"(m), "n"(N), "n"(OFF) : "memory");
  } else {
    asm volatile("s_waitcnt vmcnt(%15)\n\t" MF5("%0", "%1", "%2", "%3", "%4") RELOAD
                 : "+v"(a[0]), "+v"(a[1]), "+v"(a[2]), "+v"(a[3]), "+v"(a[4]), "+v"(h)
                 : "v"(w[0]), "v"(w[1]), "v"(w[2]), "v"(w[3]), "v"(w[4]), "v"(vo), "s"(r), "s"(so), "s"(m), "n"(N), "n"(OFF) : "memory");
  }
}
template <int N> DI void ygrp_in(f32x4 (&a)[5], v4u &h, unsigned vo, rsrc_t r, unsigned so, uint64_t m) {
  asm volatile("s_waitcnt vmcnt(%10)\n\t"
               "v_mfma_f32_16x16x32_bf16 %0, %0, %5, 0\n\tv_mfma_f32_16x16x32_bf16 %1, %1, %5, 0\n\t"
               "v_mfma_f32_16x16x32_bf16 %2, %2, %5, 0\n\tv_mfma_f32_16x16x32_bf16 %3, %3, %5, 0\n\t"
               "v_mfma_f32_16x16x32_bf16 %4, %4, %5, 0\n\t"
               "s_mov_b64 exec, %9\n\tbuffer_load_dwordx4 %5, %6, %7, %8 offen sc1\n\ts_mov_b64 exec, -1"
               : "+v"(a[0]), "+v"(a[1]), "+v"(a[2]), "+v"(a[3]), "+v"(a[4]), "+v"(h)
               : "v"(vo), "s"(r), "s"(so), "s"(m), "n"(N) : "memory");
}
DI void mfin(f32x4 (&a)[5]) {
  asm volatile("s_nop 7" : "+v"(a[0]), "+v"(a[1]), "+v"(a[2]), "+v"(a[3]), "+v"(a[4])::"memory");
}
DI void awds5(unsigned p, f32x4 (&a)[5]) {
  asm volatile("ds_write_b128 %0, %1\n\tds_write_b128 %0, %2 offset:1024\n\tds_write_b128 %0, %3 offset:2048\n\t"
               "ds_write_b128 %0, %4 offset:3072\n\tds_write_b128 %0, %5 offset:4096"
               ::"v"(p), "v"(a[0]), "v"(a[1]), "v"(a[2]), "v"(a[3]), "v"(a[4]) : "memory");
}
#define YPOLL(EX, PM, PV, RS, PS) \
  "s_mov_b32 m0, " PM "\n\ts_mov_b64 exec, " EX "\n\tbuffer_load_dword " PV ", " RS ", " PS " offen sc1 lds\n\ts_mov_b64 exec, -1\n\t"
DI void ypoll(uint64_t ex, unsigned pm, unsigned pv, rsrc_t r, unsigned ps) {
  asm volatile(YPOLL("%4", "%0", "%1", "%2", "%3")::"s"(pm), "v"(pv), "s"(r), "s"(ps), "s"(ex) : "memory");
}
#define YLD(D, VO, RS, SO, OFF) "buffer_load_dwordx4 " D ", " VO ", " RS ", " SO " offen offset:" OFF " sc1\n\t"
DI void yfetch(v4u (&h)[8], unsigned vo, rsrc_t r, unsigned sa, unsigned sb, unsigned sc, unsigned go, uint64_t ex, unsigned pm, unsigned pv, unsigned ps) {
  asm volatile("s_cmp_eq_u32 %12, 0\n\ts_cbranch_scc1 1f\n\t"
               YLD("%0", "%8", "%9", "%10", "0") YLD("%1", "%8", "%9", "%10", "1024") YLD("%2", "%8", "%9", "%10", "2048")
               YLD("%3", "%8", "%9", "%10", "3072") YPOLL("%17", "%13", "%14", "%9", "%15")
               YLD("%4", "%8", "%9", "%11", "0") YLD("%5", "%8", "%9", "%11", "1024") YLD("%6", "%8", "%9", "%16", "2048")
               YLD("%7", "%8", "%9", "%16", "3072") "\n1:"
               : "+v"(h[0]), "+v"(h[1]), "+v"(h[2]), "+v"(h[3]), "+v"(h[4]), "+v"(h[5]), "+v"(h[6]), "+v"(h[7])
               : "v"(vo), "s"(r), "s"(sa), "s"(sb), "s"(go), "s"(pm), "v"(pv), "s"(ps), "s"(sc), "s"(ex) : "memory", "scc");
}
DI void yfetch(v4u (&h)[3], unsigned vo, rsrc_t r, unsigned sa, unsigned, unsigned sc, unsigned go, uint64_t, unsigned, unsigned, unsigned) {
  asm volatile("s_cmp_eq_u32 %6, 0\n\ts_cbranch_scc1 1f\n\t"
               YLD("%0", "%3", "%4", "%5", "0") YLD("%1", "%3", "%4", "%5", "1024") YLD("%2", "%3", "%4", "%7", "2048") "\n1:"
               : "+v"(h[0]), "+v"(h[1]), "+v"(h[2]) : "v"(vo), "s"(r), "s"(sa), "s"(go), "s"(sc) : "memory", "scc");
}
template <int O1> DI void yst3(v4u a, v4u b, v4u c, unsigned v0, unsigned v2, rsrc_t r, unsigned so) {
  asm volatile("buffer_store_dwordx4 %0, %3, %5, %6 offen sc1\n\t"
               "buffer_store_dwordx4 %1, %3, %5, %6 offen offset:%7 sc1\n\t"
               "buffer_store_dwordx4 %2, %4, %5, %6 offen sc1\n\ts_nop 1"
               ::"v"(a), "v"(b), "v"(c), "v"(v0), "v"(v2), "s"(r), "s"(so), "n"(O1) : "memory");
}
DI unsigned eqall(unsigned v, unsigned t, unsigned lanes) {
  unsigned m;
  asm volatile("v_cmp_ne_u32 vcc, %1, %2\n\ts_and_b32 vcc_lo, vcc_lo, %3\n\ts_cselect_b32 %0, 0, -1" : "=s"(m) : "v"(v), "v"(t), "s"(lanes) : "vcc", "scc");
  return m;
}
DI float fmul1(float a, float b) { float d; asm("v_mul_f32 %0, %1, %2" : "=v"(d) : "v"(a), "v"(b)); return d; }
DI float sqs(float a) { float d; asm("v_mul_f32 %0, %1, |%1|" : "=v"(d) : "v"(a)); return d; }
DI void padd(f32x4 &a, f32x4 b) {
  f32x2 lo = a.xy, hi = a.zw;
  asm("v_pk_add_f32 %0, %0, %1" : "+v"(lo) : "v"(b.xy));
  asm("v_pk_add_f32 %0, %0, %1" : "+v"(hi) : "v"(b.zw));
  a = f32x4{lo.x, lo.y, hi.x, hi.y};
}
DI unsigned msel(unsigned m, unsigned a, unsigned b) { return m ? a : b; }
DI void sbar() { __builtin_amdgcn_sched_barrier(0); }
DI float u2f(unsigned u) { return __builtin_bit_cast(float, u); }
DI unsigned f2u(float f) { return __builtin_bit_cast(unsigned, f); }
DI float bflo(unsigned u) { return u2f(u << 16); }
DI float bfhi(unsigned u) { return u2f(u & 0xffff0000u); }
DI unsigned rfl(unsigned v) { return __builtin_amdgcn_readfirstlane(v); }
DI int lane_id() {
  int l;
  asm volatile("v_mbcnt_lo_u32_b32 %0, -1, 0\n\tv_mbcnt_hi_u32_b32 %0, -1, %0" : "=v"(l));
  return l;
}
DI unsigned pk2(float a, float b) {
  f32x2 v = {a, b};
  return __builtin_bit_cast(unsigned, __builtin_convertvector(v, bf16x2));
}
DI v2u pk4(f32x4 v) { return v2u{pk2(v[0], v[1]), pk2(v[2], v[3])}; }
DI f32x4 mfma16(v4u a, v4u b, f32x4 c) {
  return __builtin_amdgcn_mfma_f32_16x16x32_bf16(__builtin_bit_cast(bf16x8, a), __builtin_bit_cast(bf16x8, b), c, 0, 0, 0);
}
DI f32x16 mfma32(v4u a, v4u b, f32x16 c) {
  return __builtin_amdgcn_mfma_f32_32x32x16_bf16(__builtin_bit_cast(bf16x8, a), __builtin_bit_cast(bf16x8, b), c, 0, 0, 0);
}
DI float dot1(unsigned a, float c) {
  return __builtin_amdgcn_fdot2_f32_bf16(__builtin_bit_cast(bf16x2, a), __builtin_bit_cast(bf16x2, 0x3f803f80u), c, false);
}
DI float fmx(float a, float b) { return __builtin_elementwise_maximum(a, b); }
DI float half_sum(float x) {
  auto t = __builtin_amdgcn_permlane32_swap(f2u(x), f2u(x), false, false);
  return u2f(t[0]) + u2f(t[1]);
}
DI float row_sum(float x) {
  auto s = __builtin_amdgcn_permlane16_swap(f2u(x), f2u(x), false, false);
  return half_sum(u2f(s[0]) + u2f(s[1]));
}
DI float row_sum2(float a, float b) {
  auto s = __builtin_amdgcn_permlane16_swap(f2u(a), f2u(b), false, false);
  return half_sum(u2f(s[0]) + u2f(s[1]));
}
DI v4u ld128(rsrc_t r, unsigned vo, unsigned so) { return __builtin_amdgcn_raw_buffer_load_b128(r, vo, so, 0); }
DI v4u ld128c(rsrc_t r, unsigned vo, unsigned so) { return __builtin_amdgcn_raw_buffer_load_b128(r, vo, so, 16); }
DI v2u ld64c(rsrc_t r, unsigned vo, unsigned so) { return __builtin_amdgcn_raw_buffer_load_b64(r, vo, so, 16); }
DI void st128c(v4u v, rsrc_t r, unsigned vo, unsigned so) {
  asm volatile("" : "+v"(v));
  __builtin_amdgcn_raw_buffer_store_b128(v, r, vo, so, 16);
  asm volatile("s_nop 1" ::"v"(v) : "memory");
}
DI void st64c(v2u v, rsrc_t r, unsigned vo, unsigned so) { __builtin_amdgcn_raw_buffer_store_b64(v, r, vo, so, 16); }
template <int OFF> DI void ald128c(v4u &d, rsrc_t r, unsigned vo, unsigned so) {
  asm volatile("buffer_load_dwordx4 %0, %1, %2, %3 offen offset:%4 sc1" : "+v"(d) : "v"(vo), "s"(r), "s"(so), "n"(OFF) : "memory");
}
DI void ald128(v4u &d, rsrc_t r, unsigned vo, unsigned so) {
  asm volatile("buffer_load_dwordx4 %0, %1, %2, %3 offen" : "=&v"(d) : "v"(vo), "s"(r), "s"(so) : "memory");
}
DI void ald4x128c(v4u (&d)[4], rsrc_t r, unsigned vo, unsigned so) {
  asm volatile("s_nop 4\n\t"
               "buffer_load_dwordx4 %0, %4, %5, %6 offen sc1\n\t"
               "buffer_load_dwordx4 %1, %4, %5, %6 offen offset:1024 sc1\n\t"
               "buffer_load_dwordx4 %2, %4, %5, %6 offen offset:2048 sc1\n\t"
               "buffer_load_dwordx4 %3, %4, %5, %6 offen offset:3072 sc1"
               : "+v"(d[0]), "+v"(d[1]), "+v"(d[2]), "+v"(d[3]) : "v"(vo), "s"(r), "s"(so) : "memory");
}
DI void ald4x128w(v4u (&d)[4], rsrc_t r, unsigned vo, unsigned so) {
  asm volatile("s_nop 4\n\t"
               "buffer_load_dwordx4 %0, %4, %5, %6 offen sc1\n\t"
               "buffer_load_dwordx4 %1, %4, %5, %6 offen offset:1024 sc1\n\t"
               "buffer_load_dwordx4 %2, %4, %5, %6 offen offset:2048 sc1\n\t"
               "buffer_load_dwordx4 %3, %4, %5, %6 offen offset:3072 sc1\n\ts_waitcnt vmcnt(0)"
               : "=&v"(d[0]), "=&v"(d[1]), "=&v"(d[2]), "=&v"(d[3]) : "v"(vo), "s"(r), "s"(so) : "memory");
}
DI void ast128c(v4u d, rsrc_t r, unsigned vo, unsigned so) {
  asm volatile("s_nop 4\n\tbuffer_store_dwordx4 %0, %1, %2, %3 offen sc1\n\ts_nop 1" ::"v"(d), "v"(vo), "s"(r), "s"(so) : "memory");
}
DI void glds(rsrc_t r, unsigned vo, unsigned so, unsigned dst) {
  unsigned keep;
  asm volatile("s_mov_b32 %0, m0\n\ts_mov_b32 m0, %1\n\ts_nop 2\n\tbuffer_load_dwordx4 %2, %3, %4 offen lds\n\ts_mov_b32 m0, %0"
               : "=&s"(keep) : "s"(dst), "v"(vo), "s"(r), "s"(so) : "memory");
}
DI void glds4c(rsrc_t r, unsigned vo, unsigned so, unsigned dst) {
  unsigned keep;
  asm volatile("s_mov_b32 %0, m0\n\ts_mov_b32 m0, %1\n\ts_nop 2\n\t"
               "buffer_load_dwordx4 %2, %3, %4 offen sc1 lds\n\t"
               "buffer_load_dwordx4 %2, %3, %4 offen offset:1024 sc1 lds\n\t"
               "buffer_load_dwordx4 %2, %3, %4 offen offset:2048 sc1 lds\n\t"
               "buffer_load_dwordx4 %2, %3, %4 offen offset:3072 sc1 lds\n\t"
               "s_mov_b32 m0, %0"
               : "=&s"(keep) : "s"(dst), "v"(vo), "s"(r), "s"(so) : "memory");
}
DI v4u lds128(unsigned a) { return *(LAS v4u *)(uintptr_t)a; }
DI f32x4 ldsf4(unsigned a) { return *(LAS f32x4 *)(uintptr_t)a; }
DI void lds_st128(unsigned a, v4u v) { *(LAS v4u *)(uintptr_t)a = v; }
DI void lds_stf4(unsigned a, f32x4 v) { *(LAS f32x4 *)(uintptr_t)a = v; }
DI void lds_st32(unsigned a, unsigned v) { *(LAS unsigned *)(uintptr_t)a = v; }
DI v2u ldstr(unsigned a) {
  return __builtin_bit_cast(v2u, __builtin_amdgcn_ds_read_tr16_b64_v4i16((LAS s16x4 *)(uintptr_t)a));
}
template <int OFF> DI void ards128(v4u &d, unsigned a) {
  asm volatile("ds_read_b128 %0, %1 offset:%2" : "+v"(d) : "v"(a), "n"(OFF) : "memory");
}
template <int OFF> DI void awds128(unsigned a, v4u d) {
  asm volatile("ds_write_b128 %0, %1 offset:%2" ::"v"(a), "v"(d), "n"(OFF) : "memory");
}
template <int OFF> DI void awds32(unsigned a, unsigned d) {
  asm volatile("ds_write_b32 %0, %1 offset:%2" ::"v"(a), "v"(d), "n"(OFF) : "memory");
}
DI unsigned sel(bool c, unsigned a, unsigned b) { return c ? a : b; }
DI unsigned umin(unsigned a, unsigned b) { return a < b ? a : b; }
DI void signal(unsigned a, unsigned v) {
  cfence();
  *(volatile LAS unsigned *)(uintptr_t)a = v;
  cfence();
}
DI void signal2(unsigned a, unsigned v) {
  cfence();
  *(volatile LAS v2u *)(uintptr_t)a = v2u{v, v};
  cfence();
}
DI unsigned peek4(unsigned a) {
  const v4u v = *(volatile LAS v4u *)(uintptr_t)a;
  return rfl(umin(umin(v.x, v.y), umin(v.z, v.w)));
}
DI void wait4(unsigned a, unsigned target, int &budget) {
  cfence();
  if (UNLIKELY(peek4(a) < target)) {
    do { --budget; __builtin_amdgcn_s_sleep(1); } while (peek4(a) < target && budget > 0);
  }
  cfence();
}
DI void wait2(unsigned a, unsigned target, int &budget) {
  cfence();
  for (;;) {
    const v2u v = *(volatile LAS v2u *)(uintptr_t)a;
    if (rfl(umin(v.x, v.y)) >= target || budget <= 0) break;
    --budget;
    __builtin_amdgcn_s_sleep(1);
  }
  cfence();
}
DI void wait1(unsigned a, unsigned target, int &budget) {
  cfence();
  if (UNLIKELY(rfl(*(volatile LAS unsigned *)(uintptr_t)a) < target)) {
    do { --budget; __builtin_amdgcn_s_sleep(1); } while (rfl(*(volatile LAS unsigned *)(uintptr_t)a) < target && budget > 0);
  }
  cfence();
}
DI v2u tg(v2u t) {
  unsigned a = t.x, b = t.y;
  asm volatile("" : "+s"(a), "+s"(b));
  return v2u{a, b};
}
template <int N> DI void wmfma0(f32x16 &d, v4u a, v4u b) {
  asm volatile("s_waitcnt lgkmcnt(%3)\n\tv_mfma_f32_32x32x16_bf16 %0, %1, %2, 0" : "=&v"(d) : "v"(a), "v"(b), "n"(N) : "memory");
}
template <int N> DI void wmfma(f32x16 &d, v4u a, v4u b) {
  asm volatile("s_waitcnt lgkmcnt(%3)\n\tv_mfma_f32_32x32x16_bf16 %0, %1, %2, %0" : "+v"(d) : "v"(a), "v"(b), "n"(N) : "memory");
}
template <int N> DI void wmfmac(f32x16 &d, v4u a, v4u b, f32x16 c) {
  asm volatile("s_waitcnt lgkmcnt(%4)\n\tv_mfma_f32_32x32x16_bf16 %0, %1, %2, %3" : "=&v"(d) : "v"(a), "v"(b), "v"(c), "n"(N) : "memory");
}
DI bool any_true(bool p) { return __builtin_amdgcn_ballot_w64(p) != 0; }
DI bool all_true(bool p) { return __builtin_amdgcn_ballot_w64(!p) == 0; }
DI void poll64(rsrc_t r, unsigned vo, unsigned so, v2u tag, int &budget) {
  for (;;) {
    cfence();
    const v2u v = ld64c(r, vo, so);
    if (all_true(v.x == tag.x && v.y == tag.y) || budget <= 0) break;
    budget -= 8;
    __builtin_amdgcn_s_sleep(2);
  }
  cfence();
}
#define TS(i)
template <int K> struct XO { static constexpr int v = K * 512; };
constexpr int m_w1(int set) { return set == 0 ? 6 : 10; }
constexpr int m_w2(int set) { return set == 0 ? 10 : 18; }
constexpr int m_extra(int set, int s) {
  s = (s + 41) % 41;
  return (s == 14) + (s == 34) + (s == 36) + (s == m_w1(set)) + 2 * (s == m_w2(set));
}
constexpr int m_nout(int set, int s) {
  return 3 + m_extra(set, s - 4) + m_extra(set, s - 3) + m_extra(set, s - 2) + m_extra(set, s - 1);
}

__global__ void __launch_bounds__(512, 2) k_fwd(Params p) {
  const int tid = threadIdx.x;
  const int w = rfl(tid >> 6);
  const int bid = blockIdx.x;
  const bool isY = bid & 1;
  const int grp = 2 * ((bid & 7) >> 1) + (bid >> 7), rk = (bid >> 3) & 15;
  const rsrc_t rws = __builtin_amdgcn_make_buffer_rsrc(p.ws, 0, (int)WS_END, 0x00020000);
  const unsigned gb = grp * GRB;
  const unsigned row0 = (unsigned)(512 * (grp & 3) + (grp >> 2)) * 1280u;
  int budget = SPIN_CAP;
  TS(0);
  v2u e0 = {0u, 0u};
  if (w == 7) e0 = ld64c(rws, 0, OFF_E);
  if (tid < 64) *(volatile LAS unsigned *)(uintptr_t)(L_CTL + tid * 4) = (tid >= 4 && tid < 16 && (tid & 2)) ? 7u : 0u;
  bar();
  if (w == 7) {
    const unsigned lo = rfl(e0.x) + 1u;
    const unsigned hi = rfl(e0.y) + (lo == 0u ? 1u : 0u);
    *(volatile LAS v4u *)(uintptr_t)C_TAG = v4u{lo, hi, 1u, 0u};
    if ((tid & 63) == 0) st64c(v2u{lo, hi}, rws, bid * 8, OFF_ACK);
  }
  auto get_tag = [&]() AI {
    wait1(C_TAG + 8, 1u, budget);
    const v2u v = *(volatile LAS v2u *)(uintptr_t)C_TAG;
    return v2u{rfl(v.x), rfl(v.y)};
  };

  if (!isY) {
    const int c = rk, set = w >> 2, wq = w & 3;
    const int n1 = 4 * c + wq;
    const bool live = set == 0 || n1 < 54;
    const int step = c >= 14 ? 2 : 1;
    v2u tag = {0u, 0u};
    auto load_units = [&](int og0, int og1, int v, int base, int lane) AI {
      v4u pr[4];
      asm volatile("" : "=v"(pr[0]), "=v"(pr[1]), "=v"(pr[2]), "=v"(pr[3]));
      auto rso = [&](int og) AI { return gb + OFF_REC + (v * 16 + 2 * (7 - (og & 7))) * 2048; };
      auto okP = [&]() AI {
        const unsigned a = (pr[0].y ^ tag.x) | (pr[1].y ^ tag.x) | (pr[2].y ^ tag.x) | (pr[3].y ^ tag.x);
        const unsigned b = (pr[0].w ^ tag.y) | (pr[1].w ^ tag.y) | (pr[2].w ^ tag.y) | (pr[3].w ^ tag.y);
        return all_true((a | b) == 0u) || budget <= 0;
      };
      ald4x128c(pr, rws, lane * 16, rso(og0));
      for (int og = og0; og < og1; og += 2) {
        const int u = 7 - (og & 7);
        const unsigned slot = og % 3, xr = C_R + 16 * slot, dst = slot * 40960;
        const unsigned xfa = C_XF + ((og - 3 >= base ? v : v - step) & 1) * 16;
        asm volatile("s_waitcnt vmcnt(0)" : "+v"(pr[0]), "+v"(pr[1]), "+v"(pr[2]), "+v"(pr[3])::"memory");
        while (!okP()) {
          budget -= 8;
          __builtin_amdgcn_s_sleep(1);
          ald4x128w(pr, rws, lane * 16, rso(og));
        }
        TS(34 + 2 * (8 * v + 7 - u));
        {
          if (og >= 3) wait4(xfa, og - 2, budget);
          const unsigned sx = gb + OFF_XF + (v * 16 + 2 * u) * XT;
#pragma unroll
          for (int k = 0; k < 10; ++k) {
            const unsigned o = (k & 1) * XT + (k >> 1) * 4096;
            glds4c(rws, lane * 16, sx + o, dst + o);
          }
        }
        const float S = row_sum2(u2f(pr[0].x) + u2f(pr[1].x), u2f(pr[2].x) + u2f(pr[3].x));
        auto sb = __builtin_amdgcn_permlane16_swap(f2u(S), f2u(S), false, false);
        float q[4];
#pragma unroll
        for (int k = 0; k < 4; ++k) {
          const float d = u2f(pr[k].x) * (1.f / 80.f) - u2f(sb[k >> 1]) * (1.f / 640.f);
          q[k] = u2f(pr[k].z) + 80.f * d * d;
        }
        const float Q = row_sum2(q[0] + q[1], q[2] + q[3]);
        const float nm = S * (-1.f / 640.f), var = Q * (1.f / 640.f) + 1e-5f;
        const float rstd = __builtin_amdgcn_rsqf(var), sd = var * rstd;
        const unsigned mh = pk2(nm, sd);
        lds_st128(L_HST + slot * 512 + (lane & 31) * 16,
                  v4u{pk2(nm, nm - bflo(mh)), mh, pk2(sd - bfhi(mh), 0.f), f2u(rstd) & ~0x80u});
        ald4x128c(pr, rws, lane * 16, og + 2 < og1 ? rso(og + 2) : OOB);
        vmw<20>();
        signal(xr, 2 * og + 1);
        vmw<4>();
        signal(xr, 2 * og + 2);
        TS(35 + 2 * (8 * v + 7 - u));
      }
      asm volatile("s_waitcnt vmcnt(0)" : "+v"(pr[0]), "+v"(pr[1]), "+v"(pr[2]), "+v"(pr[3])::"memory");
    };
    if (set == 1) {
      tag = get_tag();
      if (wq >= 2) load_units(wq - 2, 3, 0, 0, lane_id());
      wait1(C_WL + wq * 4, 1u, budget);
    }
    v4u wr[40], csf;
    unsigned braw;
    {
      const int lane = lane_id();
      const rsrc_t rw = set == 0 ? __builtin_amdgcn_make_buffer_rsrc((void *)p.wqkv, 0, 640 * 1920 * 2, 0x00020000)
                                 : __builtin_amdgcn_make_buffer_rsrc((void *)p.w1, 0, live ? 640 * 1728 * 2 : 0, 0x00020000);
      const rsrc_t rb = __builtin_amdgcn_make_buffer_rsrc((void *)p.b1, 0, 1728 * 2, 0x00020000);
      const unsigned pitch = set == 0 ? 3840u : 3456u;
      unsigned col;
      if (set == 0) {
        const int cc = 4 * wq + (lane & 3);
        col = cc == 15 ? OOB / 2 : 640 * (cc / 5) + 40 * c + 8 * (cc % 5);
      } else {
        col = 32 * n1 + 8 * (lane & 3);
      }
      const unsigned vo = (lane >> 2) * pitch + col * 2;
      const int i = lane & 31, q4 = (i >> 2) & 3;
      const unsigned bc = 32 * n1 + (i & 16) + 8 * (q4 & 1) + 4 * (q4 >> 1) + (i & 3);
      asm volatile("s_nop 4\n\tbuffer_load_ushort %0, %1, %2, 0 offen" : "=&v"(braw) : "v"(bc * 2), "s"(rb) : "memory");
#pragma unroll
      for (int k = 0; k < 40; ++k) ald128(wr[k], rw, vo, k * 16 * pitch);
      asm volatile("" : "=v"(csf));
    }
    if (set == 0) tag = get_tag();

    for (int ph = 0, base = 0; ph < 4; ph += step, base += 8) {
      const int layer = ph >> 1;
      const bool isM = set == (ph & 1);
      int lane = lane_id();
      if (isM && ph < 2) {
        const unsigned stg = (set == 0 ? L_HALO : L_STG) + wq * 1024;
        const int G = lane >> 4, r4 = (lane >> 2) & 3, c4 = lane & 3;
        const unsigned tra = stg + (8 * (G >> 1) + r4) * 64 + 32 * (G & 1) + 16 * (c4 & 1) + 8 * (c4 >> 1);
        float cs[4] = {0.f, 0.f, 0.f, 0.f};
        auto tr = [&](auto kc) AI {
          constexpr int k = decltype(kc)::value;
          if constexpr (k < 40) {
            vmw<39 - k>(wr[k]);
            lds_st128(stg + lane * 16, wr[k]);
            cfence();
            const v2u lo = ldstr(tra), hi = ldstr(tra + 256);
            cfence();
            wr[k] = v4u{lo.x, lo.y, hi.x, hi.y};
            if constexpr (k == WLK) signal(C_WL + wq * 4, 1u);
          }
          if constexpr (k >= 2) {
            const v4u z = wr[k - 2];
            cs[0] = dot1(z.x, cs[0]); cs[1] = dot1(z.y, cs[1]); cs[2] = dot1(z.z, cs[2]); cs[3] = dot1(z.w, cs[3]);
          }
          sbar();
        };
        static_for<42>(tr);
        asm volatile("" : "+v"(braw));
        float bias = bflo(braw);
        if (set == 0) bias = (wq == 3 && (lane & 20) == 20) ? 1.f : 0.f;
        const float c0 = half_sum((cs[0] + cs[1]) + (cs[2] + cs[3]));
        const unsigned h2 = pk2(c0, c0);
        const float cl = c0 - bflo(h2);
        csf = v4u{h2, pk2(cl, bias), pk2(bias, 0.f), 0u};
        if (lane >= 32) csf = v4u{0u, 0u, 0u, 0u};
        TS(1);
      }
      if (wq >= 2) {
        const int og0 = isM ? base : ph == 0 ? 6 - wq : base + wq - 1;
        const int og1 = isM ? (wq == 2 && ph != 0 ? base + 1 : base) : base + 8;
        if (og0 < og1) load_units(og0, og1, ph, base, lane);
      }
      if (isM) {
        auto mloop = [&](auto setc) AI {
          constexpr int SET = decltype(setc)::value;
          const unsigned xl = ((lane >> 4) & 1) * XT + (lane & 15) * 16 + (lane >> 5) * 256;
          const unsigned ql = L_QKV + (lane & 31) * QP + 64 * wq + (lane >> 5) * 16;
          const unsigned hl = L_HST + (lane & 31) * 16, dl = L_DUMMY + lane * 16;
          v4u b[5], bx, rec;
          f32x16 out;
          asm volatile("" : "=v"(b[0]), "=v"(b[1]), "=v"(b[2]), "=v"(b[3]), "=v"(b[4]), "=v"(bx), "=v"(rec), "=v"(out));
          unsigned slot = base % 3;
          wait1(C_R + 16 * slot, 2 * base + 1, budget);
          unsigned xa = xl + slot * 40960, ra = C_R + 16 * slot, qa = dl, mda = dl, mdv = 0, ha = 0;
          ards128<XO<0>::v>(b[0], xa); ards128<XO<1>::v>(b[1], xa); ards128<XO<2>::v>(b[2], xa); ards128<XO<3>::v>(b[3], xa);
          __builtin_amdgcn_s_setprio(2);
          for (unsigned og = base;; ++og) {
            const unsigned nslot = slot == 2 ? 0 : slot + 1;
            TS(2 + 2 * (8 * layer + (og & 7)));
            f32x16 acc;
            unsigned o[8], u;
            float t0, t1;
            const bool last = (og & 7) == 7;
            unsigned lim;
            auto relu2 = [&](unsigned x) AI {
              return __builtin_bit_cast(unsigned, __builtin_elementwise_max(__builtin_bit_cast(s16x2, x), s16x2{0, 0}));
            };
            auto mm = [&](auto kc) AI {
              constexpr int s = decltype(kc)::value;
              if constexpr (s == 0) wmfma0<m_nout(SET, s)>(acc, wr[0], b[0]);
              else wmfma<m_nout(SET, s)>(acc, wr[s], b[s % 5]);
              if constexpr (s <= 35) ards128<XO<(s + 4) % 40>::v>(b[(s + 4) % 5], xa);
              else if constexpr (s == 36) ards128<0>(bx, ha);
              else ards128<XO<(s + 3) % 40>::v>(b[(s + 3) % 5], xa);
              if constexpr (s == 14 || s == 34) ards128<0>(rec, ra);
              if constexpr (s == 2) asm volatile("" : "+v"(out));
              sbar();
              if constexpr (SET == 0) {
                if constexpr (s == m_w1(SET)) awds128<0>(qa, v4u{o[0], o[1], o[2], o[3]});
                if constexpr (s >= 2 && s < 10) {
                  constexpr int j = s - 2;
                  o[j] = pk2(fmul1(out[2 * j], u2f(bx.w)), fmul1(out[2 * j + 1], u2f(bx.w)));
                  asm volatile("" : "+v"(o[j]));
                }
              } else if constexpr (s >= 2 && s <= 18) {
                constexpr int j = (s - 2) >> 1;
                if constexpr ((s & 1) == 0) {
                  if constexpr (j > 0) { o[j - 1] = relu2(u); sbar(); }
                  if constexpr (j < 8) {
                    t0 = fmul1(out[2 * j], u2f(bx.w)); t1 = fmul1(out[2 * j + 1], u2f(bx.w));
                    asm volatile("" : "+v"(t0), "+v"(t1));
                  }
                  if constexpr (j > 0) asm volatile("" : "+v"(o[j - 1]));
                  if constexpr (s == m_w1(SET)) awds128<0>(qa, v4u{o[0], o[1], o[2], o[3]});
                } else {
                  u = pk2(sqs(t0), sqs(t1));
                  asm volatile("" : "+v"(u));
                }
              }
              if constexpr (s == m_w2(SET)) { awds128<32>(qa, v4u{o[4], o[5], o[6], o[7]}); awds32<0>(mda, mdv); }
              if constexpr (s == 19) {
                asm volatile("" : "+v"(rec));
                if (UNLIKELY(any_true(rec.x < 2u * og + 2u))) wait1(C_R + 16 * slot, 2 * og + 2, budget);
              }
              if constexpr (s == 21) {
                ha = hl + slot * 512; ra = C_R + 16 * nslot; mdv = og + 1;
                asm volatile("" : "+v"(ha), "+v"(ra), "+v"(mdv));
              }
              if constexpr (s == 22) {
                qa = ql + slot * QSL; mda = C_MD + 4 * w;
                asm volatile("" : "+v"(qa), "+v"(mda));
              }
              if constexpr (s == 23) {
                lim = last ? ~0u : 2u * og + 3u;
                asm volatile("" : "+s"(lim));
              }
              if constexpr (s == 36) {
                awds32<32>(mda, mdv);
                xa = xl + nslot * 40960;
                asm volatile("" : "+v"(xa));
              }
              sbar();
            };
            static_for<40>(mm);
            asm volatile("" : "+v"(rec));
            uint64_t slow = __builtin_amdgcn_ballot_w64(umin(rec.x, umin(rec.z, rec.w)) < lim);
            asm volatile("" : "+s"(slow));
            TS(3 + 2 * (8 * layer + (og & 7)));
            wmfmac<m_nout(SET, 40)>(out, csf, bx, acc);
            if (UNLIKELY(slow != 0)) {
              asm volatile("s_nop 7\n\ts_nop 7\n\ts_nop 3" : "+v"(out) : "v"(acc));
              wait2(C_R + 16 * nslot + 8, 2 * og + 3, budget);
              const float rs = u2f(bx.w);
#pragma unroll
              for (int j = 0; j < 8; ++j) {
                float a = out[2 * j] * rs, bb = out[2 * j + 1] * rs;
                if (SET == 1) { a *= __builtin_fabsf(a); bb *= __builtin_fabsf(bb); }
                o[j] = pk2(a, bb);
                if (SET == 1) o[j] = relu2(o[j]);
              }
              awds128<0>(qa, v4u{o[0], o[1], o[2], o[3]});
              awds128<32>(qa, v4u{o[4], o[5], o[6], o[7]});
              awds32<0>(mda, mdv);
              if (last) break;
              qa = dl; mda = dl;
              wait1(C_R + 16 * nslot, 2 * og + 3, budget);
              ards128<XO<0>::v>(b[0], xa); ards128<XO<1>::v>(b[1], xa); ards128<XO<2>::v>(b[2], xa);
            }
            ards128<XO<3>::v>(b[3], xa);
            slot = nslot;
          }
          __builtin_amdgcn_s_setprio(0);
          asm volatile("s_waitcnt lgkmcnt(0)" : "+v"(b[0]), "+v"(b[1]), "+v"(b[2])::"memory");
        };
        if (!live) {
          signal(C_MD + w * 4, base + 8);
          signal(C_XF + w * 4, base + 8);
        } else if (set == 0) {
          mloop(IC<0>{});
        } else {
          mloop(IC<1>{});
        }
        if (bid == 120 && w == 3 && ph == 0) {
          for (;;) {
            cfence();
            bool ok = true;
#pragma unroll
            for (int k = 0; k < 2; ++k) {
              const v4u a = ld128c(rws, lane * 16 + k * 1024, OFF_ACK);
              ok = ok && a.x == tag.x && a.y == tag.y && a.z == tag.x && a.w == tag.y;
            }
            if (all_true(ok)) { if (lane == 0) st64c(tg(tag), rws, 0, OFF_E); break; }
            if (budget <= 0) break;
            budget -= 8;
            __builtin_amdgcn_s_sleep(2);
          }
        }
      }
      if (wq >= 2) continue;
      lane = lane_id();
      if (ph & 1) {
        if (isM) continue;
        const unsigned ra = L_QKV + (lane & 15) * QP + (lane >> 4) * 16;
        const unsigned v0 = lane * 16, v1 = c == 13 ? OOB : v0;
        for (int i = wq; i < 8; i += 2) {
          const unsigned og = base + i, slot = og % 3, u = 7 - i;
          wait4(C_MD + 16, og + 1, budget);
          TS(66 + 2 * (8 * layer + i));
          v4u d[8];
#pragma unroll
          for (int k = 0; k < 8; ++k) d[k] = lds128(ra + slot * QSL + (k >> 2) * 16 * QP + (k & 3) * 64);
          signal2(C_R + 16 * ((og + 1) % 3) + 8, 2 * og + 9);
          const unsigned so = gb + OFF_ACT + (ph * 16 + 2 * u) * HT + c * 4096;
#pragma unroll
          for (int k = 0; k < 8; ++k) st128c(d[k], rws, ((k & 3) < 2 ? v0 : v1) + (k & 3) * 1024, so + (k >> 2) * HT);
          drain();
          if (lane < 2) st64c(tg(tag), rws, lane * 2048, gb + OFF_FL + (ph * 16 + 2 * u) * 2048 + c * 128);
          TS(67 + 2 * (8 * layer + i));
        }
      } else {
        const int g = lane >> 4, t = lane & 15, c4 = lane & 3;
        const bool hi = wq == 0;
        const bool has_halo = (grp & 3) != 0, give_halo = (grp & 3) != 3;
        const int m0 = set ? 0 : 7 - wq, m1 = set ? 7 - wq : 8;
        f32x4 cP, cQ;
#pragma unroll
        for (int r = 0; r < 4; ++r) {
          const int j = 4 * g + r;
          cP[r] = j >= t + 9 ? 0.f : -INFINITY;
          cQ[r] = (j <= t && j >= t - 7) ? 0.f : -INFINITY;
        }
        const unsigned mp0 = (4 * g > t ? 0xffffu : 0u) | (4 * g + 1 > t ? 0xffff0000u : 0u);
        const unsigned mp1 = (4 * g + 2 > t ? 0xffffu : 0u) | (4 * g + 3 > t ? 0xffff0000u : 0u);
        const unsigned mqa = g == 2 ? 0u : ~0u, mqb = g == 0 ? 0u : ~0u;
        const unsigned kl = t * QP + 16 * g;
        const unsigned vrow = (4 * g + ((lane >> 2) & 3)) * QP;
        const unsigned v0 = vrow + 160 + c4 * 8, v1a = vrow + (c4 ? 240 : 192), v1b = vrow + (c4 ? 240 : 232);
        const unsigned so1 = lane < 16 ? 4096 + (c & 3) * 256 + lane * 16 : OOB;
        bool pend = false;
        unsigned pfl = OOB;
        for (int m = m0; m < m1; ++m) {
          const unsigned og = base + m, u = 7 - m;
          const unsigned need = og + (hi || m == 7 ? 1 : 2);
          if (pend && (m == 7 || peek4(C_MD) < need)) { drain(); st64c(tg(tag), rws, pfl, 0); pend = false; }
          if (!hi && m == 7) {
            if (has_halo) {
              const unsigned id = layer * 16 + c;
              poll64(rws, 0, gb - GRB + OFF_KVF + id * 128, tag, budget);
              v4u d[4];
#pragma unroll
              for (int k = 0; k < 4; ++k) d[k] = ld128c(rws, lane * 16 + k * 1024, gb - GRB + OFF_KV + id * 4096);
#pragma unroll
              for (int k = 0; k < 4; ++k) lds_st128(L_HALO + (4 * k + g) * QP + t * 16, d[k]);
            } else {
              cP = f32x4{-INFINITY, -INFINITY, -INFINITY, -INFINITY};
            }
          }
          wait4(C_MD, need, budget);
          TS(34 + 2 * (8 * layer + m));
          unsigned rc, rp;
          if (hi) { rp = L_QKV + (og % 3) * QSL; rc = rp + 16 * QP; }
          else {
            rc = L_QKV + (og % 3) * QSL;
            rp = m < 7 ? L_QKV + ((og + 1) % 3) * QSL + 16 * QP : (has_halo ? (unsigned)L_HALO : rc);
          }
          const unsigned ac = rc + kl, ap = rp + kl, aq = g == 3 ? (unsigned)C_ZERO : ac;
          v4u qa = lds128(aq), kpa = lds128(ap + 80), kca = lds128(ac + 80);
          v4u qb = lds128(aq + 32), kpb = lds128(ap + 112), kcb = lds128(ac + 112);
          v2u vp[4], vc[4];
          vp[0] = ldstr(rp + v0); vc[0] = ldstr(rc + v0); vp[1] = ldstr(rp + v1a); vc[1] = ldstr(rc + v1a);
          vp[2] = ldstr(rp + v0 + 40); vc[2] = ldstr(rc + v0 + 40); vp[3] = ldstr(rp + v1b); vc[3] = ldstr(rc + v1b);
          qa.z &= mqa; qa.w &= mqa; qb.x &= mqb; qb.y &= mqb;
          const f32x4 spa = mfma16(kpa, qa, cP), sqa = mfma16(kca, qa, cQ);
          const f32x4 spb = mfma16(kpb, qb, cP), sqb = mfma16(kcb, qb, cQ);
          f32x4 sa, sb;
#pragma unroll
          for (int r = 0; r < 4; ++r) { sa[r] = fmx(spa[r], sqa[r]); sb[r] = fmx(spb[r], sqb[r]); }
          const float sc = 0.22360679774997896f * 1.4426950408889634f;
          float na, nb;
          {
            const float xa = fmx(fmx(sa[0], sa[1]), fmx(sa[2], sa[3])), xb = fmx(fmx(sb[0], sb[1]), fmx(sb[2], sb[3]));
            auto s1 = __builtin_amdgcn_permlane16_swap(f2u(xa), f2u(xb), false, false);
            const float y = fmx(u2f(s1[0]), u2f(s1[1]));
            auto s2 = __builtin_amdgcn_permlane32_swap(f2u(y), f2u(y), false, false);
            const float z = fmx(u2f(s2[0]), u2f(s2[1])) * -sc;
            auto s3 = __builtin_amdgcn_permlane16_swap(f2u(z), f2u(z), false, false);
            na = u2f(s3[0]); nb = u2f(s3[1]);
          }
#pragma unroll
          for (int r = 0; r < 4; ++r) {
            sa[r] = __builtin_amdgcn_exp2f(__builtin_fmaf(sa[r], sc, na));
            sb[r] = __builtin_amdgcn_exp2f(__builtin_fmaf(sb[r], sc, nb));
          }
          const v2u pa = pk4(sa), pb = pk4(sb);
          const unsigned a0 = pa.x & mp0, a1 = pa.y & mp1, b0 = pb.x & mp0, b1 = pb.y & mp1;
          const v4u opa = {a0, a1, pa.x ^ a0, pa.y ^ a1}, opb = {b0, b1, pb.x ^ b0, pb.y ^ b1};
          const f32x4 z4 = {0.f, 0.f, 0.f, 0.f};
          const f32x4 oa1 = mfma16(v4u{vp[1].x, vp[1].y, vc[1].x, vc[1].y}, opa, z4);
          const f32x4 ob1 = mfma16(v4u{vp[3].x, vp[3].y, vc[3].x, vc[3].y}, opb, z4);
          const f32x4 oa0 = mfma16(v4u{vp[0].x, vp[0].y, vc[0].x, vc[0].y}, opa, z4);
          const f32x4 ob0 = mfma16(v4u{vp[2].x, vp[2].y, vc[2].x, vc[2].y}, opb, z4);
          auto la = __builtin_amdgcn_permlane16_swap(f2u(oa1[0]), f2u(oa1[0]), false, false);
          auto lb = __builtin_amdgcn_permlane16_swap(f2u(ob1[0]), f2u(ob1[0]), false, false);
          const float ia = __builtin_amdgcn_rcpf(u2f(la[1])), ib = __builtin_amdgcn_rcpf(u2f(lb[1]));
          const v2u e0 = pk4(oa0 * ia), e1 = pk4(ob0 * ib), e2 = pk4(oa1 * ia), e3 = pk4(ob1 * ib);
          const unsigned T = ph * 16 + 2 * u + (hi ? 1 : 0);
          const unsigned so = gb + OFF_ACT + T * HT + 5 * (c >> 2) * 1024;
          st128c(v4u{e0.x, e0.y, e1.x, e1.y}, rws, lane * 16 + (c & 3) * 1024, so);
          st128c(v4u{e2.x, e2.y, e3.x, e3.y}, rws, so1, so);
          if (pend) { vmw<2>(); st64c(tg(tag), rws, pfl, 0); }
          pend = true;
          pfl = lane == 0 ? gb + OFF_FL + T * 2048 + c * 128 : OOB;
          if (hi && m == 0 && give_halo) {
            const unsigned id = layer * 16 + c;
#pragma unroll
            for (int k = 0; k < 4; ++k) st128c(lds128(rc + (4 * k + g) * QP + t * 16), rws, lane * 16 + k * 1024, gb + OFF_KV + id * 4096);
            if (lane == 1) pfl = gb + OFF_KVF + id * 128;
          }
          if (!hi && m == 6) wait1(C_R + 16 * (og % 3) + 12, 2 * og + 7, budget);
          signal(C_R + 16 * ((og + 1) % 3) + 8 + wq * 4, 2 * og + 9);
          TS(35 + 2 * (8 * layer + m));
        }
        drain();
        st64c(tg(tag), rws, pfl, 0);
      }
    }
  } else {
    const unsigned ct = rk & 7, sy = rk >> 3;
    const unsigned lane = tid & 63, g = lane >> 4, hv = lane * 16;
    if (w == 7) {
      f32x32 res[5];
      float recS, recQ;
      auto stats = [&](const f32x4 (&y)[5]) AI {
        const f32x4 s4 = ((y[0] + y[1]) + (y[2] + y[3])) + y[4];
        recS = row_sum((s4[0] + s4[1]) + (s4[2] + s4[3]));
        const float mu = recS * (1.f / 80.f);
        f32x4 q4 = {0.f, 0.f, 0.f, 0.f};
#pragma unroll
        for (int j = 0; j < 5; ++j) { const f32x4 d = y[j] - mu; q4 += d * d; }
        recQ = row_sum((q4[0] + q4[1]) + (q4[2] + q4[3]));
      };
      auto third = [&](v2u c) AI {
        auto sx = __builtin_amdgcn_permlane16_swap(c.x, c.x, false, false);
        auto sw = __builtin_amdgcn_permlane16_swap(c.y, c.y, false, false);
        return v4u{sx[0], sw[0], sx[1], sw[1]};
      };
      const unsigned odd = (hv & 256) << 22, v2 = 2048 + (hv & 240) + ((hv >> 9) << 8) + odd, rv = hv < 256 ? hv : OOB;
      const unsigned xm = (hv & 240) * 160;
      unsigned xo = gb + OFF_XF + (14 + sy) * XT + ct * 2560, ro = gb + OFF_REC + (14 + sy) * 2048 + ct * 256;
      {
        const rsrc_t rx0 = __builtin_amdgcn_make_buffer_rsrc((void *)p.x, 0, 2048 * 1280, 0x00020000);
        v4u a[8], b[8];
        v2u c[8];
#pragma unroll
        for (int i = 0; i < 8; ++i) {
          const unsigned xs = row0 + (14 - 2 * i + sy) * 40960 + ct * 160;
          a[i] = ld128(rx0, xm + g * 16, xs); b[i] = ld128(rx0, xm + g * 16 + 64, xs);
          c[i] = __builtin_amdgcn_raw_buffer_load_b64(rx0, xm + g * 8 + 128, xs, 0);
        }
        sbar();
#pragma unroll
        for (int i = 0; i < 8; ++i) {
          st128c(a[i], rws, hv, xo - 2 * i * XT); st128c(b[i], rws, hv + 1024, xo - 2 * i * XT); st128c(third(c[i]), rws, v2, xo - 2 * i * XT);
        }
        signal(C_GO, 1u);
#pragma unroll
        for (int i = 0; i < 8; ++i) {
#pragma unroll
          for (int k = 0; k < 4; ++k) {
            const int e = 4 * i + 2 * (k & 1);
            res[k >> 1][e] = bflo(a[i][k]); res[k >> 1][e + 1] = bfhi(a[i][k]);
            res[2 + (k >> 1)][e] = bflo(b[i][k]); res[2 + (k >> 1)][e + 1] = bfhi(b[i][k]);
            if (k < 2) { res[4][e] = bflo(c[i][k]); res[4][e + 1] = bfhi(c[i][k]); }
          }
        }
      }
      const v2u tag = get_tag();
      auto rec_store = [&](unsigned so) AI {
        const v2u tv = tg(tag);
        ast128c(v4u{f2u(recS), tv.x, f2u(recQ), tv.y}, rws, rv, so);
      };
      TS(1);
#pragma nounroll
      for (unsigned i = 0; i < 8; ++i) {
        f32x4 y[5];
        unsigned i4 = 4 * i;
        asm volatile("" : "+s"(i4));
        i4 &= 28;
#pragma unroll
        for (int k = 0; k < 20; ++k) y[k >> 2][k & 3] = res[k >> 2][i4 + (k & 3)];
        stats(y);
        if (i == 0) drain();
        rec_store(ro - i * 4096);
      }
      TS(2);
      signal(C_PF + 28, ~0u);
      *(volatile LAS v2u *)(uintptr_t)(L_RDY + tid * 8) = v2u{~tag.x, ~tag.y};
      bar();
      TS(3);
      bool pend = false;
      unsigned recA = 0;
      unsigned pa = L_PART + hv;
      asm volatile("" : "+v"(pa));
      pa &= 0x3ffff;
#pragma nounroll
      for (unsigned r = 0; r < 32; ++r) {
        const unsigned i = r & 7, n = ((2 * r) ^ 14) + 2;
        f32x4 y[5], A[5], B[5], C[5];
        unsigned i4 = 4 * i;
        asm volatile("" : "+s"(i4));
        i4 &= 28;
#pragma unroll
        for (int k = 0; k < 20; ++k) y[k >> 2][k & 3] = res[k >> 2][i4 + (k & 3)];
        asm volatile("" : "+v"(y[0]), "+v"(y[1]), "+v"(y[2]), "+v"(y[3]), "+v"(y[4]));
        auto rd = [&](f32x4 (&o)[5], int q) AI {
#pragma unroll
          for (int j = 0; j < 5; ++j) o[j] = ldsf4(pa + q * PSZ + j * 1024);
          sbar();
        };
        auto add = [&](const f32x4 (&o)[5]) AI {
#pragma unroll
          for (int j = 0; j < 5; ++j) padd(y[j], o[j]);
          sbar();
        };
        cfence();
        unsigned lo = peek4(C_PF), hi = peek4(C_PF + 16);
        if (UNLIKELY(lo <= r)) {
          if (pend) { drain(); rec_store(recA); pend = false; }
          wait4(C_PF, r + 1, budget);
          hi = peek4(C_PF + 16);
        }
        cfence();
        TS(4 + 3 * r);
        rd(A, 0); rd(B, 1); rd(C, 2);
        add(A); rd(A, 3); add(B);
        if (UNLIKELY(hi <= r)) {
          if (pend) { drain(); rec_store(recA); pend = false; }
          wait4(C_PF + 16, r + 1, budget);
        }
        cfence();
        rd(B, 4); add(C); rd(C, 5); add(A); rd(A, 6);
        signal(C_FD, r + 1);
        add(B); add(C); add(A);
        if (pend) { drain(); rec_store(recA); pend = false; }
        const v2u e0 = pk4(y[0]), e1 = pk4(y[1]), e2 = pk4(y[2]), e3 = pk4(y[3]);
        const v4u d0 = {e0.x, e0.y, e1.x, e1.y}, d1 = {e2.x, e2.y, e3.x, e3.y}, d2 = third(pk4(y[4]));
        if (r >= 24) {
          unsigned l = hv;
          asm volatile("" : "+v"(l));
          const unsigned xl = (l & 240) * 160;
          yst3<64>(d0, d1, d2, xl + ((l >> 8) << 4), xl + 128 + ((l >> 9) << 4) + ((l & 256) << 22),
                   __builtin_amdgcn_make_buffer_rsrc(p.out, 0, 2048 * 1280, 0x00020000), row0 + (14 - 2 * i + sy) * 40960 + ct * 160);
        } else {
          yst3<1024>(d0, d1, d2, hv, v2, rws, xo + n * XT);
          TS(5 + 3 * r);
          asm volatile("" : "+s"(i4));
          i4 &= 28;
#pragma unroll
          for (int k = 0; k < 20; ++k) res[k >> 2][i4 + (k & 3)] = y[k >> 2][k & 3];
          stats(y);
          recA = ro + n * 2048;
          pend = true;
        }
        TS(6 + 3 * r);
      }
      drain();
    } else {
      const unsigned r4 = (lane >> 2) & 3, c4 = lane & 3;
      v4u w2[8][5];
      auto fbase = [&](unsigned row) AI {
        v2u a = {row + c4 * 16, row + c4 * 8};
        asm volatile("" : "+v"(a.x), "+v"(a.y));
        return a;
      };
      auto frag = [&](v2u b, int j, unsigned hi) AI {
        const unsigned a = j < 4 ? b.x + (j & 1) * 8 + (j >> 1) * 64 : b.y + 128;
        const v2u lo = ldstr(a), up = ldstr(a + hi);
        return v4u{lo.x, lo.y, up.x, up.y};
      };
      const bool bias = w == 6;
      const unsigned ones = gb + OFF_ONE + rk * 2048;
      v2u tag;
      {
        unsigned vrow[5];
#pragma unroll
        for (int m = 0; m < 5; ++m) { const int idx = m * 64 + lane; vrow[m] = (idx / 10) * 1280 + (idx % 10) * 16 + 160 * ct; }
        {
          const rsrc_t rw = __builtin_amdgcn_make_buffer_rsrc((void *)p.wo, 0, 640 * 640 * 2, 0x00020000);
          for (unsigned blk = w; blk < 20; blk += 7) {
#pragma unroll
            for (int m = 0; m < 5; ++m) glds(rw, vrow[m], blk * 40960, blk * 5120 + m * 1024);
          }
          const v4u one = {0x3f803f80u, 0x3f803f80u, 0x3f803f80u, 0x3f803f80u};
          ast128c(one, rws, hv, bias ? ones : OOB);
          ast128c(one, rws, hv + 1024, bias ? ones : OOB);
        }
        tag = get_tag();
        *(volatile LAS v2u *)(uintptr_t)(L_RDY + tid * 8) = v2u{~tag.x, ~tag.y};
        wait1(C_GO, 1u, budget);
        TS(1);
        const rsrc_t rw = __builtin_amdgcn_make_buffer_rsrc((void *)p.w2, 0, 1728 * 640 * 2, 0x00020000);
        const rsrc_t rw6 = __builtin_amdgcn_make_buffer_rsrc((void *)(bias ? p.b2 : p.w2), 0, bias ? 640 * 2 : 1728 * 640 * 2, 0x00020000);
        const unsigned s0 = 8 * w * 40960;
#pragma unroll
        for (int k = 0; k < 8; ++k)
#pragma unroll
          for (int m = 0; m < 5; ++m)
            w2[k][m] = k == 6 ? ld128(rw6, vrow[m], bias ? 0u : s0 + 6 * 40960) : ld128(rw, vrow[m], s0 + k * 40960);
        sbar();
        const unsigned stg = L_PART + w * PSZ;
        const v2u tra = fbase(stg + (8 * g + r4) * 160);
#pragma unroll
        for (int k = 0; k < 8; ++k) {
#pragma unroll
          for (int m = 0; m < 5; ++m) lds_st128(stg + m * 1024 + hv, w2[k][m]);
          cfence();
#pragma unroll
          for (int j = 0; j < 5; ++j) w2[k][j] = frag(tra, j, 640);
          cfence();
          sbar();
        }
      }
      TS(2);
      drain();
      bar();
      TS(3);
      if (w < 4) __builtin_amdgcn_s_setprio(1);
      const unsigned pvo = (lane >> 1) * 128 + (lane & 1) * 4, tagv = lane & 1 ? tag.y : tag.x, ck = (lane & 31) * 4;
      const unsigned pw = L_PART + w * PSZ + hv;
      auto phase = [&](auto pc, unsigned r0) AI {
        constexpr bool P5 = decltype(pc)::value;
        constexpr int K = P5 ? 8 : 3, N = P5 ? 9 : 3;
        const unsigned q = w & 3, n0 = 2 * r0 + 14 + sy;
        const unsigned lanes = P5 ? 15u << 4 * w : w < 3 ? 63u << 8 * w : (255u << 8 * q) | (3u << (24 + 2 * q));
        const uint64_t ex = lanes;
        unsigned fl = rfl(gb + OFF_FL + n0 * 2048);
        unsigned ac = gb + OFF_ACT + n0 * HT + (P5 ? 8 * w : 5 * q + (w < 3 ? 0 : 3)) * 1024;
        unsigned pm = L_RDY + r0 * 128;
        const unsigned d3 = w < 3 ? 0u : w == 3 ? OOB : (10 - 4 * q) * 1024;
        auto last = [&](unsigned a) AI { return P5 ? (bias ? ones - 2048 : a + 4096) : a + d3; };
        auto ready = [&](unsigned a) AI { return eqall(*(volatile LAS unsigned *)(uintptr_t)(ck + a), tagv, lanes); };
        v4u h[K], wo[10];
#pragma unroll
        for (int k = 0; k < K; ++k) asm volatile("" : "=v"(h[k]));
        v2u wr0 = {0u, 0u};
        if constexpr (!P5) {
          wr0 = fbase((160 * q + (w < 3 ? 0 : 120) + 4 * g + r4) * 160);
          const v2u wr1 = fbase((160 * q + (w < 3 ? 40 + 4 * g : 16 + 40 * g) + r4) * 160);
          const v2u wr2 = fbase(((w < 3 ? 160 * q + 80 : 480 + 40 * q) + 4 * g + r4) * 160);
#pragma unroll
          for (int j = 0; j < 5; ++j) { wo[j] = frag(wr1, j, 3200); wo[5 + j] = frag(wr2, j, 3200); }
        }
        unsigned have = 0;
        for (unsigned r = r0; r < r0 + 8; ++r, fl -= 4096, ac -= 2 * HT, pm += 128) {
          const unsigned pm1 = r < r0 + 7 ? pm + 128 : L_DUMMY, pm2 = r < r0 + 6 ? pm + 256 : L_DUMMY;
          f32x4 acc[5];
          if constexpr (!P5) {
#pragma unroll
            for (int j = 0; j < 5; ++j) acc[j] = __builtin_bit_cast(f32x4, frag(wr0, j, 3200));
          }
          if (!have) {
            while (!ready(pm) && budget > 0) {
              ypoll(ex, pm, pvo, rws, fl);
              budget -= 2;
              __builtin_amdgcn_s_sleep(2);
            }
          }
          yfetch(h, hv, rws, ac, ac + 4096, last(ac), ~have, ex, pm1, pvo, fl - 4096);
          const unsigned nxt = ready(pm + 128) & (r < r0 + 7 ? ~0u : 0u);
          const unsigned qm = msel(nxt, pm2, pm1), qf = fl - 4096 - (nxt & 4096);
          ypoll(ex, qm, pvo, rws, qf);
          const unsigned so = msel(nxt, ac - 2 * HT, OOB), sl = msel(nxt, last(ac - 2 * HT), OOB);
          const uint64_t em = ((uint64_t)nxt << 32) | (nxt | 1u);
          if constexpr (P5) {
            ygrp<N, 0, true>(acc, w2[0], h[0], hv, rws, so, em);
            TS(4 + 3 * r);
            ygrp<N, 1024, false>(acc, w2[1], h[1], hv, rws, so, em);
            ygrp<N, 2048, false>(acc, w2[2], h[2], hv, rws, so, em);
            ygrp<N, 3072, false>(acc, w2[3], h[3], hv, rws, so, em);
            ypoll(ex, qm, pvo, rws, qf);
            ygrp<N, 0, false>(acc, w2[4], h[K - 4], hv, rws, so + 4096, em);
            ygrp<N, 1024, false>(acc, w2[5], h[K - 3], hv, rws, so + 4096, em);
            ygrp<N, 2048, false>(acc, w2[6], h[K - 2], hv, rws, sl, em);
          } else {
            ygrp_in<N>(acc, h[0], hv, rws, so, em);
            TS(4 + 3 * r);
            ygrp<N, 1024, false>(acc, wo, h[1], hv, rws, so, em);
          }
          const unsigned fd = *(volatile LAS unsigned *)(uintptr_t)C_FD;
          ygrp<N, P5 ? 3072 : 2048, false>(acc, P5 ? w2[7] : wo + 5, h[K - 1], hv, rws, sl, em);
          have = nxt;
          if (UNLIKELY(rfl(fd) < r)) wait1(C_FD, r, budget);
          mfin(acc);
          awds5(pw, acc);
          signal(C_PF + 4 * w, r + 1);
          TS(5 + 3 * r);
        }
#pragma unroll
        for (int k = 0; k < K; ++k) vmw<0>(h[k]);
      };
#pragma nounroll
      for (unsigned r0 = 0; r0 < 32; r0 += 16) {
        phase(IC<0>{}, r0);
        phase(IC<1>{}, r0 + 8);
      }
    }
  }
}

extern "C" void kernel_launch(void *const *d_in, const int *in_sizes, int n_in, void *d_out, int out_size, void *d_ws,
                              size_t ws_size, hipStream_t stream) {
  Params p;
  p.x = d_in[0]; p.wqkv = d_in[1]; p.wo = d_in[2]; p.w1 = d_in[4]; p.b1 = d_in[5]; p.w2 = d_in[6]; p.b2 = d_in[7];
  p.out = d_out; p.ws = d_ws;
  (void)hipFuncSetAttribute((const void *)k_fwd, hipFuncAttributeMaxDynamicSharedMemorySize, L_TOTAL);
  k_fwd<<<256, 512, L_TOTAL, stream>>>(p);
}
```

```cpp
#include <hip/hip_runtime.h>
#include <stdint.h>

typedef unsigned v4u __attribute__((ext_vector_type(4)));
typedef unsigned v2u __attribute__((ext_vector_type(2)));
typedef float f32x4 __attribute__((ext_vector_type(4)));
typedef float f32x2 __attribute__((ext_vector_type(2)));
typedef float f32x32 __attribute__((ext_vector_type(32)));
typedef float f32x16 __attribute__((ext_vector_type(16)));
typedef __bf16 bf16x8 __attribute__((ext_vector_type(8)));
typedef __bf16 bf16x2 __attribute__((ext_vector_type(2)));
typedef short s16x4 __attribute__((ext_vector_type(4)));
typedef short s16x2 __attribute__((ext_vector_type(2)));
typedef __amdgpu_buffer_rsrc_t rsrc_t;
#define LAS __attribute__((address_space(3)))
#define AI __attribute__((always_inline))
#define DI __device__ __forceinline__
#define UNLIKELY(x) __builtin_expect(!!(x), 0)
#define WLK 28
#define SPLITX 1
#define WARM 0
template <int V> struct IC { static constexpr int value = V; };
template <int N, int I = 0, class F> DI void static_for(F &f) {
  if constexpr (I < N) { f(IC<I>{}); static_for<N, I + 1>(f); }
}

constexpr unsigned XT = 20480, HT = 55296, GRB = 8u << 20;
constexpr unsigned OFF_FL = 0;
constexpr unsigned OFF_KVF = OFF_FL + 64 * 2048;
constexpr unsigned OFF_REC = OFF_KVF + 2 * 2048;
constexpr unsigned OFF_KV = OFF_REC + 64 * 2048;
constexpr unsigned OFF_ONE = OFF_KV + 2 * 16 * 4096;
constexpr unsigned OFF_XF = OFF_ONE + 16 * 2048;
constexpr unsigned OFF_ACT = OFF_XF + 64 * XT;
constexpr unsigned OFF_E = GRB - 8192, OFF_ACK = GRB - 4096;
static_assert(OFF_ACT + 64 * HT + 2048 <= OFF_E, "ws map");
constexpr unsigned WS_END = 8 * GRB;
constexpr unsigned OOB = 0x40000000u;
constexpr unsigned L_QKV = 122880, QP = 272, QSL = 32 * QP, L_HALO = L_QKV + 3 * QSL, L_HST = L_HALO + 16 * QP;
constexpr unsigned L_STG = 155648;
constexpr unsigned L_PART = 102400, PSZ = 5120, L_RDY = L_PART + 8 * PSZ;
constexpr unsigned L_CTL = 159744, L_TS = 160256, L_DUMMY = 162304, L_TOTAL = 163840;
constexpr unsigned C_TAG = L_CTL;
constexpr unsigned C_R = L_CTL + 16, C_MD = L_CTL + 64, C_XF = L_CTL + 96, C_ZERO = L_CTL + 128, C_WL = L_CTL + 176;
constexpr unsigned C_PF = L_CTL + 64, C_FD = L_CTL + 96, C_GO = L_CTL + 100;
static_assert(L_HST + 3 * 512 <= L_STG, "lds map");
constexpr int CODE_BYTES = 36000;
constexpr int SPIN_CAP = 1 << 18;

struct Params {
  const void *x, *wqkv, *wo, *w1, *b1, *w2, *b2;
  void *out, *ws;
};

DI void cfence() { asm volatile("" ::: "memory"); }
DI void bar() { asm volatile("s_waitcnt lgkmcnt(0)\n\ts_barrier" ::: "memory"); }
DI void drain() { asm volatile("s_waitcnt vmcnt(0)" ::: "memory"); }
template <int N> DI void vmw() { asm volatile("s_waitcnt vmcnt(%0)" ::"n"(N) : "memory"); }
template <int N> DI void vmw(v4u &d) { asm volatile("s_waitcnt vmcnt(%1)" : "+v"(d) : "n"(N) : "memory"); }
#define MF5(C0, C1, C2, C3, C4)                                                                             \
  "v_mfma_f32_16x16x32_bf16 %0, %6, %5, " C0 "\n\tv_mfma_f32_16x16x32_bf16 %1, %7, %5, " C1 "\n\t"           \
  "v_mfma_f32_16x16x32_bf16 %2, %8, %5, " C2 "\n\tv_mfma_f32_16x16x32_bf16 %3, %9, %5, " C3 "\n\t"           \
  "v_mfma_f32_16x16x32_bf16 %4, %10, %5, " C4 "\n\t"
#define RELOAD "s_mov_b64 exec, %14\n\tbuffer_load_dwordx4 %5, %11, %12, %13 offen offset:%16 sc1\n\ts_mov_b64 exec, -1"
template <int N, int OFF, bool FIRST> DI void ygrp(f32x4 (&a)[5], const v4u *w, v4u &h, unsigned vo, rsrc_t r, unsigned so, uint64_t m) {
  if constexpr (FIRST) {
    asm volatile("s_waitcnt vmcnt(%15)\n\t" MF5("0", "0", "0", "0", "0") RELOAD
                 : "=&v"(a[0]), "=&v"(a[1]), "=&v"(a[2]), "=&v"(a[3]), "=&v"(a[4]), "+v"(h)
                 : "v"(w[0]), "v"(w[1]), "v"(w[2]), "v"(w[3]), "v"(w[4]), "v"(vo), "s"(r), "s"(so), "s"(m), "n"(N), "n"(OFF) : "memory");
  } else {
    asm volatile("s_waitcnt vmcnt(%15)\n\t" MF5("%0", "%1", "%2", "%3", "%4") RELOAD
                 : "+v"(a[0]), "+v"(a[1]), "+v"(a[2]), "+v"(a[3]), "+v"(a[4]), "+v"(h)
                 : "v"(w[0]), "v"(w[1]), "v"(w[2]), "v"(w[3]), "v"(w[4]), "v"(vo), "s"(r), "s"(so), "s"(m), "n"(N), "n"(OFF) : "memory");
  }
}
template <int N> DI void ygrp_in(f32x4 (&a)[5], v4u &h, unsigned vo, rsrc_t r, unsigned so, uint64_t m) {
  asm volatile("s_waitcnt vmcnt(%10)\n\t"
               "v_mfma_f32_16x16x32_bf16 %0, %0, %5, 0\n\tv_mfma_f32_16x16x32_bf16 %1, %1, %5, 0\n\t"
               "v_mfma_f32_16x16x32_bf16 %2, %2, %5, 0\n\tv_mfma_f32_16x16x32_bf16 %3, %3, %5, 0\n\t"
               "v_mfma_f32_16x16x32_bf16 %4, %4, %5, 0\n\t"
               "s_mov_b64 exec, %9\n\tbuffer_load_dwordx4 %5, %6, %7, %8 offen sc1\n\ts_mov_b64 exec, -1"
               : "+v"(a[0]), "+v"(a[1]), "+v"(a[2]), "+v"(a[3]), "+v"(a[4]), "+v"(h)
               : "v"(vo), "s"(r), "s"(so), "s"(m), "n"(N) : "memory");
}
DI void mfin(f32x4 (&a)[5]) {
  asm volatile("s_nop 7" : "+v"(a[0]), "+v"(a[1]), "+v"(a[2]), "+v"(a[3]), "+v"(a[4])::"memory");
}
DI void awds5(unsigned p, f32x4 (&a)[5]) {
  asm volatile("ds_write_b128 %0, %1\n\tds_write_b128 %0, %2 offset:1024\n\tds_write_b128 %0, %3 offset:2048\n\t"
               "ds_write_b128 %0, %4 offset:3072\n\tds_write_b128 %0, %5 offset:4096"
               ::"v"(p), "v"(a[0]), "v"(a[1]), "v"(a[2]), "v"(a[3]), "v"(a[4]) : "memory");
}
#define YPOLL(EX, PM, PV, RS, PS) \
  "s_mov_b32 m0, " PM "\n\ts_mov_b64 exec, " EX "\n\tbuffer_load_dword " PV ", " RS ", " PS " offen sc1 lds\n\ts_mov_b64 exec, -1\n\t"
DI void ypoll(uint64_t ex, unsigned pm, unsigned pv, rsrc_t r, unsigned ps) {
  asm volatile(YPOLL("%4", "%0", "%1", "%2", "%3")::"s"(pm), "v"(pv), "s"(r), "s"(ps), "s"(ex) : "memory");
}
#define YLD(D, VO, RS, SO, OFF) "buffer_load_dwordx4 " D ", " VO ", " RS ", " SO " offen offset:" OFF " sc1\n\t"
DI void yfetch(v4u (&h)[8], unsigned vo, rsrc_t r, unsigned sa, unsigned sb, unsigned sc, unsigned go, uint64_t ex, unsigned pm, unsigned pv, unsigned ps) {
  asm volatile("s_cmp_eq_u32 %12, 0\n\ts_cbranch_scc1 1f\n\t"
               YLD("%0", "%8", "%9", "%10", "0") YLD("%1", "%8", "%9", "%10", "1024") YLD("%2", "%8", "%9", "%10", "2048")
               YLD("%3", "%8", "%9", "%10", "3072") YPOLL("%17", "%13", "%14", "%9", "%15")
               YLD("%4", "%8", "%9", "%11", "0") YLD("%5", "%8", "%9", "%11", "1024") YLD("%6", "%8", "%9", "%16", "2048")
               YLD("%7", "%8", "%9", "%16", "3072") "\n1:"
               : "+v"(h[0]), "+v"(h[1]), "+v"(h[2]), "+v"(h[3]), "+v"(h[4]), "+v"(h[5]), "+v"(h[6]), "+v"(h[7])
               : "v"(vo), "s"(r), "s"(sa), "s"(sb), "s"(go), "s"(pm), "v"(pv), "s"(ps), "s"(sc), "s"(ex) : "memory", "scc");
}
DI void yfetch(v4u (&h)[3], unsigned vo, rsrc_t r, unsigned sa, unsigned, unsigned sc, unsigned go, uint64_t, unsigned, unsigned, unsigned) {
  asm volatile("s_cmp_eq_u32 %6, 0\n\ts_cbranch_scc1 1f\n\t"
               YLD("%0", "%3", "%4", "%5", "0") YLD("%1", "%3", "%4", "%5", "1024") YLD("%2", "%3", "%4", "%7", "2048") "\n1:"
               : "+v"(h[0]), "+v"(h[1]), "+v"(h[2]) : "v"(vo), "s"(r), "s"(sa), "s"(go), "s"(sc) : "memory", "scc");
}
template <int O1> DI void yst3(v4u a, v4u b, v4u c, unsigned v0, unsigned v2, rsrc_t r, unsigned so) {
  asm volatile("buffer_store_dwordx4 %0, %3, %5, %6 offen sc1\n\t"
               "buffer_store_dwordx4 %1, %3, %5, %6 offen offset:%7 sc1\n\t"
               "buffer_store_dwordx4 %2, %4, %5, %6 offen sc1\n\ts_nop 1"
               ::"v"(a), "v"(b), "v"(c), "v"(v0), "v"(v2), "s"(r), "s"(so), "n"(O1) : "memory");
}
DI unsigned eqall(unsigned v, unsigned t, unsigned lanes) {
  unsigned m;
  asm volatile("v_cmp_ne_u32 vcc, %1, %2\n\ts_and_b32 vcc_lo, vcc_lo, %3\n\ts_cselect_b32 %0, 0, -1" : "=s"(m) : "v"(v), "v"(t), "s"(lanes) : "vcc", "scc");
  return m;
}
DI float fmul1(float a, float b) { float d; asm("v_mul_f32 %0, %1, %2" : "=v"(d) : "v"(a), "v"(b)); return d; }
DI float sqs(float a) { float d; asm("v_mul_f32 %0, %1, |%1|" : "=v"(d) : "v"(a)); return d; }
DI void padd(f32x4 &a, f32x4 b) {
  f32x2 lo = a.xy, hi = a.zw;
  asm("v_pk_add_f32 %0, %0, %1" : "+v"(lo) : "v"(b.xy));
  asm("v_pk_add_f32 %0, %0, %1" : "+v"(hi) : "v"(b.zw));
  a = f32x4{lo.x, lo.y, hi.x, hi.y};
}
DI unsigned msel(unsigned m, unsigned a, unsigned b) { return m ? a : b; }
DI void sbar() { __builtin_amdgcn_sched_barrier(0); }
DI float u2f(unsigned u) { return __builtin_bit_cast(float, u); }
DI unsigned f2u(float f) { return __builtin_bit_cast(unsigned, f); }
DI float bflo(unsigned u) { return u2f(u << 16); }
DI float bfhi(unsigned u) { return u2f(u & 0xffff0000u); }
DI unsigned rfl(unsigned v) { return __builtin_amdgcn_readfirstlane(v); }
DI int lane_id() {
  int l;
  asm volatile("v_mbcnt_lo_u32_b32 %0, -1, 0\n\tv_mbcnt_hi_u32_b32 %0, -1, %0" : "=v"(l));
  return l;
}
DI unsigned pk2(float a, float b) {
  f32x2 v = {a, b};
  return __builtin_bit_cast(unsigned, __builtin_convertvector(v, bf16x2));
}
DI v2u pk4(f32x4 v) { return v2u{pk2(v[0], v[1]), pk2(v[2], v[3])}; }
DI f32x4 mfma16(v4u a, v4u b, f32x4 c) {
  return __builtin_amdgcn_mfma_f32_16x16x32_bf16(__builtin_bit_cast(bf16x8, a), __builtin_bit_cast(bf16x8, b), c, 0, 0, 0);
}
DI f32x16 mfma32(v4u a, v4u b, f32x16 c) {
  return __builtin_amdgcn_mfma_f32_32x32x16_bf16(__builtin_bit_cast(bf16x8, a), __builtin_bit_cast(bf16x8, b), c, 0, 0, 0);
}
DI float dot1(unsigned a, float c) {
  return __builtin_amdgcn_fdot2_f32_bf16(__builtin_bit_cast(bf16x2, a), __builtin_bit_cast(bf16x2, 0x3f803f80u), c, false);
}
DI float fmx(float a, float b) { return __builtin_elementwise_maximum(a, b); }
DI float half_sum(float x) {
  auto t = __builtin_amdgcn_permlane32_swap(f2u(x), f2u(x), false, false);
  return u2f(t[0]) + u2f(t[1]);
}
DI float row_sum(float x) {
  auto s = __builtin_amdgcn_permlane16_swap(f2u(x), f2u(x), false, false);
  return half_sum(u2f(s[0]) + u2f(s[1]));
}
DI float row_sum2(float a, float b) {
  auto s = __builtin_amdgcn_permlane16_swap(f2u(a), f2u(b), false, false);
  return half_sum(u2f(s[0]) + u2f(s[1]));
}
DI v4u ld128(rsrc_t r, unsigned vo, unsigned so) { return __builtin_amdgcn_raw_buffer_load_b128(r, vo, so, 0); }
DI v4u ld128c(rsrc_t r, unsigned vo, unsigned so) { return __builtin_amdgcn_raw_buffer_load_b128(r, vo, so, 16); }
DI v2u ld64c(rsrc_t r, unsigned vo, unsigned so) { return __builtin_amdgcn_raw_buffer_load_b64(r, vo, so, 16); }
DI void st128c(v4u v, rsrc_t r, unsigned vo, unsigned so) {
  asm volatile("" : "+v"(v));
  __builtin_amdgcn_raw_buffer_store_b128(v, r, vo, so, 16);
  asm volatile("s_nop 1" ::"v"(v) : "memory");
}
DI void st64c(v2u v, rsrc_t r, unsigned vo, unsigned so) { __builtin_amdgcn_raw_buffer_store_b64(v, r, vo, so, 16); }
template <int OFF> DI void ald128c(v4u &d, rsrc_t r, unsigned vo, unsigned so) {
  asm volatile("buffer_load_dwordx4 %0, %1, %2, %3 offen offset:%4 sc1" : "+v"(d) : "v"(vo), "s"(r), "s"(so), "n"(OFF) : "memory");
}
DI void ald128(v4u &d, rsrc_t r, unsigned vo, unsigned so) {
  asm volatile("buffer_load_dwordx4 %0, %1, %2, %3 offen" : "=&v"(d) : "v"(vo), "s"(r), "s"(so) : "memory");
}
DI void ald4x128c(v4u (&d)[4], rsrc_t r, unsigned vo, unsigned so) {
  asm volatile("s_nop 4\n\t"
               "buffer_load_dwordx4 %0, %4, %5, %6 offen sc1\n\t"
               "buffer_load_dwordx4 %1, %4, %5, %6 offen offset:1024 sc1\n\t"
               "buffer_load_dwordx4 %2, %4, %5, %6 offen offset:2048 sc1\n\t"
               "buffer_load_dwordx4 %3, %4, %5, %6 offen offset:3072 sc1"
               : "+v"(d[0]), "+v"(d[1]), "+v"(d[2]), "+v"(d[3]) : "v"(vo), "s"(r), "s"(so) : "memory");
}
DI void ald4x128w(v4u (&d)[4], rsrc_t r, unsigned vo, unsigned so) {
  asm volatile("s_nop 4\n\t"
               "buffer_load_dwordx4 %0, %4, %5, %6 offen sc1\n\t"
               "buffer_load_dwordx4 %1, %4, %5, %6 offen offset:1024 sc1\n\t"
               "buffer_load_dwordx4 %2, %4, %5, %6 offen offset:2048 sc1\n\t"
               "buffer_load_dwordx4 %3, %4, %5, %6 offen offset:3072 sc1\n\ts_waitcnt vmcnt(0)"
               : "=&v"(d[0]), "=&v"(d[1]), "=&v"(d[2]), "=&v"(d[3]) : "v"(vo), "s"(r), "s"(so) : "memory");
}
DI void ast128c(v4u d, rsrc_t r, unsigned vo, unsigned so) {
  asm volatile("s_nop 4\n\tbuffer_store_dwordx4 %0, %1, %2, %3 offen sc1\n\ts_nop 1" ::"v"(d), "v"(vo), "s"(r), "s"(so) : "memory");
}
DI void glds(rsrc_t r, unsigned vo, unsigned so, unsigned dst) {
  unsigned keep;
  asm volatile("s_mov_b32 %0, m0\n\ts_mov_b32 m0, %1\n\ts_nop 2\n\tbuffer_load_dwordx4 %2, %3, %4 offen lds\n\ts_mov_b32 m0, %0"
               : "=&s"(keep) : "s"(dst), "v"(vo), "s"(r), "s"(so) : "memory");
}
DI void glds4c(rsrc_t r, unsigned vo, unsigned so, unsigned dst) {
  unsigned keep;
  asm volatile("s_mov_b32 %0, m0\n\ts_mov_b32 m0, %1\n\ts_nop 2\n\t"
               "buffer_load_dwordx4 %2, %3, %4 offen sc1 lds\n\t"
               "buffer_load_dwordx4 %2, %3, %4 offen offset:1024 sc1 lds\n\t"
               "buffer_load_dwordx4 %2, %3, %4 offen offset:2048 sc1 lds\n\t"
               "buffer_load_dwordx4 %2, %3, %4 offen offset:3072 sc1 lds\n\t"
               "s_mov_b32 m0, %0"
               : "=&s"(keep) : "s"(dst), "v"(vo), "s"(r), "s"(so) : "memory");
}
DI void warm(rsrc_t r, unsigned vo, unsigned so) {
}
DI v4u lds128(unsigned a) { return *(LAS v4u *)(uintptr_t)a; }
DI f32x4 ldsf4(unsigned a) { return *(LAS f32x4 *)(uintptr_t)a; }
DI void lds_st128(unsigned a, v4u v) { *(LAS v4u *)(uintptr_t)a = v; }
DI void lds_stf4(unsigned a, f32x4 v) { *(LAS f32x4 *)(uintptr_t)a = v; }
DI void lds_st32(unsigned a, unsigned v) { *(LAS unsigned *)(uintptr_t)a = v; }
DI v2u ldstr(unsigned a) {
  return __builtin_bit_cast(v2u, __builtin_amdgcn_ds_read_tr16_b64_v4i16((LAS s16x4 *)(uintptr_t)a));
}
template <int OFF> DI void ards128(v4u &d, unsigned a) {
  asm volatile("ds_read_b128 %0, %1 offset:%2" : "+v"(d) : "v"(a), "n"(OFF) : "memory");
}
template <int OFF> DI void awds128(unsigned a, v4u d) {
  asm volatile("ds_write_b128 %0, %1 offset:%2" ::"v"(a), "v"(d), "n"(OFF) : "memory");
}
template <int OFF> DI void awds32(unsigned a, unsigned d) {
  asm volatile("ds_write_b32 %0, %1 offset:%2" ::"v"(a), "v"(d), "n"(OFF) : "memory");
}
DI unsigned sel(bool c, unsigned a, unsigned b) { return c ? a : b; }
DI unsigned umin(unsigned a, unsigned b) { return a < b ? a : b; }
DI void signal(unsigned a, unsigned v) {
  cfence();
  *(volatile LAS unsigned *)(uintptr_t)a = v;
  cfence();
}
DI void signal2(unsigned a, unsigned v) {
  cfence();
  *(volatile LAS v2u *)(uintptr_t)a = v2u{v, v};
  cfence();
}
DI unsigned peek4(unsigned a) {
  const v4u v = *(volatile LAS v4u *)(uintptr_t)a;
  return rfl(umin(umin(v.x, v.y), umin(v.z, v.w)));
}
DI void wait4(unsigned a, unsigned target, int &budget) {
  cfence();
  if (UNLIKELY(peek4(a) < target)) {
    do { --budget; __builtin_amdgcn_s_sleep(1); } while (peek4(a) < target && budget > 0);
  }
  cfence();
}
DI void wait2(unsigned a, unsigned target, int &budget) {
  cfence();
  for (;;) {
    const v2u v = *(volatile LAS v2u *)(uintptr_t)a;
    if (rfl(umin(v.x, v.y)) >= target || budget <= 0) break;
    --budget;
    __builtin_amdgcn_s_sleep(1);
  }
  cfence();
}
DI void wait1(unsigned a, unsigned target, int &budget) {
  cfence();
  if (UNLIKELY(rfl(*(volatile LAS unsigned *)(uintptr_t)a) < target)) {
    do { --budget; __builtin_amdgcn_s_sleep(1); } while (rfl(*(volatile LAS unsigned *)(uintptr_t)a) < target && budget > 0);
  }
  cfence();
}
DI v2u tg(v2u t) {
  unsigned a = t.x, b = t.y;
  asm volatile("" : "+s"(a), "+s"(b));
  return v2u{a, b};
}
template <int N> DI void wmfma0(f32x16 &d, v4u a, v4u b) {
  asm volatile("s_waitcnt lgkmcnt(%3)\n\tv_mfma_f32_32x32x16_bf16 %0, %1, %2, 0" : "=&v"(d) : "v"(a), "v"(b), "n"(N) : "memory");
}
template <int N> DI void wmfma(f32x16 &d, v4u a, v4u b) {
  asm volatile("s_waitcnt lgkmcnt(%3)\n\tv_mfma_f32_32x32x16_bf16 %0, %1, %2, %0" : "+v"(d) : "v"(a), "v"(b), "n"(N) : "memory");
}
template <int N> DI void wmfmac(f32x16 &d, v4u a, v4u b, f32x16 c) {
  asm volatile("s_waitcnt lgkmcnt(%4)\n\tv_mfma_f32_32x32x16_bf16 %0, %1, %2, %3" : "=&v"(d) : "v"(a), "v"(b), "v"(c), "n"(N) : "memory");
}
DI bool any_true(bool p) { return __builtin_amdgcn_ballot_w64(p) != 0; }
DI bool all_true(bool p) { return __builtin_amdgcn_ballot_w64(!p) == 0; }
DI void poll64(rsrc_t r, unsigned vo, unsigned so, v2u tag, int &budget) {
  for (;;) {
    cfence();
    const v2u v = ld64c(r, vo, so);
    if (all_true(v.x == tag.x && v.y == tag.y) || budget <= 0) break;
    budget -= 8;
    __builtin_amdgcn_s_sleep(2);
  }
  cfence();
}
#define TS(i)
#define TSATTR
template <int K> struct XO { static constexpr int v = K * 512; };
constexpr int m_w1(int set) { return set == 0 ? 6 : 10; }
constexpr int m_w2(int set) { return set == 0 ? 10 : 18; }
constexpr int m_extra(int set, int s) {
  s = (s + 41) % 41;
  return (s == 14) + (s == 34) + (s == 36) + (s == m_w1(set)) + 2 * (s == m_w2(set));
}
constexpr int m_nout(int set, int s) {
  return 3 + m_extra(set, s - 4) + m_extra(set, s - 3) + m_extra(set, s - 2) + m_extra(set, s - 1);
}

__global__ TSATTR void __launch_bounds__(512, 2) k_fwd(Params p) {
  const int tid = threadIdx.x;
  const int w = rfl(tid >> 6);
  const int bid = blockIdx.x;
  const bool isY = bid & 1;
  const int grp = 2 * ((bid & 7) >> 1) + (bid >> 7), rk = (bid >> 3) & 15;
  const rsrc_t rws = __builtin_amdgcn_make_buffer_rsrc(p.ws, 0, (int)WS_END, 0x00020000);
  const unsigned gb = grp * GRB;
  const unsigned row0 = (unsigned)(512 * (grp & 3) + (grp >> 2)) * 1280u;
  int budget = SPIN_CAP;
  TS(0);
  v2u e0 = {0u, 0u};
  if (w == 7) e0 = ld64c(rws, 0, OFF_E);
  if (tid < 64) *(volatile LAS unsigned *)(uintptr_t)(L_CTL + tid * 4) = (tid >= 4 && tid < 16 && (tid & 2)) ? 7u : 0u;
  bar();
  if (w == 7) {
    const unsigned lo = rfl(e0.x) + 1u;
    const unsigned hi = rfl(e0.y) + (lo == 0u ? 1u : 0u);
    *(volatile LAS v4u *)(uintptr_t)C_TAG = v4u{lo, hi, 1u, 0u};
    if ((tid & 63) == 0) st64c(v2u{lo, hi}, rws, bid * 8, OFF_ACK);
  }
  auto get_tag = [&]() AI {
    wait1(C_TAG + 8, 1u, budget);
    const v2u v = *(volatile LAS v2u *)(uintptr_t)C_TAG;
    return v2u{rfl(v.x), rfl(v.y)};
  };

  if (!isY) {
    const int c = rk, set = w >> 2, wq = w & 3;
    const int n1 = 4 * c + wq;
    const bool live = set == 0 || n1 < 54;
    const int step = c >= 14 ? 2 : 1;
    v2u tag = {0u, 0u};
    __builtin_amdgcn_s_setprio(3);
    auto load_units = [&](int og0, int og1, int v, int base, int lane) AI {
      v4u pr[4];
      asm volatile("" : "=v"(pr[0]), "=v"(pr[1]), "=v"(pr[2]), "=v"(pr[3]));
      auto rso = [&](int og) AI { return gb + OFF_REC + (v * 16 + 2 * (7 - (og & 7))) * 2048; };
      auto okP = [&]() AI {
        const unsigned a = (pr[0].y ^ tag.x) | (pr[1].y ^ tag.x) | (pr[2].y ^ tag.x) | (pr[3].y ^ tag.x);
        const unsigned b = (pr[0].w ^ tag.y) | (pr[1].w ^ tag.y) | (pr[2].w ^ tag.y) | (pr[3].w ^ tag.y);
        return all_true((a | b) == 0u) || budget <= 0;
      };
      ald4x128c(pr, rws, lane * 16, rso(og0));
      for (int og = og0; og < og1; og += 2) {
        const int u = 7 - (og & 7);
        const unsigned slot = og % 3, xr = C_R + 16 * slot, dst = slot * 40960;
        const unsigned xfa = C_XF + ((og - 3 >= base ? v : v - step) & 1) * 16;
        asm volatile("s_waitcnt vmcnt(0)" : "+v"(pr[0]), "+v"(pr[1]), "+v"(pr[2]), "+v"(pr[3])::"memory");
        while (!okP()) {
          budget -= 8;
          __builtin_amdgcn_s_sleep(1);
          ald4x128w(pr, rws, lane * 16, rso(og));
        }
        TS(34 + 2 * (8 * v + 7 - u));
        {
          if (og >= 3) wait4(xfa, og - 2, budget);
          const unsigned sx = gb + OFF_XF + (v * 16 + 2 * u) * XT;
#pragma unroll
          for (int k = 0; k < 10; ++k) {
            const unsigned o = (k & 1) * XT + (k >> 1) * 4096;
            glds4c(rws, lane * 16, sx + o, dst + o);
          }
        }
        const float S = row_sum2(u2f(pr[0].x) + u2f(pr[1].x), u2f(pr[2].x) + u2f(pr[3].x));
        auto sb = __builtin_amdgcn_permlane16_swap(f2u(S), f2u(S), false, false);
        float q[4];
#pragma unroll
        for (int k = 0; k < 4; ++k) {
          const float d = u2f(pr[k].x) * (1.f / 80.f) - u2f(sb[k >> 1]) * (1.f / 640.f);
          q[k] = u2f(pr[k].z) + 80.f * d * d;
        }
        const float Q = row_sum2(q[0] + q[1], q[2] + q[3]);
        const float nm = S * (-1.f / 640.f), var = Q * (1.f / 640.f) + 1e-5f;
        const float rstd = __builtin_amdgcn_rsqf(var), sd = var * rstd;
        const unsigned mh = pk2(nm, sd);
        lds_st128(L_HST + slot * 512 + (lane & 31) * 16,
                  v4u{pk2(nm, nm - bflo(mh)), mh, pk2(sd - bfhi(mh), 0.f), f2u(rstd) & ~0x80u});
        ald4x128c(pr, rws, lane * 16, og + 2 < og1 ? rso(og + 2) : OOB);
        vmw<20>();
        signal(xr, 2 * og + 1);
        vmw<4>();
        signal(xr, 2 * og + 2);
        TS(35 + 2 * (8 * v + 7 - u));
      }
      asm volatile("s_waitcnt vmcnt(0)" : "+v"(pr[0]), "+v"(pr[1]), "+v"(pr[2]), "+v"(pr[3])::"memory");
    };
    if (set == 1) {
      tag = get_tag();
      if (wq >= 2) load_units(wq - 2, 3, 0, 0, lane_id());
      wait1(C_WL + wq * 4, 1u, budget);
    }
    v4u wr[40], csf;
    unsigned braw;
    {
      const int lane = lane_id();
      const rsrc_t rw = set == 0 ? __builtin_amdgcn_make_buffer_rsrc((void *)p.wqkv, 0, 640 * 1920 * 2, 0x00020000)
                                 : __builtin_amdgcn_make_buffer_rsrc((void *)p.w1, 0, live ? 640 * 1728 * 2 : 0, 0x00020000);
      const rsrc_t rb = __builtin_amdgcn_make_buffer_rsrc((void *)p.b1, 0, 1728 * 2, 0x00020000);
      const unsigned pitch = set == 0 ? 3840u : 3456u;
      unsigned col;
      if (set == 0) {
        const int cc = 4 * wq + (lane & 3);
        col = cc == 15 ? OOB / 2 : 640 * (cc / 5) + 40 * c + 8 * (cc % 5);
      } else {
        col = 32 * n1 + 8 * (lane & 3);
      }
      const unsigned vo = (lane >> 2) * pitch + col * 2;
      const int i = lane & 31, q4 = (i >> 2) & 3;
      const unsigned bc = 32 * n1 + (i & 16) + 8 * (q4 & 1) + 4 * (q4 >> 1) + (i & 3);
      asm volatile("s_nop 4\n\tbuffer_load_ushort %0, %1, %2, 0 offen" : "=&v"(braw) : "v"(bc * 2), "s"(rb) : "memory");
#pragma unroll
      for (int k = 0; k < 40; ++k) ald128(wr[k], rw, vo, k * 16 * pitch);
      asm volatile("" : "=v"(csf));
    }
    if (set == 0) tag = get_tag();

    for (int ph = 0, base = 0; ph < 4; ph += step, base += 8) {
      const int layer = ph >> 1;
      const bool isM = set == (ph & 1);
      int lane = lane_id();
      if (isM && ph < 2) {
        const unsigned stg = (set == 0 ? L_HALO : L_STG) + wq * 1024;
        const int G = lane >> 4, r4 = (lane >> 2) & 3, c4 = lane & 3;
        const unsigned tra = stg + (8 * (G >> 1) + r4) * 64 + 32 * (G & 1) + 16 * (c4 & 1) + 8 * (c4 >> 1);
        float cs[4] = {0.f, 0.f, 0.f, 0.f};
        auto tr = [&](auto kc) AI {
          constexpr int k = decltype(kc)::value;
          if constexpr (k < 40) {
            vmw<39 - k>(wr[k]);
            lds_st128(stg + lane * 16, wr[k]);
            cfence();
            const v2u lo = ldstr(tra), hi = ldstr(tra + 256);
            cfence();
            wr[k] = v4u{lo.x, lo.y, hi.x, hi.y};
            if constexpr (k == WLK) signal(C_WL + wq * 4, 1u);
          }
          if constexpr (k >= 2) {
            const v4u z = wr[k - 2];
            cs[0] = dot1(z.x, cs[0]); cs[1] = dot1(z.y, cs[1]); cs[2] = dot1(z.z, cs[2]); cs[3] = dot1(z.w, cs[3]);
          }
          sbar();
        };
        static_for<42>(tr);
        asm volatile("" : "+v"(braw));
        float bias = bflo(braw);
        if (set == 0) bias = (wq == 3 && (lane & 20) == 20) ? 1.f : 0.f;
        const float c0 = half_sum((cs[0] + cs[1]) + (cs[2] + cs[3]));
        const unsigned h2 = pk2(c0, c0);
        const float cl = c0 - bflo(h2);
        csf = v4u{h2, pk2(cl, bias), pk2(bias, 0.f), 0u};
        if (lane >= 32) csf = v4u{0u, 0u, 0u, 0u};
        TS(1);
      }
      if (wq >= 2) {
        const int og0 = isM ? base : ph == 0 ? 6 - wq : base + wq - 1;
        const int og1 = isM ? (wq == 2 && ph != 0 ? base + 1 : base) : base + 8;
        if (og0 < og1) load_units(og0, og1, ph, base, lane);
      }
      if (isM) {
        auto mloop = [&](auto setc) AI {
          constexpr int SET = decltype(setc)::value;
          const unsigned xl = ((lane >> 4) & 1) * XT + (lane & 15) * 16 + (lane >> 5) * 256;
          const unsigned ql = L_QKV + (lane & 31) * QP + 64 * wq + (lane >> 5) * 16;
          const unsigned hl = L_HST + (lane & 31) * 16, dl = L_DUMMY + lane * 16;
          v4u b[5], bx, rec;
          f32x16 out;
          asm volatile("" : "=v"(b[0]), "=v"(b[1]), "=v"(b[2]), "=v"(b[3]), "=v"(b[4]), "=v"(bx), "=v"(rec), "=v"(out));
          unsigned slot = base % 3;
          wait1(C_R + 16 * slot, 2 * base + 1, budget);
          unsigned xa = xl + slot * 40960, ra = C_R + 16 * slot, qa = dl, mda = dl, mdv = 0, ha = 0;
          ards128<XO<0>::v>(b[0], xa); ards128<XO<1>::v>(b[1], xa); ards128<XO<2>::v>(b[2], xa); ards128<XO<3>::v>(b[3], xa);
          __builtin_amdgcn_s_setprio(0);
          for (unsigned og = base;; ++og) {
            const unsigned nslot = slot == 2 ? 0 : slot + 1;
            TS(2 + 2 * (8 * layer + (og & 7)));
            f32x16 acc;
            unsigned o[8], u;
            float t0, t1;
            const bool last = (og & 7) == 7;
            unsigned lim;
            auto relu2 = [&](unsigned x) AI {
              return __builtin_bit_cast(unsigned, __builtin_elementwise_max(__builtin_bit_cast(s16x2, x), s16x2{0, 0}));
            };
            auto mm = [&](auto kc) AI {
              constexpr int s = decltype(kc)::value;
              if constexpr (s == 0) wmfma0<m_nout(SET, s)>(acc, wr[0], b[0]);
              else wmfma<m_nout(SET, s)>(acc, wr[s], b[s % 5]);
              if constexpr (s <= 35) ards128<XO<(s + 4) % 40>::v>(b[(s + 4) % 5], xa);
              else if constexpr (s == 36) ards128<0>(bx, ha);
              else ards128<XO<(s + 3) % 40>::v>(b[(s + 3) % 5], xa);
              if constexpr (s == 14 || s == 34) ards128<0>(rec, ra);
              if constexpr (s == 2) asm volatile("" : "+v"(out));
              sbar();
              if constexpr (SET == 0) {
                if constexpr (s == m_w1(SET)) awds128<0>(qa, v4u{o[0], o[1], o[2], o[3]});
                if constexpr (s >= 2 && s < 10) {
                  constexpr int j = s - 2;
                  o[j] = pk2(fmul1(out[2 * j], u2f(bx.w)), fmul1(out[2 * j + 1], u2f(bx.w)));
                  asm volatile("" : "+v"(o[j]));
                }
              } else if constexpr (s >= 2 && s <= 18) {
                constexpr int j = (s - 2) >> 1;
                if constexpr ((s & 1) == 0) {
                  if constexpr (j > 0) { o[j - 1] = relu2(u); sbar(); }
                  if constexpr (j < 8) {
                    t0 = fmul1(out[2 * j], u2f(bx.w)); t1 = fmul1(out[2 * j + 1], u2f(bx.w));
                    asm volatile("" : "+v"(t0), "+v"(t1));
                  }
                  if constexpr (j > 0) asm volatile("" : "+v"(o[j - 1]));
                  if constexpr (s == m_w1(SET)) awds128<0>(qa, v4u{o[0], o[1], o[2], o[3]});
                } else {
                  u = pk2(sqs(t0), sqs(t1));
                  asm volatile("" : "+v"(u));
                }
              }
              if constexpr (s == m_w2(SET)) { awds128<32>(qa, v4u{o[4], o[5], o[6], o[7]}); awds32<0>(mda, mdv); }
              if constexpr (s == 19) {
                asm volatile("" : "+v"(rec));
                if (UNLIKELY(any_true(rec.x < 2u * og + 2u))) wait1(C_R + 16 * slot, 2 * og + 2, budget);
              }
              if constexpr (s == 21) {
                ha = hl + slot * 512; ra = C_R + 16 * nslot; mdv = og + 1;
                asm volatile("" : "+v"(ha), "+v"(ra), "+v"(mdv));
              }
              if constexpr (s == 22) {
                qa = ql + slot * QSL; mda = C_MD + 4 * w;
                asm volatile("" : "+v"(qa), "+v"(mda));
              }
              if constexpr (s == 23) {
                lim = last ? ~0u : 2u * og + 3u;
                asm volatile("" : "+s"(lim));
              }
              if constexpr (s == 36) {
                awds32<32>(mda, mdv);
                xa = xl + nslot * 40960;
                asm volatile("" : "+v"(xa));
              }
              sbar();
            };
            static_for<40>(mm);
            asm volatile("" : "+v"(rec));
            uint64_t slow = __builtin_amdgcn_ballot_w64(umin(rec.x, umin(rec.z, rec.w)) < lim);
            asm volatile("" : "+s"(slow));
            TS(3 + 2 * (8 * layer + (og & 7)));
            wmfmac<m_nout(SET, 40)>(out, csf, bx, acc);
            if (UNLIKELY(slow != 0)) {
              asm volatile("s_nop 7\n\ts_nop 7\n\ts_nop 3" : "+v"(out) : "v"(acc));
              wait2(C_R + 16 * nslot + 8, 2 * og + 3, budget);
              const float rs = u2f(bx.w);
#pragma unroll
              for (int j = 0; j < 8; ++j) {
                float a = out[2 * j] * rs, bb = out[2 * j + 1] * rs;
                if (SET == 1) { a *= __builtin_fabsf(a); bb *= __builtin_fabsf(bb); }
                o[j] = pk2(a, bb);
                if (SET == 1) o[j] = relu2(o[j]);
              }
              awds128<0>(qa, v4u{o[0], o[1], o[2], o[3]});
              awds128<32>(qa, v4u{o[4], o[5], o[6], o[7]});
              awds32<0>(mda, mdv);
              if (last) break;
              qa = dl; mda = dl;
              wait1(C_R + 16 * nslot, 2 * og + 3, budget);
              ards128<XO<0>::v>(b[0], xa); ards128<XO<1>::v>(b[1], xa); ards128<XO<2>::v>(b[2], xa);
            }
            ards128<XO<3>::v>(b[3], xa);
            slot = nslot;
          }
          __builtin_amdgcn_s_setprio(3);
          asm volatile("s_waitcnt lgkmcnt(0)" : "+v"(b[0]), "+v"(b[1]), "+v"(b[2])::"memory");
        };
        if (!live) {
          signal(C_MD + w * 4, base + 8);
          signal(C_XF + w * 4, base + 8);
        } else if (set == 0) {
          mloop(IC<0>{});
        } else {
          mloop(IC<1>{});
        }
        if (bid == 120 && w == 3 && ph == 0) {
          for (;;) {
            cfence();
            bool ok = true;
#pragma unroll
            for (int k = 0; k < 2; ++k) {
              const v4u a = ld128c(rws, lane * 16 + k * 1024, OFF_ACK);
              ok = ok && a.x == tag.x && a.y == tag.y && a.z == tag.x && a.w == tag.y;
            }
            if (all_true(ok)) { if (lane == 0) st64c(tg(tag), rws, 0, OFF_E); break; }
            if (budget <= 0) break;
            budget -= 8;
            __builtin_amdgcn_s_sleep(2);
          }
        }
      }
      if (wq >= 2) continue;
      lane = lane_id();
      if (ph & 1) {
        if (isM) continue;
        const unsigned ra = L_QKV + (lane & 15) * QP + (lane >> 4) * 16;
        const unsigned v0 = lane * 16, v1 = c == 13 ? OOB : v0;
        const unsigned vw = c == 13 && (lane & 16) ? OOB : (lane >> 5) * HT + (lane & 31) * 128;
        for (int i = wq; i < 8; i += 2) {
          const unsigned og = base + i, slot = og % 3, u = 7 - i;
          wait4(C_MD + 16, og + 1, budget);
          TS(66 + 2 * (8 * layer + i));
          v4u d[8];
#pragma unroll
          for (int k = 0; k < 8; ++k) d[k] = lds128(ra + slot * QSL + (k >> 2) * 16 * QP + (k & 3) * 64);
          signal2(C_R + 16 * ((og + 1) % 3) + 8, 2 * og + 9);
          const unsigned so = gb + OFF_ACT + (ph * 16 + 2 * u) * HT + c * 4096;
#pragma unroll
          for (int k = 0; k < 8; ++k) st128c(d[k], rws, ((k & 3) < 2 ? v0 : v1) + (k & 3) * 1024, so + (k >> 2) * HT);
          drain();
          warm(rws, vw, so);
          if (lane < 2) st64c(tg(tag), rws, lane * 2048, gb + OFF_FL + (ph * 16 + 2 * u) * 2048 + c * 128);
          TS(67 + 2 * (8 * layer + i));
        }
      } else {
        const int g = lane >> 4, t = lane & 15, c4 = lane & 3;
        const bool hi = wq == 0;
        const bool has_halo = (grp & 3) != 0, give_halo = (grp & 3) != 3;
        const int m0 = set ? 0 : 7 - wq, m1 = set ? 7 - wq : 8;
        f32x4 cP, cQ;
#pragma unroll
        for (int r = 0; r < 4; ++r) {
          const int j = 4 * g + r;
          cP[r] = j >= t + 9 ? 0.f : -INFINITY;
          cQ[r] = (j <= t && j >= t - 7) ? 0.f : -INFINITY;
        }
        const unsigned mp0 = (4 * g > t ? 0xffffu : 0u) | (4 * g + 1 > t ? 0xffff0000u : 0u);
        const unsigned mp1 = (4 * g + 2 > t ? 0xffffu : 0u) | (4 * g + 3 > t ? 0xffff0000u : 0u);
        const unsigned mqa = g == 2 ? 0u : ~0u, mqb = g == 0 ? 0u : ~0u;
        const unsigned kl = t * QP + 16 * g;
        const unsigned vrow = (4 * g + ((lane >> 2) & 3)) * QP;
        const unsigned v0 = vrow + 160 + c4 * 8, v1a = vrow + (c4 ? 240 : 192), v1b = vrow + (c4 ? 240 : 232);
        const unsigned so1 = lane < 16 ? 4096 + (c & 3) * 256 + lane * 16 : OOB;
        const unsigned vw = lane < 8 ? (c & 3) * 1024 + lane * 128 : lane < 10 ? 4096 - 1024 + (c & 3) * 256 + lane * 128 : OOB;
        unsigned pso = OOB;
        bool pend = false;
        unsigned pfl = OOB;
        for (int m = m0; m < m1; ++m) {
          const unsigned og = base + m, u = 7 - m;
          const unsigned need = og + (hi || m == 7 ? 1 : 2);
          if (pend && (m == 7 || peek4(C_MD) < need)) { drain(); warm(rws, vw, pso); st64c(tg(tag), rws, pfl, 0); pend = false; }
          if (!hi && m == 7) {
            if (has_halo) {
              const unsigned id = layer * 16 + c;
              poll64(rws, 0, gb - GRB + OFF_KVF + id * 128, tag, budget);
              v4u d[4];
#pragma unroll
              for (int k = 0; k < 4; ++k) d[k] = ld128c(rws, lane * 16 + k * 1024, gb - GRB + OFF_KV + id * 4096);
#pragma unroll
              for (int k = 0; k < 4; ++k) lds_st128(L_HALO + (4 * k + g) * QP + t * 16, d[k]);
            } else {
              cP = f32x4{-INFINITY, -INFINITY, -INFINITY, -INFINITY};
            }
          }
          wait4(C_MD, need, budget);
          TS(34 + 2 * (8 * layer + m));
          unsigned rc, rp;
          if (hi) { rp = L_QKV + (og % 3) * QSL; rc = rp + 16 * QP; }
          else {
            rc = L_QKV + (og % 3) * QSL;
            rp = m < 7 ? L_QKV + ((og + 1) % 3) * QSL + 16 * QP : (has_halo ? (unsigned)L_HALO : rc);
          }
          const unsigned ac = rc + kl, ap = rp + kl, aq = g == 3 ? (unsigned)C_ZERO : ac;
          v4u qa = lds128(aq), kpa = lds128(ap + 80), kca = lds128(ac + 80);
          v4u qb = lds128(aq + 32), kpb = lds128(ap + 112), kcb = lds128(ac + 112);
          v2u vp[4], vc[4];
          vp[0] = ldstr(rp + v0); vc[0] = ldstr(rc + v0); vp[1] = ldstr(rp + v1a); vc[1] = ldstr(rc + v1a);
          vp[2] = ldstr(rp + v0 + 40); vc[2] = ldstr(rc + v0 + 40); vp[3] = ldstr(rp + v1b); vc[3] = ldstr(rc + v1b);
          qa.z &= mqa; qa.w &= mqa; qb.x &= mqb; qb.y &= mqb;
          const f32x4 spa = mfma16(kpa, qa, cP), sqa = mfma16(kca, qa, cQ);
          const f32x4 spb = mfma16(kpb, qb, cP), sqb = mfma16(kcb, qb, cQ);
          f32x4 sa, sb;
#pragma unroll
          for (int r = 0; r < 4; ++r) { sa[r] = fmx(spa[r], sqa[r]); sb[r] = fmx(spb[r], sqb[r]); }
          const float sc = 0.22360679774997896f * 1.4426950408889634f;
          float na, nb;
          {
            const float xa = fmx(fmx(sa[0], sa[1]), fmx(sa[2], sa[3])), xb = fmx(fmx(sb[0], sb[1]), fmx(sb[2], sb[3]));
            auto s1 = __builtin_amdgcn_permlane16_swap(f2u(xa), f2u(xb), false, false);
            const float y = fmx(u2f(s1[0]), u2f(s1[1]));
            auto s2 = __builtin_amdgcn_permlane32_swap(f2u(y), f2u(y), false, false);
            const float z = fmx(u2f(s2[0]), u2f(s2[1])) * -sc;
            auto s3 = __builtin_amdgcn_permlane16_swap(f2u(z), f2u(z), false, false);
            na = u2f(s3[0]); nb = u2f(s3[1]);
          }
#pragma unroll
          for (int r = 0; r < 4; ++r) {
            sa[r] = __builtin_amdgcn_exp2f(__builtin_fmaf(sa[r], sc, na));
            sb[r] = __builtin_amdgcn_exp2f(__builtin_fmaf(sb[r], sc, nb));
          }
          const v2u pa = pk4(sa), pb = pk4(sb);
          const unsigned a0 = pa.x & mp0, a1 = pa.y & mp1, b0 = pb.x & mp0, b1 = pb.y & mp1;
          const v4u opa = {a0, a1, pa.x ^ a0, pa.y ^ a1}, opb = {b0, b1, pb.x ^ b0, pb.y ^ b1};
          const f32x4 z4 = {0.f, 0.f, 0.f, 0.f};
          const f32x4 oa1 = mfma16(v4u{vp[1].x, vp[1].y, vc[1].x, vc[1].y}, opa, z4);
          const f32x4 ob1 = mfma16(v4u{vp[3].x, vp[3].y, vc[3].x, vc[3].y}, opb, z4);
          const f32x4 oa0 = mfma16(v4u{vp[0].x, vp[0].y, vc[0].x, vc[0].y}, opa, z4);
          const f32x4 ob0 = mfma16(v4u{vp[2].x, vp[2].y, vc[2].x, vc[2].y}, opb, z4);
          auto la = __builtin_amdgcn_permlane16_swap(f2u(oa1[0]), f2u(oa1[0]), false, false);
          auto lb = __builtin_amdgcn_permlane16_swap(f2u(ob1[0]), f2u(ob1[0]), false, false);
          const float ia = __builtin_amdgcn_rcpf(u2f(la[1])), ib = __builtin_amdgcn_rcpf(u2f(lb[1]));
          const v2u e0 = pk4(oa0 * ia), e1 = pk4(ob0 * ib), e2 = pk4(oa1 * ia), e3 = pk4(ob1 * ib);
          const unsigned T = ph * 16 + 2 * u + (hi ? 1 : 0);
          const unsigned so = gb + OFF_ACT + T * HT + 5 * (c >> 2) * 1024;
          st128c(v4u{e0.x, e0.y, e1.x, e1.y}, rws, lane * 16 + (c & 3) * 1024, so);
          st128c(v4u{e2.x, e2.y, e3.x, e3.y}, rws, so1, so);
          if (pend) { vmw<2>(); warm(rws, vw, pso); st64c(tg(tag), rws, pfl, 0); }
          pend = true;
          pso = so;
          pfl = lane == 0 ? gb + OFF_FL + T * 2048 + c * 128 : OOB;
          if (hi && m == 0 && give_halo) {
            const unsigned id = layer * 16 + c;
#pragma unroll
            for (int k = 0; k < 4; ++k) st128c(lds128(rc + (4 * k + g) * QP + t * 16), rws, lane * 16 + k * 1024, gb + OFF_KV + id * 4096);
            if (lane == 1) pfl = gb + OFF_KVF + id * 128;
          }
          if (!hi && m == 6) wait1(C_R + 16 * (og % 3) + 12, 2 * og + 7, budget);
          signal(C_R + 16 * ((og + 1) % 3) + 8 + wq * 4, 2 * og + 9);
          TS(35 + 2 * (8 * layer + m));
        }
        drain();
        warm(rws, vw, pso);
        st64c(tg(tag), rws, pfl, 0);
      }
    }
  } else {
    const unsigned ct = rk & 7, sy = rk >> 3;
    const unsigned lane = tid & 63, g = lane >> 4, hv = lane * 16;
    if (w == 7) {
      __builtin_amdgcn_s_setprio(3);
      f32x32 res[5];
      float recS, recQ;
      auto stats = [&](const f32x4 (&y)[5]) AI {
        const f32x4 s4 = ((y[0] + y[1]) + (y[2] + y[3])) + y[4];
        recS = row_sum((s4[0] + s4[1]) + (s4[2] + s4[3]));
        const float mu = recS * (1.f / 80.f);
        f32x4 q4 = {0.f, 0.f, 0.f, 0.f};
#pragma unroll
        for (int j = 0; j < 5; ++j) { const f32x4 d = y[j] - mu; q4 += d * d; }
        recQ = row_sum((q4[0] + q4[1]) + (q4[2] + q4[3]));
      };
      auto third = [&](v2u c) AI {
        auto sx = __builtin_amdgcn_permlane16_swap(c.x, c.x, false, false);
        auto sw = __builtin_amdgcn_permlane16_swap(c.y, c.y, false, false);
        return v4u{sx[0], sw[0], sx[1], sw[1]};
      };
      const unsigned odd = (hv & 256) << 22, v2 = 2048 + (hv & 240) + ((hv >> 9) << 8) + odd, rv = hv < 256 ? hv : OOB;
      const unsigned xm = (hv & 240) * 160;
      unsigned xo = gb + OFF_XF + (14 + sy) * XT + ct * 2560, ro = gb + OFF_REC + (14 + sy) * 2048 + ct * 256;
      {
        const rsrc_t rx0 = __builtin_amdgcn_make_buffer_rsrc((void *)p.x, 0, 2048 * 1280, 0x00020000);
        v4u a[8], b[8];
        v2u c[8];
#pragma unroll
        for (int i = 0; i < 8; ++i) {
          const unsigned xs = row0 + (14 - 2 * i + sy) * 40960 + ct * 160;
          a[i] = ld128(rx0, xm + g * 16, xs); b[i] = ld128(rx0, xm + g * 16 + 64, xs);
          c[i] = __builtin_amdgcn_raw_buffer_load_b64(rx0, xm + g * 8 + 128, xs, 0);
        }
        sbar();
#pragma unroll
        for (int i = 0; i < 8; ++i) {
          st128c(a[i], rws, hv, xo - 2 * i * XT); st128c(b[i], rws, hv + 1024, xo - 2 * i * XT); st128c(third(c[i]), rws, v2, xo - 2 * i * XT);
        }
        signal(C_GO, 1u);
#pragma unroll
        for (int i = 0; i < 8; ++i) {
#pragma unroll
          for (int k = 0; k < 4; ++k) {
            const int e = 4 * i + 2 * (k & 1);
            res[k >> 1][e] = bflo(a[i][k]); res[k >> 1][e + 1] = bfhi(a[i][k]);
            res[2 + (k >> 1)][e] = bflo(b[i][k]); res[2 + (k >> 1)][e + 1] = bfhi(b[i][k]);
            if (k < 2) { res[4][e] = bflo(c[i][k]); res[4][e + 1] = bfhi(c[i][k]); }
          }
        }
      }
      const v2u tag = get_tag();
      const unsigned vw = lane < 20 ? lane * 128 : OOB;
      auto rec_store = [&](unsigned so) AI {
        warm(rws, vw, xo + (so - ro) * 10);
        const v2u tv = tg(tag);
        ast128c(v4u{f2u(recS), tv.x, f2u(recQ), tv.y}, rws, rv, so);
      };
      TS(1);
#pragma nounroll
      for (unsigned i = 0; i < 8; ++i) {
        f32x4 y[5];
        unsigned i4 = 4 * i;
        asm volatile("" : "+s"(i4));
        i4 &= 28;
#pragma unroll
        for (int k = 0; k < 20; ++k) y[k >> 2][k & 3] = res[k >> 2][i4 + (k & 3)];
        stats(y);
        if (i == 0) drain();
        rec_store(ro - i * 4096);
      }
      TS(2);
      signal(C_PF + 28, ~0u);
      *(volatile LAS v2u *)(uintptr_t)(L_RDY + tid * 8) = v2u{~tag.x, ~tag.y};
      bar();
      TS(3);
      bool pend = false;
      unsigned recA = 0;
      unsigned pa = L_PART + hv;
      asm volatile("" : "+v"(pa));
      pa &= 0x3ffff;
#pragma nounroll
      for (unsigned r = 0; r < 32; ++r) {
        const unsigned i = r & 7, n = ((2 * r) ^ 14) + 2;
        f32x4 y[5], A[5], B[5], C[5];
        unsigned i4 = 4 * i;
        asm volatile("" : "+s"(i4));
        i4 &= 28;
#pragma unroll
        for (int k = 0; k < 20; ++k) y[k >> 2][k & 3] = res[k >> 2][i4 + (k & 3)];
        asm volatile("" : "+v"(y[0]), "+v"(y[1]), "+v"(y[2]), "+v"(y[3]), "+v"(y[4]));
        auto rd = [&](f32x4 (&o)[5], int q) AI {
#pragma unroll
          for (int j = 0; j < 5; ++j) o[j] = ldsf4(pa + q * PSZ + j * 1024);
          sbar();
        };
        auto add = [&](const f32x4 (&o)[5]) AI {
#pragma unroll
          for (int j = 0; j < 5; ++j) padd(y[j], o[j]);
          sbar();
        };
        cfence();
        unsigned lo = peek4(C_PF), hi = peek4(C_PF + 16);
        if (UNLIKELY(lo <= r)) {
          if (pend) { drain(); rec_store(recA); pend = false; }
          wait4(C_PF, r + 1, budget);
          hi = peek4(C_PF + 16);
        }
        cfence();
        TS(4 + 3 * r);
        rd(A, 0); rd(B, 1); rd(C, 2);
        add(A); rd(A, 3); add(B);
        if (UNLIKELY(hi <= r)) {
          if (pend) { drain(); rec_store(recA); pend = false; }
          wait4(C_PF + 16, r + 1, budget);
        }
        cfence();
        rd(B, 4); add(C); rd(C, 5); add(A); rd(A, 6);
        signal(C_FD, r + 1);
        add(B); add(C); add(A);
        if (pend) { drain(); rec_store(recA); pend = false; }
        const v2u e0 = pk4(y[0]), e1 = pk4(y[1]), e2 = pk4(y[2]), e3 = pk4(y[3]);
        const v4u d0 = {e0.x, e0.y, e1.x, e1.y}, d1 = {e2.x, e2.y, e3.x, e3.y}, d2 = third(pk4(y[4]));
        if (r >= 24) {
          unsigned l = hv;
          asm volatile("" : "+v"(l));
          const unsigned xl = (l & 240) * 160;
          yst3<64>(d0, d1, d2, xl + ((l >> 8) << 4), xl + 128 + ((l >> 9) << 4) + ((l & 256) << 22),
                   __builtin_amdgcn_make_buffer_rsrc(p.out, 0, 2048 * 1280, 0x00020000), row0 + (14 - 2 * i + sy) * 40960 + ct * 160);
        } else {
          yst3<1024>(d0, d1, d2, hv, v2, rws, xo + n * XT);
          TS(5 + 3 * r);
          asm volatile("" : "+s"(i4));
          i4 &= 28;
#pragma unroll
          for (int k = 0; k < 20; ++k) res[k >> 2][i4 + (k & 3)] = y[k >> 2][k & 3];
          stats(y);
          recA = ro + n * 2048;
          pend = true;
        }
        TS(6 + 3 * r);
      }
      drain();
    } else {
      const unsigned r4 = (lane >> 2) & 3, c4 = lane & 3;
      v4u w2[8][5];
      auto fbase = [&](unsigned row) AI {
        v2u a = {row + c4 * 16, row + c4 * 8};
        asm volatile("" : "+v"(a.x), "+v"(a.y));
        return a;
      };
      auto frag = [&](v2u b, int j, unsigned hi) AI {
        const unsigned a = j < 4 ? b.x + (j & 1) * 8 + (j >> 1) * 64 : b.y + 128;
        const v2u lo = ldstr(a), up = ldstr(a + hi);
        return v4u{lo.x, lo.y, up.x, up.y};
      };
      const bool bias = w == 6;
      const unsigned ones = gb + OFF_ONE + rk * 2048;
      v2u tag;
      {
        unsigned vrow[5];
#pragma unroll
        for (int m = 0; m < 5; ++m) { const int idx = m * 64 + lane; vrow[m] = (idx / 10) * 1280 + (idx % 10) * 16 + 160 * ct; }
        {
          const rsrc_t rw = __builtin_amdgcn_make_buffer_rsrc((void *)p.wo, 0, 640 * 640 * 2, 0x00020000);
          for (unsigned blk = w; blk < 20; blk += 7) {
#pragma unroll
            for (int m = 0; m < 5; ++m) glds(rw, vrow[m], blk * 40960, blk * 5120 + m * 1024);
          }
          const v4u one = {0x3f803f80u, 0x3f803f80u, 0x3f803f80u, 0x3f803f80u};
          ast128c(one, rws, hv, bias ? ones : OOB);
          ast128c(one, rws, hv + 1024, bias ? ones : OOB);
        }
        tag = get_tag();
        *(volatile LAS v2u *)(uintptr_t)(L_RDY + tid * 8) = v2u{~tag.x, ~tag.y};
        wait1(C_GO, 1u, budget);
        TS(1);
        const rsrc_t rw = __builtin_amdgcn_make_buffer_rsrc((void *)p.w2, 0, 1728 * 640 * 2, 0x00020000);
        const rsrc_t rw6 = __builtin_amdgcn_make_buffer_rsrc((void *)(bias ? p.b2 : p.w2), 0, bias ? 640 * 2 : 1728 * 640 * 2, 0x00020000);
        const unsigned s0 = 8 * w * 40960;
#pragma unroll
        for (int k = 0; k < 8; ++k)
#pragma unroll
          for (int m = 0; m < 5; ++m)
            w2[k][m] = k == 6 ? ld128(rw6, vrow[m], bias ? 0u : s0 + 6 * 40960) : ld128(rw, vrow[m], s0 + k * 40960);
        sbar();
        const unsigned stg = L_PART + w * PSZ;
        const v2u tra = fbase(stg + (8 * g + r4) * 160);
#pragma unroll
        for (int k = 0; k < 8; ++k) {
#pragma unroll
          for (int m = 0; m < 5; ++m) lds_st128(stg + m * 1024 + hv, w2[k][m]);
          cfence();
#pragma unroll
          for (int j = 0; j < 5; ++j) w2[k][j] = frag(tra, j, 640);
          cfence();
          sbar();
        }
      }
      TS(2);
      drain();
      bar();
      TS(3);
      if (w < 4) __builtin_amdgcn_s_setprio(1);
      const unsigned pvo = (lane >> 1) * 128 + (lane & 1) * 4, tagv = lane & 1 ? tag.y : tag.x, ck = (lane & 31) * 4;
      const unsigned pw = L_PART + w * PSZ + hv;
      auto phase = [&](auto pc, unsigned r0) AI {
        constexpr bool P5 = decltype(pc)::value;
        constexpr int K = P5 ? 8 : 3, N = P5 ? 9 : 3;
        const unsigned q = w & 3, n0 = 2 * r0 + 14 + sy;
        const unsigned lanes = P5 ? 15u << 4 * w : w < 3 ? 63u << 8 * w : (255u << 8 * q) | (3u << (24 + 2 * q));
        const uint64_t ex = lanes;
        unsigned fl = rfl(gb + OFF_FL + n0 * 2048);
        unsigned ac = gb + OFF_ACT + n0 * HT + (P5 ? 8 * w : 5 * q + (w < 3 ? 0 : 3)) * 1024;
        unsigned pm = L_RDY + r0 * 128;
        const unsigned d3 = w < 3 ? 0u : w == 3 ? OOB : (10 - 4 * q) * 1024;
        auto last = [&](unsigned a) AI { return P5 ? (bias ? ones - 2048 : a + 4096) : a + d3; };
        auto ready = [&](unsigned a) AI { return eqall(*(volatile LAS unsigned *)(uintptr_t)(ck + a), tagv, lanes); };
        v4u h[K], wo[10];
#pragma unroll
        for (int k = 0; k < K; ++k) asm volatile("" : "=v"(h[k]));
        v2u wr0 = {0u, 0u};
        if constexpr (!P5) {
          wr0 = fbase((160 * q + (w < 3 ? 0 : 120) + 4 * g + r4) * 160);
          const v2u wr1 = fbase((160 * q + (w < 3 ? 40 + 4 * g : 16 + 40 * g) + r4) * 160);
          const v2u wr2 = fbase(((w < 3 ? 160 * q + 80 : 480 + 40 * q) + 4 * g + r4) * 160);
#pragma unroll
          for (int j = 0; j < 5; ++j) { wo[j] = frag(wr1, j, 3200); wo[5 + j] = frag(wr2, j, 3200); }
        }
        unsigned have = 0;
        for (unsigned r = r0; r < r0 + 8; ++r, fl -= 4096, ac -= 2 * HT, pm += 128) {
          const unsigned pm1 = r < r0 + 7 ? pm + 128 : L_DUMMY, pm2 = r < r0 + 6 ? pm + 256 : L_DUMMY;
          f32x4 acc[5];
          if constexpr (!P5) {
#pragma unroll
            for (int j = 0; j < 5; ++j) acc[j] = __builtin_bit_cast(f32x4, frag(wr0, j, 3200));
          }
          if (!have) {
            while (!ready(pm) && budget > 0) {
              ypoll(ex, pm, pvo, rws, fl);
              budget -= 2;
              __builtin_amdgcn_s_sleep(2);
            }
          }
          yfetch(h, hv, rws, ac, ac + 4096, last(ac), ~have, ex, pm1, pvo, fl - 4096);
          const unsigned nxt = ready(pm + 128) & (r < r0 + 7 ? ~0u : 0u);
          const unsigned qm = msel(nxt, pm2, pm1), qf = fl - 4096 - (nxt & 4096);
          ypoll(ex, qm, pvo, rws, qf);
          const unsigned so = msel(nxt, ac - 2 * HT, OOB), sl = msel(nxt, last(ac - 2 * HT), OOB);
          const uint64_t em = ((uint64_t)nxt << 32) | (nxt | 1u);
          if constexpr (P5) {
            ygrp<N, 0, true>(acc, w2[0], h[0], hv, rws, so, em);
            TS(4 + 3 * r);
            ygrp<N, 1024, false>(acc, w2[1], h[1], hv, rws, so, em);
            ygrp<N, 2048, false>(acc, w2[2], h[2], hv, rws, so, em);
            ygrp<N, 3072, false>(acc, w2[3], h[3], hv, rws, so, em);
            ypoll(ex, qm, pvo, rws, qf);
            ygrp<N, 0, false>(acc, w2[4], h[K - 4], hv, rws, so + 4096, em);
            ygrp<N, 1024, false>(acc, w2[5], h[K - 3], hv, rws, so + 4096, em);
            ygrp<N, 2048, false>(acc, w2[6], h[K - 2], hv, rws, sl, em);
          } else {
            ygrp_in<N>(acc, h[0], hv, rws, so, em);
            TS(4 + 3 * r);
            ygrp<N, 1024, false>(acc, wo, h[1], hv, rws, so, em);
          }
          const unsigned fd = *(volatile LAS unsigned *)(uintptr_t)C_FD;
          ygrp<N, P5 ? 3072 : 2048, false>(acc, P5 ? w2[7] : wo + 5, h[K - 1], hv, rws, sl, em);
          have = nxt;
          if (UNLIKELY(rfl(fd) < r)) wait1(C_FD, r, budget);
          mfin(acc);
          awds5(pw, acc);
          signal(C_PF + 4 * w, r + 1);
          TS(5 + 3 * r);
        }
#pragma unroll
        for (int k = 0; k < K; ++k) vmw<0>(h[k]);
      };
#pragma nounroll
      for (unsigned r0 = 0; r0 < 32; r0 += 16) {
        phase(IC<0>{}, r0);
        phase(IC<1>{}, r0 + 8);
      }
    }
  }
  drain();
}

extern "C" void kernel_launch(void *const *d_in, const int *in_sizes, int n_in, void *d_out, int out_size, void *d_ws,
                              size_t ws_size, hipStream_t stream) {
  Params p;
  p.x = d_in[0]; p.wqkv = d_in[1]; p.wo = d_in[2]; p.w1 = d_in[4]; p.b1 = d_in[5]; p.w2 = d_in[6]; p.b2 = d_in[7];
  p.out = d_out; p.ws = d_ws;
  (void)hipFuncSetAttribute((const void *)k_fwd, hipFuncAttributeMaxDynamicSharedMemorySize, L_TOTAL);
  k_fwd<<<256, 512, L_TOTAL, stream>>>(p);
}
```

```cpp
#include <hip/hip_runtime.h>
#include <stdint.h>

typedef unsigned v4u __attribute__((ext_vector_type(4)));
typedef unsigned v2u __attribute__((ext_vector_type(2)));
typedef float f32x4 __attribute__((ext_vector_type(4)));
typedef float f32x2 __attribute__((ext_vector_type(2)));
typedef float f32x32 __attribute__((ext_vector_type(32)));
typedef float f32x16 __attribute__((ext_vector_type(16)));
typedef __bf16 bf16x8 __attribute__((ext_vector_type(8)));
typedef __bf16 bf16x2 __attribute__((ext_vector_type(2)));
typedef short s16x4 __attribute__((ext_vector_type(4)));
typedef short s16x2 __attribute__((ext_vector_type(2)));
typedef __amdgpu_buffer_rsrc_t rsrc_t;
#define LAS __attribute__((address_space(3)))
#define AI __attribute__((always_inline))
#define DI __device__ __forceinline__
#define UNLIKELY(x) __builtin_expect(!!(x), 0)
#define WLK 28
#define SPLITX 1
#define XMAP 2
#define WARM 0
template <int V> struct IC { static constexpr int value = V; };
template <int N, int I = 0, class F> DI void static_for(F &f) {
  if constexpr (I < N) { f(IC<I>{}); static_for<N, I + 1>(f); }
}

constexpr unsigned XT = 20480, HT = 55296, GRB = 8u << 20;
constexpr unsigned OFF_FL = 8192;
constexpr unsigned OFF_KVF = OFF_FL + 64 * 2048;
constexpr unsigned OFF_REC = OFF_KVF + 2 * 2048;
constexpr unsigned OFF_KV = OFF_REC + 64 * 2048;
constexpr unsigned OFF_ONE = OFF_KV + 2 * 16 * 4096;
constexpr unsigned OFF_XF = OFF_ONE + 16 * 2048;
constexpr unsigned OFF_ACT = OFF_XF + 64 * XT;
constexpr unsigned OFF_E = GRB - 8192, OFF_ACK = GRB - 4096;
static_assert(OFF_ACT + 64 * HT + 2048 <= OFF_E, "ws map");
constexpr unsigned OFF_W0 = 8 * GRB, OFF_W1 = OFF_W0 + 80 * 1920 * 16, OFF_W2 = OFF_W1 + 80 * 1728 * 16;
constexpr unsigned OFF_W3 = OFF_W2 + 55 * 40960;
constexpr unsigned OFF_WF = OFF_W3 + 20 * 40960;
constexpr unsigned WS_END = OFF_WF + 77 * 512;
constexpr unsigned OOB = 0x40000000u;
constexpr unsigned L_QKV = 122880, QP = 272, QSL = 32 * QP, L_HALO = L_QKV + 3 * QSL, L_HST = L_HALO + 16 * QP;
constexpr unsigned L_STG = 155648;
constexpr unsigned L_PART = 102400, PSZ = 5120, L_RDY = L_PART + 8 * PSZ;
constexpr unsigned L_CTL = 159744, L_TS = 160256, L_DUMMY = 162304, L_TOTAL = 163840;
constexpr unsigned C_R = L_CTL + 16, C_MD = L_CTL + 64, C_XF = L_CTL + 96, C_ZERO = L_CTL + 128, C_WL = L_CTL + 176;
constexpr unsigned C_PF = L_CTL + 64, C_FD = L_CTL + 96, C_GO = L_CTL + 100;
static_assert(L_HST + 3 * 512 <= L_STG, "lds map");
constexpr int SPIN_CAP = 1 << 18;

struct Params {
  const void *x, *wqkv, *wo, *w1, *b1, *w2, *b2;
  void *out, *ws;
};

DI void cfence() { asm volatile("" ::: "memory"); }
DI void bar() { asm volatile("s_waitcnt lgkmcnt(0)\n\ts_barrier" ::: "memory"); }
DI void drain() { asm volatile("s_waitcnt vmcnt(0)" ::: "memory"); }
template <int N> DI void vmw() { asm volatile("s_waitcnt vmcnt(%0)" ::"n"(N) : "memory"); }
template <int N> DI void vmw(v4u &d) { asm volatile("s_waitcnt vmcnt(%1)" : "+v"(d) : "n"(N) : "memory"); }
#define MF5(C0, C1, C2, C3, C4)                                                                             \
  "v_mfma_f32_16x16x32_bf16 %0, %6, %5, " C0 "\n\tv_mfma_f32_16x16x32_bf16 %1, %7, %5, " C1 "\n\t"           \
  "v_mfma_f32_16x16x32_bf16 %2, %8, %5, " C2 "\n\tv_mfma_f32_16x16x32_bf16 %3, %9, %5, " C3 "\n\t"           \
  "v_mfma_f32_16x16x32_bf16 %4, %10, %5, " C4 "\n\t"
#define RELOAD "s_mov_b64 exec, %14\n\tbuffer_load_dwordx4 %5, %11, %12, %13 offen offset:%16 sc1\n\ts_mov_b64 exec, -1"
template <int N, int OFF, bool FIRST> DI void ygrp(f32x4 (&a)[5], const v4u *w, v4u &h, unsigned vo, rsrc_t r, unsigned so, uint64_t m) {
  if constexpr (FIRST) {
    asm volatile("s_waitcnt vmcnt(%15)\n\t" MF5("0", "0", "0", "0", "0") RELOAD
                 : "=&v"(a[0]), "=&v"(a[1]), "=&v"(a[2]), "=&v"(a[3]), "=&v"(a[4]), "+v"(h)
                 : "v"(w[0]), "v"(w[1]), "v"(w[2]), "v"(w[3]), "v"(w[4]), "v"(vo), "s"(r), "s"(so), "s"(m), "n"(N), "n"(OFF) : "memory");
  } else {
    asm volatile("s_waitcnt vmcnt(%15)\n\t" MF5("%0", "%1", "%2", "%3", "%4") RELOAD
                 : "+v"(a[0]), "+v"(a[1]), "+v"(a[2]), "+v"(a[3]), "+v"(a[4]), "+v"(h)
                 : "v"(w[0]), "v"(w[1]), "v"(w[2]), "v"(w[3]), "v"(w[4]), "v"(vo), "s"(r), "s"(so), "s"(m), "n"(N), "n"(OFF) : "memory");
  }
}
template <int N> DI void ygrp_in(f32x4 (&a)[5], v4u &h, unsigned vo, rsrc_t r, unsigned so, uint64_t m) {
  asm volatile("s_waitcnt vmcnt(%10)\n\t"
               "v_mfma_f32_16x16x32_bf16 %0, %0, %5, 0\n\tv_mfma_f32_16x16x32_bf16 %1, %1, %5, 0\n\t"
               "v_mfma_f32_16x16x32_bf16 %2, %2, %5, 0\n\tv_mfma_f32_16x16x32_bf16 %3, %3, %5, 0\n\t"
               "v_mfma_f32_16x16x32_bf16 %4, %4, %5, 0\n\t"
               "s_mov_b64 exec, %9\n\tbuffer_load_dwordx4 %5, %6, %7, %8 offen sc1\n\ts_mov_b64 exec, -1"
               : "+v"(a[0]), "+v"(a[1]), "+v"(a[2]), "+v"(a[3]), "+v"(a[4]), "+v"(h)
               : "v"(vo), "s"(r), "s"(so), "s"(m), "n"(N) : "memory");
}
DI void mfin(f32x4 (&a)[5]) {
  asm volatile("s_nop 7" : "+v"(a[0]), "+v"(a[1]), "+v"(a[2]), "+v"(a[3]), "+v"(a[4])::"memory");
}
DI void awds5(unsigned p, f32x4 (&a)[5]) {
  asm volatile("ds_write_b128 %0, %1\n\tds_write_b128 %0, %2 offset:1024\n\tds_write_b128 %0, %3 offset:2048\n\t"
               "ds_write_b128 %0, %4 offset:3072\n\tds_write_b128 %0, %5 offset:4096"
               ::"v"(p), "v"(a[0]), "v"(a[1]), "v"(a[2]), "v"(a[3]), "v"(a[4]) : "memory");
}
#define YPOLL(EX, PM, PV, RS, PS) \
  "s_mov_b32 m0, " PM "\n\ts_mov_b64 exec, " EX "\n\tbuffer_load_dword " PV ", " RS ", " PS " offen sc1 lds\n\ts_mov_b64 exec, -1\n\t"
DI void ypoll(uint64_t ex, unsigned pm, unsigned pv, rsrc_t r, unsigned ps) {
  asm volatile(YPOLL("%4", "%0", "%1", "%2", "%3")::"s"(pm), "v"(pv), "s"(r), "s"(ps), "s"(ex) : "memory");
}
#define YLD(D, VO, RS, SO, OFF) "buffer_load_dwordx4 " D ", " VO ", " RS ", " SO " offen offset:" OFF " sc1\n\t"
DI void yfetch(v4u (&h)[8], unsigned vo, rsrc_t r, unsigned sa, unsigned sb, unsigned sc, unsigned go, uint64_t ex, unsigned pm, unsigned pv, unsigned ps) {
  asm volatile("s_cmp_eq_u32 %12, 0\n\ts_cbranch_scc1 1f\n\t"
               YLD("%0", "%8", "%9", "%10", "0") YLD("%1", "%8", "%9", "%10", "1024") YLD("%2", "%8", "%9", "%10", "2048")
               YLD("%3", "%8", "%9", "%10", "3072") YPOLL("%17", "%13", "%14", "%9", "%15")
               YLD("%4", "%8", "%9", "%11", "0") YLD("%5", "%8", "%9", "%11", "1024") YLD("%6", "%8", "%9", "%16", "2048")
               YLD("%7", "%8", "%9", "%16", "3072") "\n1:"
               : "+v"(h[0]), "+v"(h[1]), "+v"(h[2]), "+v"(h[3]), "+v"(h[4]), "+v"(h[5]), "+v"(h[6]), "+v"(h[7])
               : "v"(vo), "s"(r), "s"(sa), "s"(sb), "s"(go), "s"(pm), "v"(pv), "s"(ps), "s"(sc), "s"(ex) : "memory", "scc");
}
DI void yfetch(v4u (&h)[3], unsigned vo, rsrc_t r, unsigned sa, unsigned, unsigned sc, unsigned go, uint64_t, unsigned, unsigned, unsigned) {
  asm volatile("s_cmp_eq_u32 %6, 0\n\ts_cbranch_scc1 1f\n\t"
               YLD("%0", "%3", "%4", "%5", "0") YLD("%1", "%3", "%4", "%5", "1024") YLD("%2", "%3", "%4", "%7", "2048") "\n1:"
               : "+v"(h[0]), "+v"(h[1]), "+v"(h[2]) : "v"(vo), "s"(r), "s"(sa), "s"(go), "s"(sc) : "memory", "scc");
}
template <int O1> DI void yst3(v4u a, v4u b, v4u c, unsigned v0, unsigned v2, rsrc_t r, unsigned so) {
  asm volatile("buffer_store_dwordx4 %0, %3, %5, %6 offen sc1\n\t"
               "buffer_store_dwordx4 %1, %3, %5, %6 offen offset:%7 sc1\n\t"
               "buffer_store_dwordx4 %2, %4, %5, %6 offen sc1\n\ts_nop 1"
               ::"v"(a), "v"(b), "v"(c), "v"(v0), "v"(v2), "s"(r), "s"(so), "n"(O1) : "memory");
}
DI unsigned eqall(unsigned v, unsigned t, unsigned lanes) {
  unsigned m;
  asm volatile("v_cmp_ne_u32 vcc, %1, %2\n\ts_and_b32 vcc_lo, vcc_lo, %3\n\ts_cselect_b32 %0, 0, -1" : "=s"(m) : "v"(v), "v"(t), "s"(lanes) : "vcc", "scc");
  return m;
}
DI float fmul1(float a, float b) { float d; asm("v_mul_f32 %0, %1, %2" : "=v"(d) : "v"(a), "v"(b)); return d; }
DI float sqs(float a) { float d; asm("v_mul_f32 %0, %1, |%1|" : "=v"(d) : "v"(a)); return d; }
DI void padd(f32x4 &a, f32x4 b) {
  f32x2 lo = a.xy, hi = a.zw;
  asm("v_pk_add_f32 %0, %0, %1" : "+v"(lo) : "v"(b.xy));
  asm("v_pk_add_f32 %0, %0, %1" : "+v"(hi) : "v"(b.zw));
  a = f32x4{lo.x, lo.y, hi.x, hi.y};
}
DI unsigned msel(unsigned m, unsigned a, unsigned b) { return m ? a : b; }
DI void sbar() { __builtin_amdgcn_sched_barrier(0); }
DI float u2f(unsigned u) { return __builtin_bit_cast(float, u); }
DI unsigned f2u(float f) { return __builtin_bit_cast(unsigned, f); }
DI float bflo(unsigned u) { return u2f(u << 16); }
DI float bfhi(unsigned u) { return u2f(u & 0xffff0000u); }
DI unsigned rfl(unsigned v) { return __builtin_amdgcn_readfirstlane(v); }
DI int lane_id() {
  int l;
  asm volatile("v_mbcnt_lo_u32_b32 %0, -1, 0\n\tv_mbcnt_hi_u32_b32 %0, -1, %0" : "=v"(l));
  return l;
}
DI unsigned pk2(float a, float b) {
  f32x2 v = {a, b};
  return __builtin_bit_cast(unsigned, __builtin_convertvector(v, bf16x2));
}
DI v2u pk4(f32x4 v) { return v2u{pk2(v[0], v[1]), pk2(v[2], v[3])}; }
DI f32x4 mfma16(v4u a, v4u b, f32x4 c) {
  return __builtin_amdgcn_mfma_f32_16x16x32_bf16(__builtin_bit_cast(bf16x8, a), __builtin_bit_cast(bf16x8, b), c, 0, 0, 0);
}
DI f32x16 mfma32(v4u a, v4u b, f32x16 c) {
  return __builtin_amdgcn_mfma_f32_32x32x16_bf16(__builtin_bit_cast(bf16x8, a), __builtin_bit_cast(bf16x8, b), c, 0, 0, 0);
}
DI float dot1(unsigned a, float c) {
  return __builtin_amdgcn_fdot2_f32_bf16(__builtin_bit_cast(bf16x2, a), __builtin_bit_cast(bf16x2, 0x3f803f80u), c, false);
}
DI float fmx(float a, float b) { return __builtin_elementwise_maximum(a, b); }
DI float half_sum(float x) {
  auto t = __builtin_amdgcn_permlane32_swap(f2u(x), f2u(x), false, false);
  return u2f(t[0]) + u2f(t[1]);
}
DI float row_sum(float x) {
  auto s = __builtin_amdgcn_permlane16_swap(f2u(x), f2u(x), false, false);
  return half_sum(u2f(s[0]) + u2f(s[1]));
}
DI float row_sum2(float a, float b) {
  auto s = __builtin_amdgcn_permlane16_swap(f2u(a), f2u(b), false, false);
  return half_sum(u2f(s[0]) + u2f(s[1]));
}
DI v4u ld128(rsrc_t r, unsigned vo, unsigned so) { return __builtin_amdgcn_raw_buffer_load_b128(r, vo, so, 0); }
DI v4u ld128c(rsrc_t r, unsigned vo, unsigned so) { return __builtin_amdgcn_raw_buffer_load_b128(r, vo, so, 16); }
DI v2u ld64c(rsrc_t r, unsigned vo, unsigned so) { return __builtin_amdgcn_raw_buffer_load_b64(r, vo, so, 16); }
DI void st128c(v4u v, rsrc_t r, unsigned vo, unsigned so) {
  asm volatile("" : "+v"(v));
  __builtin_amdgcn_raw_buffer_store_b128(v, r, vo, so, 16);
  asm volatile("s_nop 1" ::"v"(v) : "memory");
}
DI void st64c(v2u v, rsrc_t r, unsigned vo, unsigned so) { __builtin_amdgcn_raw_buffer_store_b64(v, r, vo, so, 16); }
template <int OFF> DI void ald128c(v4u &d, rsrc_t r, unsigned vo, unsigned so) {
  asm volatile("buffer_load_dwordx4 %0, %1, %2, %3 offen offset:%4 sc1" : "+v"(d) : "v"(vo), "s"(r), "s"(so), "n"(OFF) : "memory");
}
DI void ald128(v4u &d, rsrc_t r, unsigned vo, unsigned so) {
  asm volatile("buffer_load_dwordx4 %0, %1, %2, %3 offen" : "=&v"(d) : "v"(vo), "s"(r), "s"(so) : "memory");
}
template <int OFF> DI void ald128n(v4u &d, rsrc_t r, unsigned vo, unsigned so) {
  asm volatile("buffer_load_dwordx4 %0, %1, %2, %3 offen offset:%4 sc1" : "=&v"(d) : "v"(vo), "s"(r), "s"(so), "n"(OFF) : "memory");
}
DI void ald4x128c(v4u (&d)[4], rsrc_t r, unsigned vo, unsigned so) {
  asm volatile("s_nop 4\n\t"
               "buffer_load_dwordx4 %0, %4, %5, %6 offen sc1\n\t"
               "buffer_load_dwordx4 %1, %4, %5, %6 offen offset:1024 sc1\n\t"
               "buffer_load_dwordx4 %2, %4, %5, %6 offen offset:2048 sc1\n\t"
               "buffer_load_dwordx4 %3, %4, %5, %6 offen offset:3072 sc1"
               : "+v"(d[0]), "+v"(d[1]), "+v"(d[2]), "+v"(d[3]) : "v"(vo), "s"(r), "s"(so) : "memory");
}
DI void ald4x128w(v4u (&d)[4], rsrc_t r, unsigned vo, unsigned so) {
  asm volatile("s_nop 4\n\t"
               "buffer_load_dwordx4 %0, %4, %5, %6 offen sc1\n\t"
               "buffer_load_dwordx4 %1, %4, %5, %6 offen offset:1024 sc1\n\t"
               "buffer_load_dwordx4 %2, %4, %5, %6 offen offset:2048 sc1\n\t"
               "buffer_load_dwordx4 %3, %4, %5, %6 offen offset:3072 sc1\n\ts_waitcnt vmcnt(0)"
               : "=&v"(d[0]), "=&v"(d[1]), "=&v"(d[2]), "=&v"(d[3]) : "v"(vo), "s"(r), "s"(so) : "memory");
}
DI void ast128c(v4u d, rsrc_t r, unsigned vo, unsigned so) {
  asm volatile("s_nop 4\n\tbuffer_store_dwordx4 %0, %1, %2, %3 offen sc1\n\ts_nop 1" ::"v"(d), "v"(vo), "s"(r), "s"(so) : "memory");
}
DI void glds(rsrc_t r, unsigned vo, unsigned so, unsigned dst) {
  unsigned keep;
  asm volatile("s_mov_b32 %0, m0\n\ts_mov_b32 m0, %1\n\ts_nop 2\n\tbuffer_load_dwordx4 %2, %3, %4 offen sc1 lds\n\ts_mov_b32 m0, %0"
               : "=&s"(keep) : "s"(dst), "v"(vo), "s"(r), "s"(so) : "memory");
}
DI void glds4c(rsrc_t r, unsigned vo, unsigned so, unsigned dst) {
  unsigned keep;
  asm volatile("s_mov_b32 %0, m0\n\ts_mov_b32 m0, %1\n\ts_nop 2\n\t"
               "buffer_load_dwordx4 %2, %3, %4 offen sc1 lds\n\t"
               "buffer_load_dwordx4 %2, %3, %4 offen offset:1024 sc1 lds\n\t"
               "buffer_load_dwordx4 %2, %3, %4 offen offset:2048 sc1 lds\n\t"
               "buffer_load_dwordx4 %2, %3, %4 offen offset:3072 sc1 lds\n\t"
               "s_mov_b32 m0, %0"
               : "=&s"(keep) : "s"(dst), "v"(vo), "s"(r), "s"(so) : "memory");
}
DI void warm(rsrc_t r, unsigned vo, unsigned so) {
}
DI v4u lds128(unsigned a) { return *(LAS v4u *)(uintptr_t)a; }
DI f32x4 ldsf4(unsigned a) { return *(LAS f32x4 *)(uintptr_t)a; }
DI void lds_st128(unsigned a, v4u v) { *(LAS v4u *)(uintptr_t)a = v; }
DI void lds_stf4(unsigned a, f32x4 v) { *(LAS f32x4 *)(uintptr_t)a = v; }
DI void lds_st32(unsigned a, unsigned v) { *(LAS unsigned *)(uintptr_t)a = v; }
DI v2u ldstr(unsigned a) {
  return __builtin_bit_cast(v2u, __builtin_amdgcn_ds_read_tr16_b64_v4i16((LAS s16x4 *)(uintptr_t)a));
}
template <int OFF> DI void ards128(v4u &d, unsigned a) {
  asm volatile("ds_read_b128 %0, %1 offset:%2" : "+v"(d) : "v"(a), "n"(OFF) : "memory");
}
template <int OFF> DI void awds128(unsigned a, v4u d) {
  asm volatile("ds_write_b128 %0, %1 offset:%2" ::"v"(a), "v"(d), "n"(OFF) : "memory");
}
template <int OFF> DI void awds32(unsigned a, unsigned d) {
  asm volatile("ds_write_b32 %0, %1 offset:%2" ::"v"(a), "v"(d), "n"(OFF) : "memory");
}
DI unsigned sel(bool c, unsigned a, unsigned b) { return c ? a : b; }
DI unsigned umin(unsigned a, unsigned b) { return a < b ? a : b; }
DI void signal(unsigned a, unsigned v) {
  cfence();
  *(volatile LAS unsigned *)(uintptr_t)a = v;
  cfence();
}
DI void signal2(unsigned a, unsigned v) {
  cfence();
  *(volatile LAS v2u *)(uintptr_t)a = v2u{v, v};
  cfence();
}
DI unsigned peek4(unsigned a) {
  const v4u v = *(volatile LAS v4u *)(uintptr_t)a;
  return rfl(umin(umin(v.x, v.y), umin(v.z, v.w)));
}
DI void wait4(unsigned a, unsigned target, int &budget) {
  cfence();
  if (UNLIKELY(peek4(a) < target)) {
    do { --budget; __builtin_amdgcn_s_sleep(1); } while (peek4(a) < target && budget > 0);
  }
  cfence();
}
DI void wait2(unsigned a, unsigned target, int &budget) {
  cfence();
  for (;;) {
    const v2u v = *(volatile LAS v2u *)(uintptr_t)a;
    if (rfl(umin(v.x, v.y)) >= target || budget <= 0) break;
    --budget;
    __builtin_amdgcn_s_sleep(1);
  }
  cfence();
}
DI void wait1(unsigned a, unsigned target, int &budget) {
  cfence();
  if (UNLIKELY(rfl(*(volatile LAS unsigned *)(uintptr_t)a) < target)) {
    do { --budget; __builtin_amdgcn_s_sleep(1); } while (rfl(*(volatile LAS unsigned *)(uintptr_t)a) < target && budget > 0);
  }
  cfence();
}
DI v2u tg(v2u t) {
  unsigned a = t.x, b = t.y;
  asm volatile("" : "+s"(a), "+s"(b));
  return v2u{a, b};
}
template <int N> DI void wmfma0(f32x16 &d, v4u a, v4u b) {
  asm volatile("s_waitcnt lgkmcnt(%3)\n\tv_mfma_f32_32x32x16_bf16 %0, %1, %2, 0" : "=&v"(d) : "v"(a), "v"(b), "n"(N) : "memory");
}
template <int N> DI void wmfma(f32x16 &d, v4u a, v4u b) {
  asm volatile("s_waitcnt lgkmcnt(%3)\n\tv_mfma_f32_32x32x16_bf16 %0, %1, %2, %0" : "+v"(d) : "v"(a), "v"(b), "n"(N) : "memory");
}
template <int N> DI void wmfmac(f32x16 &d, v4u a, v4u b, f32x16 c) {
  asm volatile("s_waitcnt lgkmcnt(%4)\n\tv_mfma_f32_32x32x16_bf16 %0, %1, %2, %3" : "=&v"(d) : "v"(a), "v"(b), "v"(c), "n"(N) : "memory");
}
DI bool any_true(bool p) { return __builtin_amdgcn_ballot_w64(p) != 0; }
DI bool all_true(bool p) { return __builtin_amdgcn_ballot_w64(!p) == 0; }
DI void poll64(rsrc_t r, unsigned vo, unsigned so, v2u tag, int &budget) {
  for (;;) {
    cfence();
    const v2u v = ld64c(r, vo, so);
    if (all_true(v.x == tag.x && v.y == tag.y) || budget <= 0) break;
    budget -= 8;
    __builtin_amdgcn_s_sleep(2);
  }
  cfence();
}
#define TS(i)
#define TSATTR
template <int K> struct XO { static constexpr int v = K * 512; };
constexpr int m_w1(int set) { return set == 0 ? 6 : 10; }
constexpr int m_w2(int set) { return set == 0 ? 10 : 18; }
constexpr int m_extra(int set, int s) {
  s = (s + 41) % 41;
  return (s == 14) + (s == 34) + (s == 36) + (s == m_w1(set)) + 2 * (s == m_w2(set));
}
constexpr int m_nout(int set, int s) {
  return 3 + m_extra(set, s - 4) + m_extra(set, s - 3) + m_extra(set, s - 2) + m_extra(set, s - 1);
}

__global__ TSATTR void __launch_bounds__(512, 2) k_fwd(Params p) {
  const int tid = threadIdx.x;
  const int w = rfl(tid >> 6);
  const int bid = blockIdx.x;
  asm volatile("" ::"s"(p.x), "s"(p.wqkv), "s"(p.wo), "s"(p.w1), "s"(p.b1), "s"(p.w2), "s"(p.b2), "s"(p.out), "s"(p.ws));
  const bool isY = bid & 1;
  const int xj = (bid >> 1) & 3, xm_ = bid >> 3;
  const int grp = isY ? 2 * xj + (xm_ >> 4) : xm_ >> 2, rk = isY ? xm_ & 15 : 4 * xj + (xm_ & 3);
  const rsrc_t rws = __builtin_amdgcn_make_buffer_rsrc(p.ws, 0, (int)WS_END, 0x00020000);
  const unsigned gb = grp * GRB;
  const unsigned row0 = (unsigned)(512 * (grp & 3) + (grp >> 2)) * 1280u;
  int budget = SPIN_CAP;
  TS(0);
  const v2u e0 = ld64c(rws, 0, OFF_E);
  if (tid < 64) *(volatile LAS unsigned *)(uintptr_t)(L_CTL + tid * 4) = (tid >= 4 && tid < 16 && (tid & 2)) ? 7u : 0u;
  v2u tag;
  float recS, recQ;
  auto stats = [&](const f32x4 (&y)[5]) AI {
    const f32x4 s4 = ((y[0] + y[1]) + (y[2] + y[3])) + y[4];
    recS = row_sum((s4[0] + s4[1]) + (s4[2] + s4[3]));
    const float mu = recS * (1.f / 80.f);
    f32x4 q4 = {0.f, 0.f, 0.f, 0.f};
#pragma unroll
    for (int j = 0; j < 5; ++j) { const f32x4 d = y[j] - mu; q4 += d * d; }
    recQ = row_sum((q4[0] + q4[1]) + (q4[2] + q4[3]));
  };
  auto third = [&](v2u c) AI {
    auto sx = __builtin_amdgcn_permlane16_swap(c.x, c.x, false, false);
    auto sw = __builtin_amdgcn_permlane16_swap(c.y, c.y, false, false);
    return v4u{sx[0], sw[0], sx[1], sw[1]};
  };
  auto rec_store = [&](unsigned vo, unsigned so) AI {
    const v2u tv = tg(tag);
    ast128c(v4u{f2u(recS), tv.x, f2u(recQ), tv.y}, rws, vo, so);
  };
  const unsigned ct = rk & 7, sy = rk >> 3;
  const unsigned xo = gb + OFF_XF + (14 + sy) * XT + ct * 2560, ro = gb + OFF_REC + (14 + sy) * 2048 + ct * 256;
  const rsrc_t rx0 = __builtin_amdgcn_make_buffer_rsrc((void *)p.x, 0, 2048 * 1280, 0x00020000);
  {
    const unsigned lane = tid & 63, g = lane >> 4, hv = lane * 16;
    v4u a, b;
    v2u c;
    if (isY) {
      const unsigned xm = (hv & 240) * 160, xs = row0 + (14 - 2 * w + sy) * 40960 + ct * 160;
      a = ld128(rx0, xm + g * 16, xs); b = ld128(rx0, xm + g * 16 + 64, xs);
      c = __builtin_amdgcn_raw_buffer_load_b64(rx0, xm + g * 8 + 128, xs, 0);
      const unsigned v2 = 2048 + (hv & 240) + ((hv >> 9) << 8) + ((hv & 256) << 22);
      st128c(a, rws, hv, xo - 2 * w * XT); st128c(b, rws, hv + 1024, xo - 2 * w * XT); st128c(third(c), rws, v2, xo - 2 * w * XT);
      TS(101);
    }
    const unsigned t = w * 128 + grp * 16 + rk;
    const unsigned mt = !isY ? (t < 600 ? 0 : 1) : t < 200 ? 3 : t < 316 ? 1 : t < 866 ? 2 : 4;
    const unsigned tn = !isY ? (t < 600 ? t : t - 600) : t < 200 ? t : t < 316 ? t + 224 : t - 316;
    const unsigned ncb = mt == 0 ? 30 : mt == 1 ? 27 : 10, kb = mt == 0 ? tn / 30 : mt == 1 ? tn / 27 : tn / 10, cb = tn - kb * ncb;
    const bool isb = kb == 54, prm = mt == 3;
    const unsigned pitch = ncb * 128, q5 = kb / 5, i5 = kb - 5 * q5;
    const rsrc_t rw = __builtin_amdgcn_make_buffer_rsrc((void *)(mt == 0 ? p.wqkv : mt == 1 ? p.w1 : prm ? p.wo : isb ? p.b2 : p.w2), 0,
                                                        mt == 4 ? 0 : (mt == 2 ? (isb ? 1 : 1728) : 640) * pitch, 0x00020000);
    const unsigned rb = prm ? 160 * q5 + (i5 < 4 ? 40 * i5 : 16) : isb ? 0 : 32 * kb, rs = prm ? (i5 < 4 ? 4 : 40) : 8;
    const unsigned l3 = lane >> 3, vo = (prm ? (l3 & 3) + 20 * (l3 >> 2) : l3) * pitch + cb * 128 + (lane & 7) * 16;
    v4u d[4];
#pragma unroll
    for (int n = 0; n < 4; ++n) d[n] = ld128(rw, vo, (rb + n * rs) * pitch);
    sbar();
    tag.x = rfl(e0.x) + 1u;
    tag.y = rfl(e0.y) + (tag.x == 0u ? 1u : 0u);
    if (isY) {
      const f32x4 y[5] = {{bflo(a.x), bfhi(a.x), bflo(a.y), bfhi(a.y)}, {bflo(a.z), bfhi(a.z), bflo(a.w), bfhi(a.w)},
                          {bflo(b.x), bfhi(b.x), bflo(b.y), bfhi(b.y)}, {bflo(b.z), bfhi(b.z), bflo(b.w), bfhi(b.w)},
                          {bflo(c.x), bfhi(c.x), bflo(c.y), bfhi(c.y)}};
      stats(y);
      *(volatile LAS v2u *)(uintptr_t)(L_RDY + tid * 8) = v2u{~tag.x, ~tag.y};
      vmw<4>();
      rec_store(hv < 256 ? hv : OOB, ro - w * 4096);
      TS(105);
    }
    const unsigned stg = isY ? L_PART + w * PSZ : w < 7 ? L_QKV + w * 4096 : L_STG;
#pragma unroll
    for (int n = 0; n < 4; ++n) lds_st128(stg + n * 1024 + hv, d[n]);
    cfence();
    TS(102);
    const unsigned col = cb * 64 + lane, c80 = col / 80, r = col - 80 * c80;
    const unsigned pos = mt < 2 || r >= 64 ? col : 80 * c80 + (r & 32) + 4 * (r & 4) + ((r >> 1) & 12) + (r & 3);
    const unsigned tra = stg + ((lane >> 2) & 3) * 128 + g * 32 + (lane & 3) * 8;
    const unsigned wo = mt == 4 ? OOB : (mt == 0 ? OFF_W0 : mt == 1 ? OFF_W1 : mt == 2 ? OFF_W2 : OFF_W3) + kb * pitch * 32;
#pragma unroll
    for (int n = 0; n < 4; ++n) {
      const v2u lo = ldstr(tra + n * 1024), up = ldstr(tra + n * 1024 + 512);
      st128c(v4u{lo.x, lo.y, up.x, up.y}, rws, pos * 16, wo + n * pitch * 8);
    }
    TS(103);
    bar();
    drain();
    const unsigned fo = OFF_WF + ((mt == 0 ? 0 : mt == 1 ? 30 : mt == 2 ? 57 : 67) + cb) * 512 + kb * 8;
    st64c(tg(tag), rws, lane == 0 && mt != 4 ? fo : lane == 1 && w == 7 ? OFF_ACK + bid * 8 : OOB, 0);
    TS(1);
  }

  if (!isY) {
    const int c = rk, set = w >> 2, wq = w & 3;
    const int n1 = 4 * c + wq;
    const bool live = set == 0 || n1 < 54;
    const int step = c >= 14 ? 2 : 1;
    __builtin_amdgcn_s_setprio(3);
    auto load_units = [&](int og0, int og1, int v, int base, int lane) AI {
      v4u pr[4];
      asm volatile("" : "=v"(pr[0]), "=v"(pr[1]), "=v"(pr[2]), "=v"(pr[3]));
      auto rso = [&](int og) AI { return gb + OFF_REC + (v * 16 + 2 * (7 - (og & 7))) * 2048; };
      auto okP = [&]() AI {
        const unsigned a = (pr[0].y ^ tag.x) | (pr[1].y ^ tag.x) | (pr[2].y ^ tag.x) | (pr[3].y ^ tag.x);
        const unsigned b = (pr[0].w ^ tag.y) | (pr[1].w ^ tag.y) | (pr[2].w ^ tag.y) | (pr[3].w ^ tag.y);
        return all_true((a | b) == 0u) || budget <= 0;
      };
      ald4x128c(pr, rws, lane * 16, rso(og0));
      for (int og = og0; og < og1; og += 2) {
        const int u = 7 - (og & 7);
        const unsigned slot = og % 3, xr = C_R + 16 * slot, dst = slot * 40960;
        const unsigned xfa = C_XF + ((og - 3 >= base ? v : v - step) & 1) * 16;
        asm volatile("s_waitcnt vmcnt(0)" : "+v"(pr[0]), "+v"(pr[1]), "+v"(pr[2]), "+v"(pr[3])::"memory");
        while (!okP()) {
          budget -= 8;
          __builtin_amdgcn_s_sleep(1);
          ald4x128w(pr, rws, lane * 16, rso(og));
        }
        TS(34 + 2 * (8 * v + 7 - u));
        {
          if (og >= 3) wait4(xfa, og - 2, budget);
          const unsigned sx = gb + OFF_XF + (v * 16 + 2 * u) * XT;
#pragma unroll
          for (int k = 0; k < 10; ++k) {
            const unsigned o = (k & 1) * XT + (k >> 1) * 4096;
            glds4c(rws, lane * 16, sx + o, dst + o);
          }
        }
        const float S = row_sum2(u2f(pr[0].x) + u2f(pr[1].x), u2f(pr[2].x) + u2f(pr[3].x));
        auto sb = __builtin_amdgcn_permlane16_swap(f2u(S), f2u(S), false, false);
        float q[4];
#pragma unroll
        for (int k = 0; k < 4; ++k) {
          const float d = u2f(pr[k].x) * (1.f / 80.f) - u2f(sb[k >> 1]) * (1.f / 640.f);
          q[k] = u2f(pr[k].z) + 80.f * d * d;
        }
        const float Q = row_sum2(q[0] + q[1], q[2] + q[3]);
        const float nm = S * (-1.f / 640.f), var = Q * (1.f / 640.f) + 1e-5f;
        const float rstd = __builtin_amdgcn_rsqf(var), sd = var * rstd;
        const unsigned mh = pk2(nm, sd);
        lds_st128(L_HST + slot * 512 + (lane & 31) * 16,
                  v4u{pk2(nm, nm - bflo(mh)), mh, pk2(sd - bfhi(mh), 0.f), f2u(rstd) & ~0x80u});
        ald4x128c(pr, rws, lane * 16, og + 2 < og1 ? rso(og + 2) : OOB);
        vmw<20>();
        signal(xr, 2 * og + 1);
        vmw<4>();
        signal(xr, 2 * og + 2);
        TS(35 + 2 * (8 * v + 7 - u));
      }
      asm volatile("s_waitcnt vmcnt(0)" : "+v"(pr[0]), "+v"(pr[1]), "+v"(pr[2]), "+v"(pr[3])::"memory");
    };
    v4u wr[40], csf;
    unsigned braw;
    if (set == 1) {
      if (wq == 3) wait1(C_WL + 12, 1u, budget);
      if (wq >= 2) load_units(wq - 2, 3, 0, 0, lane_id());
      wait1(C_WL + wq * 4, 2u, budget);
    }
    {
      const int lane = lane_id();
      const unsigned nc = set == 0 ? 1920u : 1728u;
      auto colof = [&](int cw) AI {
        const int cc = 4 * wq + (cw >> 3);
        return set == 0 ? 640 * (cc / 5) + 40 * c + 8 * (cc % 5) + (cw & 7) : 32 * n1 + cw;
      };
      if (live) {
        const unsigned fo = umin(colof(8 * (lane >> 4)) >> 6, 29u) * 512 + umin(lane & 15, 9) * 16;
        for (;;) {
          cfence();
          const v4u f = ld128c(rws, fo, OFF_WF + set * 30 * 512);
          if (all_true(f.x == tag.x && f.y == tag.y && f.z == tag.x && f.w == tag.y) || budget <= 0) break;
          budget -= 8;
          __builtin_amdgcn_s_sleep(1);
        }
        cfence();
      }
      TS(98);
      const int i = lane & 31, q4 = (i >> 2) & 3;
      const unsigned col = colof((i & 16) + 8 * (q4 & 1) + 4 * (q4 >> 1) + (i & 3));
      const unsigned vo = col < nc ? ((lane >> 5) * nc + col) * 16 : OOB;
      const rsrc_t rb = __builtin_amdgcn_make_buffer_rsrc((void *)p.b1, 0, 1728 * 2, 0x00020000);
      asm volatile("s_nop 4\n\tbuffer_load_ushort %0, %1, %2, 0 offen" : "=&v"(braw) : "v"(col * 2), "s"(rb) : "memory");
      unsigned so = set == 0 ? OFF_W0 : OFF_W1;
#pragma unroll
      for (int k = 0; k < 40; ++k, so += nc * 32) ald128n<0>(wr[k], rws, vo, so);
      if (set == 0) signal(C_WL + wq * 4, 1u);
      asm volatile("" : "=v"(csf));
    }

    for (int ph = 0, base = 0; ph < 4; ph += step, base += 8) {
      const int layer = ph >> 1;
      const bool isM = set == (ph & 1);
      int lane = lane_id();
      if (isM && ph < 2) {
        float cs[4] = {0.f, 0.f, 0.f, 0.f};
        auto tr = [&](auto kc) AI {
          constexpr int k = decltype(kc)::value;
          vmw<39 - k>(wr[k]);
          if constexpr (k == WLK) signal(C_WL + wq * 4, 2u);
          const v4u z = wr[k];
          cs[0] = dot1(z.x, cs[0]); cs[1] = dot1(z.y, cs[1]); cs[2] = dot1(z.z, cs[2]); cs[3] = dot1(z.w, cs[3]);
          sbar();
        };
        static_for<40>(tr);
        asm volatile("" : "+v"(braw));
        float bias = bflo(braw);
        if (set == 0) bias = (wq == 3 && (lane & 20) == 20) ? 1.f : 0.f;
        const float c0 = half_sum((cs[0] + cs[1]) + (cs[2] + cs[3]));
        const unsigned h2 = pk2(c0, c0);
        const float cl = c0 - bflo(h2);
        csf = v4u{h2, pk2(cl, bias), pk2(bias, 0.f), 0u};
        if (lane >= 32) csf = v4u{0u, 0u, 0u, 0u};
        TS(99);
      }
      if (wq >= 2) {
        const int og0 = isM ? base : ph == 0 ? 6 - wq : base + wq - 1;
        const int og1 = isM ? (wq == 2 && ph != 0 ? base + 1 : base) : base + 8;
        if (og0 < og1) load_units(og0, og1, ph, base, lane);
      }
      if (isM) {
        auto mloop = [&](auto setc) AI {
          constexpr int SET = decltype(setc)::value;
          const unsigned xl = ((lane >> 4) & 1) * XT + (lane & 15) * 16 + (lane >> 5) * 256;
          const unsigned ql = L_QKV + (lane & 31) * QP + 64 * wq + (lane >> 5) * 16;
          const unsigned hl = L_HST + (lane & 31) * 16, dl = L_DUMMY + lane * 16;
          v4u b[5], bx, rec;
          f32x16 out;
          asm volatile("" : "=v"(b[0]), "=v"(b[1]), "=v"(b[2]), "=v"(b[3]), "=v"(b[4]), "=v"(bx), "=v"(rec), "=v"(out));
          unsigned slot = base % 3;
          wait1(C_R + 16 * slot, 2 * base + 1, budget);
          unsigned xa = xl + slot * 40960, ra = C_R + 16 * slot, qa = dl, mda = dl, mdv = 0, ha = 0;
          ards128<XO<0>::v>(b[0], xa); ards128<XO<1>::v>(b[1], xa); ards128<XO<2>::v>(b[2], xa); ards128<XO<3>::v>(b[3], xa);
          __builtin_amdgcn_s_setprio(0);
          for (unsigned og = base;; ++og) {
            const unsigned nslot = slot == 2 ? 0 : slot + 1;
            TS(2 + 2 * (8 * layer + (og & 7)));
            f32x16 acc;
            unsigned o[8], u;
            float t0, t1;
            const bool last = (og & 7) == 7;
            unsigned lim;
            auto relu2 = [&](unsigned x) AI {
              return __builtin_bit_cast(unsigned, __builtin_elementwise_max(__builtin_bit_cast(s16x2, x), s16x2{0, 0}));
            };
            auto mm = [&](auto kc) AI {
              constexpr int s = decltype(kc)::value;
              if constexpr (s == 0) wmfma0<m_nout(SET, s)>(acc, wr[0], b[0]);
              else wmfma<m_nout(SET, s)>(acc, wr[s], b[s % 5]);
              if constexpr (s <= 35) ards128<XO<(s + 4) % 40>::v>(b[(s + 4) % 5], xa);
              else if constexpr (s == 36) ards128<0>(bx, ha);
              else ards128<XO<(s + 3) % 40>::v>(b[(s + 3) % 5], xa);
              if constexpr (s == 14 || s == 34) ards128<0>(rec, ra);
              if constexpr (s == 2) asm volatile("" : "+v"(out));
              sbar();
              if constexpr (SET == 0) {
                if constexpr (s == m_w1(SET)) awds128<0>(qa, v4u{o[0], o[1], o[2], o[3]});
                if constexpr (s >= 2 && s < 10) {
                  constexpr int j = s - 2;
                  o[j] = pk2(fmul1(out[2 * j], u2f(bx.w)), fmul1(out[2 * j + 1], u2f(bx.w)));
                  asm volatile("" : "+v"(o[j]));
                }
              } else if constexpr (s >= 2 && s <= 18) {
                constexpr int j = (s - 2) >> 1;
                if constexpr ((s & 1) == 0) {
                  if constexpr (j > 0) { o[j - 1] = relu2(u); sbar(); }
                  if constexpr (j < 8) {
                    t0 = fmul1(out[2 * j], u2f(bx.w)); t1 = fmul1(out[2 * j + 1], u2f(bx.w));
                    asm volatile("" : "+v"(t0), "+v"(t1));
                  }
                  if constexpr (j > 0) asm volatile("" : "+v"(o[j - 1]));
                  if constexpr (s == m_w1(SET)) awds128<0>(qa, v4u{o[0], o[1], o[2], o[3]});
                } else {
                  u = pk2(sqs(t0), sqs(t1));
                  asm volatile("" : "+v"(u));
                }
              }
              if constexpr (s == m_w2(SET)) { awds128<32>(qa, v4u{o[4], o[5], o[6], o[7]}); awds32<0>(mda, mdv); }
              if constexpr (s == 19) {
                asm volatile("" : "+v"(rec));
                if (UNLIKELY(any_true(rec.x < 2u * og + 2u))) wait1(C_R + 16 * slot, 2 * og + 2, budget);
              }
              if constexpr (s == 21) {
                ha = hl + slot * 512; ra = C_R + 16 * nslot; mdv = og + 1;
                asm volatile("" : "+v"(ha), "+v"(ra), "+v"(mdv));
              }
              if constexpr (s == 22) {
                qa = ql + slot * QSL; mda = C_MD + 4 * w;
                asm volatile("" : "+v"(qa), "+v"(mda));
              }
              if constexpr (s == 23) {
                lim = last ? ~0u : 2u * og + 3u;
                asm volatile("" : "+s"(lim));
              }
              if constexpr (s == 36) {
                awds32<32>(mda, mdv);
                xa = xl + nslot * 40960;
                asm volatile("" : "+v"(xa));
              }
              sbar();
            };
            static_for<40>(mm);
            asm volatile("" : "+v"(rec));
            uint64_t slow = __builtin_amdgcn_ballot_w64(umin(rec.x, umin(rec.z, rec.w)) < lim);
            asm volatile("" : "+s"(slow));
            TS(3 + 2 * (8 * layer + (og & 7)));
            wmfmac<m_nout(SET, 40)>(out, csf, bx, acc);
            if (UNLIKELY(slow != 0)) {
              asm volatile("s_nop 7\n\ts_nop 7\n\ts_nop 3" : "+v"(out) : "v"(acc));
              wait2(C_R + 16 * nslot + 8, 2 * og + 3, budget);
              const float rs = u2f(bx.w);
#pragma unroll
              for (int j = 0; j < 8; ++j) {
                float a = out[2 * j] * rs, bb = out[2 * j + 1] * rs;
                if (SET == 1) { a *= __builtin_fabsf(a); bb *= __builtin_fabsf(bb); }
                o[j] = pk2(a, bb);
                if (SET == 1) o[j] = relu2(o[j]);
              }
              awds128<0>(qa, v4u{o[0], o[1], o[2], o[3]});
              awds128<32>(qa, v4u{o[4], o[5], o[6], o[7]});
              awds32<0>(mda, mdv);
              if (last) break;
              qa = dl; mda = dl;
              wait1(C_R + 16 * nslot, 2 * og + 3, budget);
              ards128<XO<0>::v>(b[0], xa); ards128<XO<1>::v>(b[1], xa); ards128<XO<2>::v>(b[2], xa);
            }
            ards128<XO<3>::v>(b[3], xa);
            slot = nslot;
          }
          __builtin_amdgcn_s_setprio(3);
          asm volatile("s_waitcnt lgkmcnt(0)" : "+v"(b[0]), "+v"(b[1]), "+v"(b[2])::"memory");
        };
        if (!live) {
          signal(C_MD + w * 4, base + 8);
          signal(C_XF + w * 4, base + 8);
        } else if (set == 0) {
          mloop(IC<0>{});
        } else {
          mloop(IC<1>{});
        }
        if (bid == 126 && w == 3 && ph == 0) {
          for (;;) {
            cfence();
            bool ok = true;
#pragma unroll
            for (int k = 0; k < 2; ++k) {
              const v4u a = ld128c(rws, lane * 16 + k * 1024, OFF_ACK);
              ok = ok && a.x == tag.x && a.y == tag.y && a.z == tag.x && a.w == tag.y;
            }
            if (all_true(ok)) { if (lane == 0) st64c(tg(tag), rws, 0, OFF_E); break; }
            if (budget <= 0) break;
            budget -= 8;
            __builtin_amdgcn_s_sleep(2);
          }
        }
      }
      if (wq >= 2) continue;
      lane = lane_id();
      if (ph & 1) {
        if (isM) continue;
        const unsigned ra = L_QKV + (lane & 15) * QP + (lane >> 4) * 16;
        const unsigned v0 = lane * 16, v1 = c == 13 ? OOB : v0;
        const unsigned vw = c == 13 && (lane & 16) ? OOB : (lane >> 5) * HT + (lane & 31) * 128;
        for (int i = wq; i < 8; i += 2) {
          const unsigned og = base + i, slot = og % 3, u = 7 - i;
          wait4(C_MD + 16, og + 1, budget);
          TS(66 + 2 * (8 * layer + i));
          v4u d[8];
#pragma unroll
          for (int k = 0; k < 8; ++k) d[k] = lds128(ra + slot * QSL + (k >> 2) * 16 * QP + (k & 3) * 64);
          signal2(C_R + 16 * ((og + 1) % 3) + 8, 2 * og + 9);
          const unsigned so = gb + OFF_ACT + (ph * 16 + 2 * u) * HT + c * 4096;
#pragma unroll
          for (int k = 0; k < 8; ++k) st128c(d[k], rws, ((k & 3) < 2 ? v0 : v1) + (k & 3) * 1024, so + (k >> 2) * HT);
          drain();
          warm(rws, vw, so);
          if (lane < 2) st64c(tg(tag), rws, lane * 2048, gb + OFF_FL + (ph * 16 + 2 * u) * 2048 + c * 128);
          TS(67 + 2 * (8 * layer + i));
        }
      } else {
        const int g = lane >> 4, t = lane & 15, c4 = lane & 3;
        const bool hi = wq == 0;
        const bool has_halo = (grp & 3) != 0, give_halo = (grp & 3) != 3;
        const int m0 = set ? 0 : 7 - wq, m1 = set ? 7 - wq : 8;
        f32x4 cP, cQ;
#pragma unroll
        for (int r = 0; r < 4; ++r) {
          const int j = 4 * g + r;
          cP[r] = j >= t + 9 ? 0.f : -INFINITY;
          cQ[r] = (j <= t && j >= t - 7) ? 0.f : -INFINITY;
        }
        const unsigned mp0 = (4 * g > t ? 0xffffu : 0u) | (4 * g + 1 > t ? 0xffff0000u : 0u);
        const unsigned mp1 = (4 * g + 2 > t ? 0xffffu : 0u) | (4 * g + 3 > t ? 0xffff0000u : 0u);
        const unsigned mqa = g == 2 ? 0u : ~0u, mqb = g == 0 ? 0u : ~0u;
        const unsigned kl = t * QP + 16 * g;
        const unsigned vrow = (4 * g + ((lane >> 2) & 3)) * QP;
        const unsigned v0 = vrow + 160 + c4 * 8, v1a = vrow + (c4 ? 240 : 192), v1b = vrow + (c4 ? 240 : 232);
        const unsigned so1 = lane < 16 ? 4096 + (c & 3) * 256 + lane * 16 : OOB;
        const unsigned vw = lane < 8 ? (c & 3) * 1024 + lane * 128 : lane < 10 ? 4096 - 1024 + (c & 3) * 256 + lane * 128 : OOB;
        unsigned pso = OOB;
        bool pend = false;
        unsigned pfl = OOB;
        for (int m = m0; m < m1; ++m) {
          const unsigned og = base + m, u = 7 - m;
          const unsigned need = og + (hi || m == 7 ? 1 : 2);
          if (pend && (m == 7 || peek4(C_MD) < need)) { drain(); warm(rws, vw, pso); st64c(tg(tag), rws, pfl, 0); pend = false; }
          if (!hi && m == 7) {
            if (has_halo) {
              const unsigned id = layer * 16 + c;
              poll64(rws, 0, gb - GRB + OFF_KVF + id * 128, tag, budget);
              v4u d[4];
#pragma unroll
              for (int k = 0; k < 4; ++k) d[k] = ld128c(rws, lane * 16 + k * 1024, gb - GRB + OFF_KV + id * 4096);
#pragma unroll
              for (int k = 0; k < 4; ++k) lds_st128(L_HALO + (4 * k + g) * QP + t * 16, d[k]);
            } else {
              cP = f32x4{-INFINITY, -INFINITY, -INFINITY, -INFINITY};
            }
          }
          wait4(C_MD, need, budget);
          TS(34 + 2 * (8 * layer + m));
          unsigned rc, rp;
          if (hi) { rp = L_QKV + (og % 3) * QSL; rc = rp + 16 * QP; }
          else {
            rc = L_QKV + (og % 3) * QSL;
            rp = m < 7 ? L_QKV + ((og + 1) % 3) * QSL + 16 * QP : (has_halo ? (unsigned)L_HALO : rc);
          }
          const unsigned ac = rc + kl, ap = rp + kl, aq = g == 3 ? (unsigned)C_ZERO : ac;
          v4u qa = lds128(aq), kpa = lds128(ap + 80), kca = lds128(ac + 80);
          v4u qb = lds128(aq + 32), kpb = lds128(ap + 112), kcb = lds128(ac + 112);
          v2u vp[4], vc[4];
          vp[0] = ldstr(rp + v0); vc[0] = ldstr(rc + v0); vp[1] = ldstr(rp + v1a); vc[1] = ldstr(rc + v1a);
          vp[2] = ldstr(rp + v0 + 40); vc[2] = ldstr(rc + v0 + 40); vp[3] = ldstr(rp + v1b); vc[3] = ldstr(rc + v1b);
          qa.z &= mqa; qa.w &= mqa; qb.x &= mqb; qb.y &= mqb;
          const f32x4 spa = mfma16(kpa, qa, cP), sqa = mfma16(kca, qa, cQ);
          const f32x4 spb = mfma16(kpb, qb, cP), sqb = mfma16(kcb, qb, cQ);
          f32x4 sa, sb;
#pragma unroll
          for (int r = 0; r < 4; ++r) { sa[r] = fmx(spa[r], sqa[r]); sb[r] = fmx(spb[r], sqb[r]); }
          const float sc = 0.22360679774997896f * 1.4426950408889634f;
          float na, nb;
          {
            const float xa = fmx(fmx(sa[0], sa[1]), fmx(sa[2], sa[3])), xb = fmx(fmx(sb[0], sb[1]), fmx(sb[2], sb[3]));
            auto s1 = __builtin_amdgcn_permlane16_swap(f2u(xa), f2u(xb), false, false);
            const float y = fmx(u2f(s1[0]), u2f(s1[1]));
            auto s2 = __builtin_amdgcn_permlane32_swap(f2u(y), f2u(y), false, false);
            const float z = fmx(u2f(s2[0]), u2f(s2[1])) * -sc;
            auto s3 = __builtin_amdgcn_permlane16_swap(f2u(z), f2u(z), false, false);
            na = u2f(s3[0]); nb = u2f(s3[1]);
          }
#pragma unroll
          for (int r = 0; r < 4; ++r) {
            sa[r] = __builtin_amdgcn_exp2f(__builtin_fmaf(sa[r], sc, na));
            sb[r] = __builtin_amdgcn_exp2f(__builtin_fmaf(sb[r], sc, nb));
          }
          const v2u pa = pk4(sa), pb = pk4(sb);
          const unsigned a0 = pa.x & mp0, a1 = pa.y & mp1, b0 = pb.x & mp0, b1 = pb.y & mp1;
          const v4u opa = {a0, a1, pa.x ^ a0, pa.y ^ a1}, opb = {b0, b1, pb.x ^ b0, pb.y ^ b1};
          const f32x4 z4 = {0.f, 0.f, 0.f, 0.f};
          const f32x4 oa1 = mfma16(v4u{vp[1].x, vp[1].y, vc[1].x, vc[1].y}, opa, z4);
          const f32x4 ob1 = mfma16(v4u{vp[3].x, vp[3].y, vc[3].x, vc[3].y}, opb, z4);
          const f32x4 oa0 = mfma16(v4u{vp[0].x, vp[0].y, vc[0].x, vc[0].y}, opa, z4);
          const f32x4 ob0 = mfma16(v4u{vp[2].x, vp[2].y, vc[2].x, vc[2].y}, opb, z4);
          auto la = __builtin_amdgcn_permlane16_swap(f2u(oa1[0]), f2u(oa1[0]), false, false);
          auto lb = __builtin_amdgcn_permlane16_swap(f2u(ob1[0]), f2u(ob1[0]), false, false);
          const float ia = __builtin_amdgcn_rcpf(u2f(la[1])), ib = __builtin_amdgcn_rcpf(u2f(lb[1]));
          const v2u e0 = pk4(oa0 * ia), e1 = pk4(ob0 * ib), e2 = pk4(oa1 * ia), e3 = pk4(ob1 * ib);
          const unsigned T = ph * 16 + 2 * u + (hi ? 1 : 0);
          const unsigned so = gb + OFF_ACT + T * HT + 5 * (c >> 2) * 1024;
          st128c(v4u{e0.x, e0.y, e1.x, e1.y}, rws, lane * 16 + (c & 3) * 1024, so);
          st128c(v4u{e2.x, e2.y, e3.x, e3.y}, rws, so1, so);
          if (pend) { vmw<2>(); warm(rws, vw, pso); st64c(tg(tag), rws, pfl, 0); }
          pend = true;
          pso = so;
          pfl = lane == 0 ? gb + OFF_FL + T * 2048 + c * 128 : OOB;
          if (hi && m == 0 && give_halo) {
            const unsigned id = layer * 16 + c;
#pragma unroll
            for (int k = 0; k < 4; ++k) st128c(lds128(rc + (4 * k + g) * QP + t * 16), rws, lane * 16 + k * 1024, gb + OFF_KV + id * 4096);
            if (lane == 1) pfl = gb + OFF_KVF + id * 128;
          }
          if (!hi && m == 6) wait1(C_R + 16 * (og % 3) + 12, 2 * og + 7, budget);
          signal(C_R + 16 * ((og + 1) % 3) + 8 + wq * 4, 2 * og + 9);
          TS(35 + 2 * (8 * layer + m));
        }
        drain();
        warm(rws, vw, pso);
        st64c(tg(tag), rws, pfl, 0);
      }
    }
  } else {
    const unsigned lane = tid & 63, g = lane >> 4, hv = lane * 16;
    if (w == 7) {
      __builtin_amdgcn_s_setprio(3);
      f32x32 res[5];
      const unsigned odd = (hv & 256) << 22, v2 = 2048 + (hv & 240) + ((hv >> 9) << 8) + odd, rv = hv < 256 ? hv : OOB;
      {
        const unsigned xm = (hv & 240) * 160;
        v4u a[8], b[8];
        v2u c[8];
#pragma unroll
        for (int i = 0; i < 8; ++i) {
          const unsigned xs = row0 + (14 - 2 * i + sy) * 40960 + ct * 160;
          a[i] = ld128(rx0, xm + g * 16, xs); b[i] = ld128(rx0, xm + g * 16 + 64, xs);
          c[i] = __builtin_amdgcn_raw_buffer_load_b64(rx0, xm + g * 8 + 128, xs, 0);
        }
        sbar();
#pragma unroll
        for (int i = 0; i < 8; ++i) {
#pragma unroll
          for (int k = 0; k < 4; ++k) {
            const int e = 4 * i + 2 * (k & 1);
            res[k >> 1][e] = bflo(a[i][k]); res[k >> 1][e + 1] = bfhi(a[i][k]);
            res[2 + (k >> 1)][e] = bflo(b[i][k]); res[2 + (k >> 1)][e + 1] = bfhi(b[i][k]);
            if (k < 2) { res[4][e] = bflo(c[i][k]); res[4][e + 1] = bfhi(c[i][k]); }
          }
        }
      }
      TS(2);
      signal(C_PF + 28, ~0u);
      bar();
      TS(3);
      bool pend = false;
      unsigned recA = 0;
      unsigned pa = L_PART + hv;
      asm volatile("" : "+v"(pa));
      pa &= 0x3ffff;
#pragma nounroll
      for (unsigned r = 0; r < 32; ++r) {
        const unsigned i = r & 7, n = ((2 * r) ^ 14) + 2;
        f32x4 y[5], A[5], B[5], C[5];
        unsigned i4 = 4 * i;
        asm volatile("" : "+s"(i4));
        i4 &= 28;
#pragma unroll
        for (int k = 0; k < 20; ++k) y[k >> 2][k & 3] = res[k >> 2][i4 + (k & 3)];
        asm volatile("" : "+v"(y[0]), "+v"(y[1]), "+v"(y[2]), "+v"(y[3]), "+v"(y[4]));
        auto rd = [&](f32x4 (&o)[5], int q) AI {
#pragma unroll
          for (int j = 0; j < 5; ++j) o[j] = ldsf4(pa + q * PSZ + j * 1024);
          sbar();
        };
        auto add = [&](const f32x4 (&o)[5]) AI {
#pragma unroll
          for (int j = 0; j < 5; ++j) padd(y[j], o[j]);
          sbar();
        };
        cfence();
        unsigned lo = peek4(C_PF), hi = peek4(C_PF + 16);
        if (UNLIKELY(lo <= r)) {
          if (pend) { drain(); rec_store(rv, recA); pend = false; }
          wait4(C_PF, r + 1, budget);
          hi = peek4(C_PF + 16);
        }
        cfence();
        TS(4 + 3 * r);
        rd(A, 0); rd(B, 1); rd(C, 2);
        add(A); rd(A, 3); add(B);
        if (UNLIKELY(hi <= r)) {
          if (pend) { drain(); rec_store(rv, recA); pend = false; }
          wait4(C_PF + 16, r + 1, budget);
        }
        cfence();
        rd(B, 4); add(C); rd(C, 5); add(A); rd(A, 6);
        signal(C_FD, r + 1);
        add(B); add(C); add(A);
        if (pend) { drain(); rec_store(rv, recA); pend = false; }
        const v2u e0 = pk4(y[0]), e1 = pk4(y[1]), e2 = pk4(y[2]), e3 = pk4(y[3]);
        const v4u d0 = {e0.x, e0.y, e1.x, e1.y}, d1 = {e2.x, e2.y, e3.x, e3.y}, d2 = third(pk4(y[4]));
        if (r >= 24) {
          unsigned l = hv;
          asm volatile("" : "+v"(l));
          const unsigned xl = (l & 240) * 160;
          yst3<64>(d0, d1, d2, xl + ((l >> 8) << 4), xl + 128 + ((l >> 9) << 4) + ((l & 256) << 22),
                   __builtin_amdgcn_make_buffer_rsrc(p.out, 0, 2048 * 1280, 0x00020000), row0 + (14 - 2 * i + sy) * 40960 + ct * 160);
        } else {
          yst3<1024>(d0, d1, d2, hv, v2, rws, xo + n * XT);
          TS(5 + 3 * r);
          asm volatile("" : "+s"(i4));
          i4 &= 28;
#pragma unroll
          for (int k = 0; k < 20; ++k) res[k >> 2][i4 + (k & 3)] = y[k >> 2][k & 3];
          stats(y);
          recA = ro + n * 2048;
          pend = true;
        }
        TS(6 + 3 * r);
      }
      drain();
    } else {
      v4u w2[8][5];
      const bool bias = w == 6;
      const unsigned ones = gb + OFF_ONE + rk * 2048;
      {
        const v4u one = {0x3f803f80u, 0x3f803f80u, 0x3f803f80u, 0x3f803f80u};
        ast128c(one, rws, hv, bias ? ones : OOB);
        ast128c(one, rws, hv + 1024, bias ? ones : OOB);
        const unsigned cb = (80 * ct >> 6) + ((lane >> 3) & 1), l7 = lane & 7;
        poll64(rws, lane < 16 ? (57 + cb) * 512 + umin(8 * w + l7, 54u) * 8 : (67 + cb) * 512 + (w + 7 * l7 < 20 ? w + 7 * l7 : w) * 8,
               OFF_WF, tag, budget);
        TS(100);
        const unsigned vo = g * 10240 + (lane & 15) * 16;
        for (unsigned blk = w; blk < 20; blk += 7) {
#pragma unroll
          for (int j = 0; j < 5; ++j) glds(rws, vo, OFF_W3 + blk * 40960 + ct * 1280 + j * 256, blk * 5120 + j * 1024);
        }
        auto gl = [&](auto kc) AI {
          constexpr int k = decltype(kc)::value / 5, j = decltype(kc)::value % 5;
          ald128n<j * 256>(w2[k][j], rws, vo, k == 7 && bias ? OOB : OFF_W2 + (8 * w + k) * 40960 + ct * 1280);
        };
        static_for<40>(gl);
      }
      drain();
#pragma unroll
      for (int k = 0; k < 40; ++k) asm volatile("" : "+v"(w2[k / 5][k % 5]));
      TS(2);
      bar();
      TS(3);
      if (w < 4) __builtin_amdgcn_s_setprio(1);
      const unsigned pvo = (lane < 32 ? 4096 : 0) + ((lane & 31) >> 1) * 128 + (lane & 1) * 4, tagv = lane & 1 ? tag.y : tag.x, ck = (lane & 31) * 4;
      const unsigned pw = L_PART + w * PSZ + hv;
      auto phase = [&](auto pc, unsigned r0) AI {
        constexpr bool P5 = decltype(pc)::value;
        constexpr int K = P5 ? 8 : 3, N = P5 ? 9 : 3;
        const unsigned q = w & 3, n0 = 2 * r0 + 14 + sy;
        const unsigned lanes = P5 ? 15u << 4 * w : w < 3 ? 63u << 8 * w : (255u << 8 * q) | (3u << (24 + 2 * q));
        auto ex = [&](unsigned f) AI { return ((uint64_t)(f < r0 + 7 ? lanes : 0u) << 32) | lanes; };
        unsigned fl = rfl(gb + OFF_FL + n0 * 2048 - 4096);
        unsigned ac = gb + OFF_ACT + n0 * HT + (P5 ? 8 * w : 5 * q + (w < 3 ? 0 : 3)) * 1024;
        unsigned pm = L_RDY + r0 * 128;
        const unsigned d3 = w < 3 ? 0u : w == 3 ? OOB : (10 - 4 * q) * 1024;
        auto last = [&](unsigned a) AI { return P5 ? (bias ? ones - 2048 : a + 4096) : a + d3; };
        auto ready = [&](unsigned a) AI { return eqall(*(volatile LAS unsigned *)(uintptr_t)(ck + a), tagv, lanes); };
        v4u h[K], wo[10];
#pragma unroll
        for (int k = 0; k < K; ++k) asm volatile("" : "=v"(h[k]));
        const unsigned wr0 = (5 * q + (w < 3 ? 0 : 3)) * 5120 + hv;
        if constexpr (!P5) {
          const unsigned wr2 = w < 3 ? wr0 + 10240 : (15 + q) * 5120 + hv;
#pragma unroll
          for (int j = 0; j < 5; ++j) { wo[j] = lds128(wr0 + 5120 + j * 1024); wo[5 + j] = lds128(wr2 + j * 1024); }
        }
        unsigned have = 0;
        for (unsigned r = r0; r < r0 + 8; ++r, fl -= 4096, ac -= 2 * HT, pm += 128) {
          const unsigned pm1 = r < r0 + 7 ? pm + 128 : L_DUMMY, pm2 = r < r0 + 6 ? pm + 256 : L_DUMMY;
          f32x4 acc[5];
          if constexpr (!P5) {
#pragma unroll
            for (int j = 0; j < 5; ++j) acc[j] = ldsf4(wr0 + j * 1024);
          }
          if (!have) {
            while (!ready(pm) && budget > 0) {
              ypoll(ex(r), pm, pvo, rws, fl);
              budget -= 2;
              __builtin_amdgcn_s_sleep(2);
            }
          }
          yfetch(h, hv, rws, ac, ac + 4096, last(ac), ~have, ex(r + 1), pm1, pvo, fl - 4096);
          const unsigned nxt = ready(pm + 128) & (r < r0 + 7 ? ~0u : 0u);
          const unsigned qm = msel(nxt, pm2, pm1), qf = fl - 4096 - (nxt & 4096);
          const uint64_t qx = ex(r + 1 - nxt);
          ypoll(qx, qm, pvo, rws, qf);
          const unsigned so = msel(nxt, ac - 2 * HT, OOB), sl = msel(nxt, last(ac - 2 * HT), OOB);
          const uint64_t em = ((uint64_t)nxt << 32) | (nxt | 1u);
          if constexpr (P5) {
            ygrp<N, 0, true>(acc, w2[0], h[0], hv, rws, so, em);
            TS(4 + 3 * r);
            ygrp<N, 1024, false>(acc, w2[1], h[1], hv, rws, so, em);
            ygrp<N, 2048, false>(acc, w2[2], h[2], hv, rws, so, em);
            ygrp<N, 3072, false>(acc, w2[3], h[3], hv, rws, so, em);
            ypoll(qx, qm, pvo, rws, qf);
            ygrp<N, 0, false>(acc, w2[4], h[K - 4], hv, rws, so + 4096, em);
            ygrp<N, 1024, false>(acc, w2[5], h[K - 3], hv, rws, so + 4096, em);
            ygrp<N, 2048, false>(acc, w2[6], h[K - 2], hv, rws, sl, em);
          } else {
            ygrp_in<N>(acc, h[0], hv, rws, so, em);
            TS(4 + 3 * r);
            ygrp<N, 1024, false>(acc, wo, h[1], hv, rws, so, em);
          }
          const unsigned fd = *(volatile LAS unsigned *)(uintptr_t)C_FD;
          ygrp<N, P5 ? 3072 : 2048, false>(acc, P5 ? w2[7] : wo + 5, h[K - 1], hv, rws, sl, em);
          have = nxt;
          if (UNLIKELY(rfl(fd) < r)) wait1(C_FD, r, budget);
          mfin(acc);
          awds5(pw, acc);
          signal(C_PF + 4 * w, r + 1);
          TS(5 + 3 * r);
        }
#pragma unroll
        for (int k = 0; k < K; ++k) vmw<0>(h[k]);
      };
#pragma nounroll
      for (unsigned r0 = 0; r0 < 32; r0 += 16) {
        phase(IC<0>{}, r0);
        phase(IC<1>{}, r0 + 8);
      }
    }
  }
  drain();
}

extern "C" void kernel_launch(void *const *d_in, const int *in_sizes, int n_in, void *d_out, int out_size, void *d_ws,
                              size_t ws_size, hipStream_t stream) {
  Params p;
  p.x = d_in[0]; p.wqkv = d_in[1]; p.wo = d_in[2]; p.w1 = d_in[4]; p.b1 = d_in[5]; p.w2 = d_in[6]; p.b2 = d_in[7];
  p.out = d_out; p.ws = d_ws;
  (void)hipFuncSetAttribute((const void *)k_fwd, hipFuncAttributeMaxDynamicSharedMemorySize, L_TOTAL);
  k_fwd<<<256, 512, L_TOTAL, stream>>>(p);
}
```

```cpp
#include <hip/hip_runtime.h>
#include <stdint.h>

typedef unsigned v4u __attribute__((ext_vector_type(4)));
typedef unsigned v2u __attribute__((ext_vector_type(2)));
typedef float f32x4 __attribute__((ext_vector_type(4)));
typedef float f32x2 __attribute__((ext_vector_type(2)));
typedef float f32x32 __attribute__((ext_vector_type(32)));
typedef float f32x16 __attribute__((ext_vector_type(16)));
typedef __bf16 bf16x8 __attribute__((ext_vector_type(8)));
typedef __bf16 bf16x2 __attribute__((ext_vector_type(2)));
typedef short s16x4 __attribute__((ext_vector_type(4)));
typedef short s16x2 __attribute__((ext_vector_type(2)));
typedef __amdgpu_buffer_rsrc_t rsrc_t;
#define LAS __attribute__((address_space(3)))
#define AI __attribute__((always_inline))
#define DI __device__ __forceinline__
#define UNLIKELY(x) __builtin_expect(!!(x), 0)
#define YD 0
#define WU 4
#define SPLITX 1
#define XMAP 2
template <int V> struct IC { static constexpr int value = V; };
template <int N, int I = 0, class F> DI void static_for(F &f) {
  if constexpr (I < N) { f(IC<I>{}); static_for<N, I + 1>(f); }
}

constexpr unsigned XT = 20480, HT = 55296, GRB = 8u << 20;
constexpr unsigned OFF_KVF = 0;
constexpr unsigned OFF_REC = OFF_KVF + 2 * 2048;
constexpr unsigned OFF_KV = OFF_REC + 64 * 2048, KVP = 4608;
constexpr unsigned OFF_ONE = OFF_KV + 2 * 16 * KVP;
constexpr unsigned OFF_XF = OFF_ONE + 16 * 4096;
constexpr unsigned OFF_ACT = OFF_XF + 64 * XT;
constexpr unsigned OFF_E = GRB - 8192, OFF_ACK = GRB - 4096;
static_assert(OFF_ACT + 48 * HT + 2048 <= OFF_E, "ws map");
constexpr unsigned OFF_W0 = 8 * GRB, OFF_W1 = OFF_W0 + 80 * 1920 * 16, OFF_W2 = OFF_W1 + 80 * 1728 * 16;
constexpr unsigned OFF_W3 = OFF_W2 + 55 * 40960;
constexpr unsigned OFF_WF = OFF_W3 + 20 * 40960;
constexpr unsigned OFF_CS = OFF_WF + 77 * 512;
constexpr unsigned CSP = 3648 * 4, WS_END = OFF_CS + 20 * CSP;
constexpr unsigned OOB = 0x40000000u;
constexpr unsigned L_QKV = 122880, QP = 272, QSL = 32 * QP, L_HALO = L_QKV + 3 * QSL, L_HST = L_HALO + 16 * QP;
constexpr unsigned L_STG = 155648;
constexpr unsigned L_PART = 102400, PSZ = 5120;
constexpr unsigned L_CTL = 159744, L_TS = 160256, L_DUMMY = 162304, L_TOTAL = 163840;
constexpr unsigned C_R = L_CTL + 16, C_MD = L_CTL + 64, C_XF = L_CTL + 96, C_ZERO = L_CTL + 128, C_WL = L_CTL + 176, C_W1 = L_CTL + 208;
constexpr unsigned C_PF = L_CTL + 64, C_PL = L_CTL + 80, C_FD = L_CTL + 96;
static_assert(L_HST + 3 * 512 <= L_STG, "lds map");
static_assert(WU <= 4, "start-up order");
constexpr int SPIN_CAP = 1 << 18;

struct Params {
  const void *x, *wqkv, *wo, *w1, *b1, *w2, *b2;
  void *out, *ws;
};

DI void cfence() { asm volatile("" ::: "memory"); }
DI void bar() { asm volatile("s_waitcnt lgkmcnt(0)\n\ts_barrier" ::: "memory"); }
DI void drain() { asm volatile("s_waitcnt vmcnt(0)" ::: "memory"); }
template <int N> DI void vmw() { asm volatile("s_waitcnt vmcnt(%0)" ::"n"(N) : "memory"); }
template <int N> DI void vmw(v4u &d) { asm volatile("s_waitcnt vmcnt(%1)" : "+v"(d) : "n"(N) : "memory"); }
template <int O1> DI void yst3(v4u a, v4u b, v4u c, unsigned v0, unsigned v2, rsrc_t r, unsigned so) {
  asm volatile("buffer_store_dwordx4 %0, %3, %5, %6 offen sc1\n\t"
               "buffer_store_dwordx4 %1, %3, %5, %6 offen offset:%7 sc1\n\t"
               "buffer_store_dwordx4 %2, %4, %5, %6 offen sc1\n\ts_nop 1"
               ::"v"(a), "v"(b), "v"(c), "v"(v0), "v"(v2), "s"(r), "s"(so), "n"(O1) : "memory");
}
DI float fmul1(float a, float b) { float d; asm("v_mul_f32 %0, %1, %2" : "=v"(d) : "v"(a), "v"(b)); return d; }
DI float sqs(float a) { float d; asm("v_mul_f32 %0, %1, |%1|" : "=v"(d) : "v"(a)); return d; }
DI void padd(f32x4 &a, f32x4 b) {
  float x = a.x, y = a.y, z = a.z, u = a.w;
  asm("v_add_f32 %0, %0, %4\n\tv_add_f32 %1, %1, %5\n\tv_add_f32 %2, %2, %6\n\tv_add_f32 %3, %3, %7"
      : "+v"(x), "+v"(y), "+v"(z), "+v"(u) : "v"(b.x), "v"(b.y), "v"(b.z), "v"(b.w));
  a = f32x4{x, y, z, u};
}
DI void psq(float &q0, float &q1, f32x4 a, float m) {
  float x, y;
  asm("v_sub_f32 %2, %4, %8\n\tv_sub_f32 %3, %5, %8\n\tv_fmac_f32 %0, %2, %2\n\tv_fmac_f32 %1, %3, %3\n\t"
      "v_sub_f32 %2, %6, %8\n\tv_sub_f32 %3, %7, %8\n\tv_fmac_f32 %0, %2, %2\n\tv_fmac_f32 %1, %3, %3"
      : "+v"(q0), "+v"(q1), "=&v"(x), "=&v"(y) : "v"(a.x), "v"(a.y), "v"(a.z), "v"(a.w), "v"(m));
}
DI void psum(float &s0, float &s1, f32x4 a) {
  asm("v_add_f32 %0, %0, %2\n\tv_add_f32 %1, %1, %3\n\tv_add_f32 %0, %0, %4\n\tv_add_f32 %1, %1, %5"
      : "+v"(s0), "+v"(s1) : "v"(a.x), "v"(a.y), "v"(a.z), "v"(a.w));
}
DI void sbar() { __builtin_amdgcn_sched_barrier(0); }
DI float u2f(unsigned u) { return __builtin_bit_cast(float, u); }
DI unsigned f2u(float f) { return __builtin_bit_cast(unsigned, f); }
DI float bflo(unsigned u) { return u2f(u << 16); }
DI float bfhi(unsigned u) { return u2f(u & 0xffff0000u); }
DI unsigned rfl(unsigned v) { return __builtin_amdgcn_readfirstlane(v); }
DI int lane_id() {
  int l;
  asm volatile("v_mbcnt_lo_u32_b32 %0, -1, 0\n\tv_mbcnt_hi_u32_b32 %0, -1, %0" : "=v"(l));
  return l;
}
DI unsigned pk2(float a, float b) {
  f32x2 v = {a, b};
  return __builtin_bit_cast(unsigned, __builtin_convertvector(v, bf16x2));
}
DI v2u pk4(f32x4 v) { return v2u{pk2(v[0], v[1]), pk2(v[2], v[3])}; }
DI f32x4 mfma16(v4u a, v4u b, f32x4 c) {
  return __builtin_amdgcn_mfma_f32_16x16x32_bf16(__builtin_bit_cast(bf16x8, a), __builtin_bit_cast(bf16x8, b), c, 0, 0, 0);
}
DI f32x16 mfma32(v4u a, v4u b, f32x16 c) {
  return __builtin_amdgcn_mfma_f32_32x32x16_bf16(__builtin_bit_cast(bf16x8, a), __builtin_bit_cast(bf16x8, b), c, 0, 0, 0);
}
DI float dot1(unsigned a, float c) {
  return __builtin_amdgcn_fdot2_f32_bf16(__builtin_bit_cast(bf16x2, a), __builtin_bit_cast(bf16x2, 0x3f803f80u), c, false);
}
DI float fmx(float a, float b) { return __builtin_elementwise_maximum(a, b); }
DI float half_sum(float x) {
  auto t = __builtin_amdgcn_permlane32_swap(f2u(x), f2u(x), false, false);
  return u2f(t[0]) + u2f(t[1]);
}
DI float row_sum(float x) {
  auto s = __builtin_amdgcn_permlane16_swap(f2u(x), f2u(x), false, false);
  return half_sum(u2f(s[0]) + u2f(s[1]));
}
DI float row_sum2(float a, float b) {
  auto s = __builtin_amdgcn_permlane16_swap(f2u(a), f2u(b), false, false);
  return half_sum(u2f(s[0]) + u2f(s[1]));
}
DI v4u ld128(rsrc_t r, unsigned vo, unsigned so) { return __builtin_amdgcn_raw_buffer_load_b128(r, vo, so, 0); }
DI v4u ld128c(rsrc_t r, unsigned vo, unsigned so) { return __builtin_amdgcn_raw_buffer_load_b128(r, vo, so, 16); }
DI v2u ld64c(rsrc_t r, unsigned vo, unsigned so) { return __builtin_amdgcn_raw_buffer_load_b64(r, vo, so, 16); }
DI void st128c(v4u v, rsrc_t r, unsigned vo, unsigned so) {
  asm volatile("" : "+v"(v));
  __builtin_amdgcn_raw_buffer_store_b128(v, r, vo, so, 16);
  asm volatile("s_nop 1" ::"v"(v) : "memory");
}
DI void st64c(v2u v, rsrc_t r, unsigned vo, unsigned so) { __builtin_amdgcn_raw_buffer_store_b64(v, r, vo, so, 16); }
template <int OFF> DI void ald128c(v4u &d, rsrc_t r, unsigned vo, unsigned so) {
  asm volatile("buffer_load_dwordx4 %0, %1, %2, %3 offen offset:%4 sc1" : "+v"(d) : "v"(vo), "s"(r), "s"(so), "n"(OFF) : "memory");
}
DI void ald128(v4u &d, rsrc_t r, unsigned vo, unsigned so) {
  asm volatile("buffer_load_dwordx4 %0, %1, %2, %3 offen" : "=&v"(d) : "v"(vo), "s"(r), "s"(so) : "memory");
}
template <int OFF> DI void ald128n(v4u &d, rsrc_t r, unsigned vo, unsigned so) {
  asm volatile("buffer_load_dwordx4 %0, %1, %2, %3 offen offset:%4 sc1" : "=&v"(d) : "v"(vo), "s"(r), "s"(so), "n"(OFF) : "memory");
}
DI void ald4x128c(v4u (&d)[4], rsrc_t r, unsigned vo, unsigned so) {
  asm volatile("s_nop 4\n\t"
               "buffer_load_dwordx4 %0, %4, %5, %6 offen sc1\n\t"
               "buffer_load_dwordx4 %1, %4, %5, %6 offen offset:1024 sc1\n\t"
               "buffer_load_dwordx4 %2, %4, %5, %6 offen offset:2048 sc1\n\t"
               "buffer_load_dwordx4 %3, %4, %5, %6 offen offset:3072 sc1"
               : "+v"(d[0]), "+v"(d[1]), "+v"(d[2]), "+v"(d[3]) : "v"(vo), "s"(r), "s"(so) : "memory");
}
DI void ald4x128w(v4u (&d)[4], rsrc_t r, unsigned vo, unsigned so) {
  asm volatile("s_nop 4\n\t"
               "buffer_load_dwordx4 %0, %4, %5, %6 offen sc1\n\t"
               "buffer_load_dwordx4 %1, %4, %5, %6 offen offset:1024 sc1\n\t"
               "buffer_load_dwordx4 %2, %4, %5, %6 offen offset:2048 sc1\n\t"
               "buffer_load_dwordx4 %3, %4, %5, %6 offen offset:3072 sc1\n\ts_waitcnt vmcnt(0)"
               : "=&v"(d[0]), "=&v"(d[1]), "=&v"(d[2]), "=&v"(d[3]) : "v"(vo), "s"(r), "s"(so) : "memory");
}
DI void ast128c(v4u d, rsrc_t r, unsigned vo, unsigned so) {
  asm volatile("s_nop 4\n\tbuffer_store_dwordx4 %0, %1, %2, %3 offen sc1\n\ts_nop 1" ::"v"(d), "v"(vo), "s"(r), "s"(so) : "memory");
}
template <int OFF> DI void ast128o(v4u d, rsrc_t r, unsigned vo, unsigned so) {
  asm volatile("buffer_store_dwordx4 %0, %1, %2, %3 offen offset:%4 sc1\n\ts_nop 1" ::"v"(d), "v"(vo), "s"(r), "s"(so), "n"(OFF) : "memory");
}
DI void glds(rsrc_t r, unsigned vo, unsigned so, unsigned dst) {
  unsigned keep;
  asm volatile("s_mov_b32 %0, m0\n\ts_mov_b32 m0, %1\n\ts_nop 2\n\tbuffer_load_dwordx4 %2, %3, %4 offen sc1 lds\n\ts_mov_b32 m0, %0"
               : "=&s"(keep) : "s"(dst), "v"(vo), "s"(r), "s"(so) : "memory");
}
DI void glds4c(rsrc_t r, unsigned vo, unsigned so, unsigned dst) {
  unsigned keep;
  asm volatile("s_mov_b32 %0, m0\n\ts_mov_b32 m0, %1\n\ts_nop 2\n\t"
               "buffer_load_dwordx4 %2, %3, %4 offen sc1 lds\n\t"
               "buffer_load_dwordx4 %2, %3, %4 offen offset:1024 sc1 lds\n\t"
               "buffer_load_dwordx4 %2, %3, %4 offen offset:2048 sc1 lds\n\t"
               "buffer_load_dwordx4 %2, %3, %4 offen offset:3072 sc1 lds\n\t"
               "s_mov_b32 m0, %0"
               : "=&s"(keep) : "s"(dst), "v"(vo), "s"(r), "s"(so) : "memory");
}
DI void glds3(rsrc_t r, unsigned v0, unsigned v1, unsigned v2, unsigned so, unsigned dst) {
  unsigned keep;
  asm volatile("s_mov_b32 %0, m0\n\ts_mov_b32 m0, %1\n\ts_nop 2\n\t"
               "buffer_load_dwordx4 %2, %5, %6 offen lds\n\t"
               "buffer_load_dwordx4 %3, %5, %6 offen offset:1024 lds\n\t"
               "buffer_load_dwordx4 %4, %5, %6 offen offset:2048 lds\n\t"
               "s_mov_b32 m0, %0"
               : "=&s"(keep) : "s"(dst), "v"(v0), "v"(v1), "v"(v2), "s"(r), "s"(so) : "memory");
}
DI v2u lds64(unsigned a) { return *(LAS v2u *)(uintptr_t)a; }
DI v4u lds128(unsigned a) { return *(LAS v4u *)(uintptr_t)a; }
DI f32x4 ldsf4(unsigned a) { return *(LAS f32x4 *)(uintptr_t)a; }
DI void lds_st128(unsigned a, v4u v) { *(LAS v4u *)(uintptr_t)a = v; }
DI void lds_stf4(unsigned a, f32x4 v) { *(LAS f32x4 *)(uintptr_t)a = v; }
DI void lds_st32(unsigned a, unsigned v) { *(LAS unsigned *)(uintptr_t)a = v; }
DI v2u ldstr(unsigned a) {
  return __builtin_bit_cast(v2u, __builtin_amdgcn_ds_read_tr16_b64_v4i16((LAS s16x4 *)(uintptr_t)a));
}
template <int OFF> DI void ards128(v4u &d, unsigned a) {
  asm volatile("ds_read_b128 %0, %1 offset:%2" : "+v"(d) : "v"(a), "n"(OFF) : "memory");
}
template <int OFF> DI void awds128(unsigned a, v4u d) {
  asm volatile("ds_write_b128 %0, %1 offset:%2" ::"v"(a), "v"(d), "n"(OFF) : "memory");
}
template <int OFF> DI void awds32(unsigned a, unsigned d) {
  asm volatile("ds_write_b32 %0, %1 offset:%2" ::"v"(a), "v"(d), "n"(OFF) : "memory");
}
DI unsigned sel(bool c, unsigned a, unsigned b) { return c ? a : b; }
DI unsigned umin(unsigned a, unsigned b) { return a < b ? a : b; }
DI void signal(unsigned a, unsigned v) {
  cfence();
  *(volatile LAS unsigned *)(uintptr_t)a = v;
  cfence();
}
DI void signal2(unsigned a, unsigned v) {
  cfence();
  *(volatile LAS v2u *)(uintptr_t)a = v2u{v, v};
  cfence();
}
DI unsigned peek4(unsigned a) {
  const v4u v = *(volatile LAS v4u *)(uintptr_t)a;
  return rfl(umin(umin(v.x, v.y), umin(v.z, v.w)));
}
DI void wait4(unsigned a, unsigned target, int &budget) {
  cfence();
  if (UNLIKELY(peek4(a) < target)) {
    do { --budget; __builtin_amdgcn_s_sleep(1); } while (peek4(a) < target && budget > 0);
  }
  cfence();
}
DI void wait2(unsigned a, unsigned target, int &budget) {
  cfence();
  for (;;) {
    const v2u v = *(volatile LAS v2u *)(uintptr_t)a;
    if (rfl(umin(v.x, v.y)) >= target || budget <= 0) break;
    --budget;
    __builtin_amdgcn_s_sleep(1);
  }
  cfence();
}
DI void wait1(unsigned a, unsigned target, int &budget) {
  cfence();
  if (UNLIKELY(rfl(*(volatile LAS unsigned *)(uintptr_t)a) < target)) {
    do { --budget; __builtin_amdgcn_s_sleep(1); } while (rfl(*(volatile LAS unsigned *)(uintptr_t)a) < target && budget > 0);
  }
  cfence();
}
DI v2u tg(v2u t, unsigned n = 0) {
  unsigned a = t.x + n, b = t.y + n;
  asm volatile("" : "+s"(a), "+s"(b));
  return v2u{a, b};
}
DI bool any_true(bool p) { return __builtin_amdgcn_ballot_w64(p) != 0; }
DI bool all_true(bool p) { return __builtin_amdgcn_ballot_w64(!p) == 0; }
DI void poll64(rsrc_t r, unsigned vo, unsigned so, v2u tag, int &budget) {
  for (;;) {
    cfence();
    const v2u v = ld64c(r, vo, so);
    if (all_true(v.x == tag.x && v.y == tag.y) || budget <= 0) break;
    budget -= 8;
    __builtin_amdgcn_s_sleep(2);
  }
  cfence();
}
#define TS(i)
#define TSD(i)
#define TSP(i)
#define YTS(i)
#define YTP(i)
#define FTS(i)
#define FTP(i)
#define XTS(i)
#define ATS(i)
#define LTS(i)
#define XTI(i)
#define XTP(i)
#define TSATTR
#define TSARG
#define YPROG \
  "s_mov_b32 s50, 0x40000000\ns_add_u32 s49, %[ct], 0\ns_and_b32 s49, s49, 7\ns_mul_i32 s48, s49, 40960\ns_add_u32 s48, s48, %[wb]\n" \
  "s_cmp_gt_u32 s49, %[nb]\ns_cselect_b32 s60, s50, s48\nbuffer_load_dwordx4 v[40:43], %[vo], %[rs], s60 offen offset:0 sc1\n" \
  "buffer_load_dwordx4 v[44:47], %[vo], %[rs], s60 offen offset:256 sc1\nbuffer_load_dwordx4 v[48:51], %[vo], %[rs], s60 offen offset:512 sc1\n" \
  "buffer_load_dwordx4 v[52:55], %[vo], %[rs], s60 offen offset:768 sc1\nbuffer_load_dwordx4 v[56:59], %[vo], %[rs], s60 offen offset:1024 sc1\n" \
  "s_add_u32 s49, %[ct], 1\ns_and_b32 s49, s49, 7\ns_mul_i32 s48, s49, 40960\ns_add_u32 s48, s48, %[wb]\ns_cmp_gt_u32 s49, %[nb]\n" \
  "s_cselect_b32 s61, s50, s48\nbuffer_load_dwordx4 v[60:63], %[vo], %[rs], s61 offen offset:0 sc1\n" \
  "buffer_load_dwordx4 v[64:67], %[vo], %[rs], s61 offen offset:256 sc1\nbuffer_load_dwordx4 v[68:71], %[vo], %[rs], s61 offen offset:512 sc1\n" \
  "buffer_load_dwordx4 v[72:75], %[vo], %[rs], s61 offen offset:768 sc1\nbuffer_load_dwordx4 v[76:79], %[vo], %[rs], s61 offen offset:1024 sc1\n" \
  "s_add_u32 s49, %[ct], 2\ns_and_b32 s49, s49, 7\ns_mul_i32 s48, s49, 40960\ns_add_u32 s48, s48, %[wb]\ns_cmp_gt_u32 s49, %[nb]\n" \
  "s_cselect_b32 s62, s50, s48\nbuffer_load_dwordx4 v[80:83], %[vo], %[rs], s62 offen offset:0 sc1\n" \
  "buffer_load_dwordx4 v[84:87], %[vo], %[rs], s62 offen offset:256 sc1\nbuffer_load_dwordx4 v[88:91], %[vo], %[rs], s62 offen offset:512 sc1\n" \
  "buffer_load_dwordx4 v[92:95], %[vo], %[rs], s62 offen offset:768 sc1\nbuffer_load_dwordx4 v[96:99], %[vo], %[rs], s62 offen offset:1024 sc1\n" \
  "s_add_u32 s49, %[ct], 3\ns_and_b32 s49, s49, 7\ns_mul_i32 s48, s49, 40960\ns_add_u32 s48, s48, %[wb]\ns_cmp_gt_u32 s49, %[nb]\n" \
  "s_cselect_b32 s63, s50, s48\nbuffer_load_dwordx4 v[100:103], %[vo], %[rs], s63 offen offset:0 sc1\n" \
  "buffer_load_dwordx4 v[104:107], %[vo], %[rs], s63 offen offset:256 sc1\nbuffer_load_dwordx4 v[108:111], %[vo], %[rs], s63 offen offset:512 sc1\n" \
  "buffer_load_dwordx4 v[112:115], %[vo], %[rs], s63 offen offset:768 sc1\nbuffer_load_dwordx4 v[116:119], %[vo], %[rs], s63 offen offset:1024 sc1\n" \
  "s_add_u32 s49, %[ct], 4\ns_and_b32 s49, s49, 7\ns_mul_i32 s48, s49, 40960\ns_add_u32 s48, s48, %[wb]\ns_cmp_gt_u32 s49, %[nb]\n" \
  "s_cselect_b32 s64, s50, s48\ns_add_u32 s49, %[ct], 5\ns_and_b32 s49, s49, 7\ns_mul_i32 s48, s49, 40960\ns_add_u32 s48, s48, %[wb]\n" \
  "s_cmp_gt_u32 s49, %[nb]\ns_cselect_b32 s65, s50, s48\ns_add_u32 s49, %[ct], 6\ns_and_b32 s49, s49, 7\ns_mul_i32 s48, s49, 40960\n" \
  "s_add_u32 s48, s48, %[wb]\ns_cmp_gt_u32 s49, %[nb]\ns_cselect_b32 s66, s50, s48\ns_add_u32 s49, %[ct], 7\ns_and_b32 s49, s49, 7\n" \
  "s_mul_i32 s48, s49, 40960\ns_add_u32 s48, s48, %[wb]\ns_cmp_gt_u32 s49, %[nb]\ns_cselect_b32 s67, s50, s48\ns_mov_b32 s46, 1\ns_mov_b32 s51, %[cl]\n" \
  "v_mov_b32 v246, -1\nv_mov_b32 v247, -1\nv_mov_b32 v248, -1\nv_mov_b32 v249, -1\ns_mov_b32 s47, %[gd]\ns_getpc_b64 s[68:69]\n.LY%=_P:\n" \
  "s_add_u32 s68, s68, .LY%=_W-.LY%=_P\ns_addc_u32 s69, s69, 0\ns_waitcnt vmcnt(20)\n" \
  YTS(-1) \
  "s_barrier\n" \
  YTS(0) \
  "s_add_u32 s52, %[a3], 0\ns_add_u32 s53, s52, 1024\ns_add_u32 s54, s52, %[d3]\nbuffer_load_dwordx4 v[8:11], %[hv], %[rs], s52 offen sc1\n" \
  "buffer_load_dwordx4 v[12:15], %[hv], %[rs], s53 offen sc1\nbuffer_load_dwordx4 v[16:19], %[hv], %[rs], s54 offen sc1\n" \
  "buffer_load_dword v244, %[hv], %[rs], s50 offen\nbuffer_load_dword v244, %[hv], %[rs], s50 offen\nbuffer_load_dword v244, %[hv], %[rs], s50 offen\n" \
  "buffer_load_dword v244, %[hv], %[rs], s50 offen\nbuffer_load_dword v244, %[hv], %[rs], s50 offen\nbuffer_load_dword v244, %[hv], %[rs], s50 offen\n" \
  "buffer_load_dword v244, %[hv], %[rs], s50 offen\ns_mov_b32 s48, 8\nds_read_b128 v[20:23], %[wr2] offset:0\nds_read_b128 v[24:27], %[wr2] offset:1024\n" \
  "ds_read_b128 v[28:31], %[wr2] offset:2048\nds_read_b128 v[32:35], %[wr2] offset:3072\nds_read_b128 v[36:39], %[wr2] offset:4096\n.LY%=_0J0:\n" \
  "ds_read_b128 v[200:203], %[wr0] offset:0\nds_read_b128 v[204:207], %[wr0] offset:1024\nds_read_b128 v[208:211], %[wr0] offset:2048\n" \
  "ds_read_b128 v[212:215], %[wr0] offset:3072\nds_read_b128 v[216:219], %[wr0] offset:4096\nds_read_b128 v[220:223], %[wr0] offset:5120\n" \
  "ds_read_b128 v[224:227], %[wr0] offset:6144\nds_read_b128 v[228:231], %[wr0] offset:7168\nds_read_b128 v[232:235], %[wr0] offset:8192\n" \
  "ds_read_b128 v[236:239], %[wr0] offset:9216\ns_sub_u32 s48, s48, 1\ns_cmp_eq_u32 s48, 0\ns_cselect_b32 s49, s50, 0\nv_or_b32 v245, s49, %[hv]\n" \
  "v_mov_b32 v241, s46\n.LY%=_0T0:\ns_waitcnt vmcnt(9) lgkmcnt(5)\nv_cmp_eq_u32 vcc, -1, v8\ns_cbranch_vccnz .LY%=_0R0\n.LY%=_0B0:\n" \
  "v_mfma_f32_16x16x32_bf16 v[200:203], v[200:203], v[8:11], 0\ns_sub_u32 s52, s52, 110592\nv_mfma_f32_16x16x32_bf16 v[204:207], v[204:207], v[8:11], 0\n" \
  "v_mfma_f32_16x16x32_bf16 v[208:211], v[208:211], v[8:11], 0\nv_mfma_f32_16x16x32_bf16 v[212:215], v[212:215], v[8:11], 0\n" \
  "v_mfma_f32_16x16x32_bf16 v[216:219], v[216:219], v[8:11], 0\nbuffer_load_dwordx4 v[8:11], v245, %[rs], s52 offen sc1\n" \
  YTS(1) \
  ".LY%=_0T1:\ns_waitcnt vmcnt(9) lgkmcnt(0)\nv_cmp_eq_u32 vcc, -1, v12\ns_cbranch_vccnz .LY%=_0R1\n.LY%=_0B1:\n" \
  "v_mfma_f32_16x16x32_bf16 v[200:203], v[220:223], v[12:15], v[200:203]\ns_sub_u32 s53, s53, 110592\n" \
  "v_mfma_f32_16x16x32_bf16 v[204:207], v[224:227], v[12:15], v[204:207]\nv_mfma_f32_16x16x32_bf16 v[208:211], v[228:231], v[12:15], v[208:211]\n" \
  "v_mfma_f32_16x16x32_bf16 v[212:215], v[232:235], v[12:15], v[212:215]\nv_mfma_f32_16x16x32_bf16 v[216:219], v[236:239], v[12:15], v[216:219]\n" \
  "buffer_load_dwordx4 v[12:15], v245, %[rs], s53 offen sc1\nds_read_b32 v240, %[ga]\n.LY%=_0T2:\ns_waitcnt vmcnt(9)\nv_cmp_eq_u32 vcc, -1, v16\n" \
  "s_cbranch_vccnz .LY%=_0R2\n.LY%=_0B2:\nv_mfma_f32_16x16x32_bf16 v[200:203], v[20:23], v[16:19], v[200:203]\ns_sub_u32 s54, s54, 110592\n" \
  "v_mfma_f32_16x16x32_bf16 v[204:207], v[24:27], v[16:19], v[204:207]\nv_mfma_f32_16x16x32_bf16 v[208:211], v[28:31], v[16:19], v[208:211]\n" \
  "v_mfma_f32_16x16x32_bf16 v[212:215], v[32:35], v[16:19], v[212:215]\nv_mfma_f32_16x16x32_bf16 v[216:219], v[36:39], v[16:19], v[216:219]\n" \
  "buffer_load_dwordx4 v[16:19], v245, %[rs], s54 offen sc1\ns_setpc_b64 s[68:69]\n.LY%=_Q:\ns_add_u32 s68, s68, 44\ns_addc_u32 s69, s69, 0\n" \
  "s_cmp_ge_u32 s48, 2\ns_cselect_b32 s49, s51, s50\nbuffer_store_dwordx4 v[246:249], %[c0], %[rs], s49 offen sc1\n" \
  "buffer_store_dwordx4 v[246:249], %[c1], %[rs], s51 offen sc1\ns_sub_u32 s51, s51, 110592\n.LY%=_0S0:\ns_waitcnt lgkmcnt(0)\n" \
  "v_readfirstlane_b32 s49, v240\nds_read_b32 v240, %[ga]\ns_cmp_lt_i32 s49, s47\ns_cselect_b32 s49, %[bud], 0\ns_subb_u32 %[bud], %[bud], 0\n" \
  "s_cmp_gt_i32 s49, 0\ns_cbranch_scc1 .LY%=_0S0\ns_cmp_eq_u32 %[hi], 0\ns_cbranch_scc1 .LY%=_0S1\nds_read_b128 v[220:223], %[pw] offset:0\n" \
  "ds_read_b128 v[224:227], %[pw] offset:1024\nds_read_b128 v[228:231], %[pw] offset:2048\nds_read_b128 v[232:235], %[pw] offset:3072\n" \
  "ds_read_b128 v[236:239], %[pw] offset:4096\ns_waitcnt lgkmcnt(4)\nv_add_f32 v200, v200, v220\nv_add_f32 v201, v201, v221\nv_add_f32 v202, v202, v222\n" \
  "v_add_f32 v203, v203, v223\ns_waitcnt lgkmcnt(3)\nv_add_f32 v204, v204, v224\nv_add_f32 v205, v205, v225\nv_add_f32 v206, v206, v226\n" \
  "v_add_f32 v207, v207, v227\ns_waitcnt lgkmcnt(2)\nv_add_f32 v208, v208, v228\nv_add_f32 v209, v209, v229\nv_add_f32 v210, v210, v230\n" \
  "v_add_f32 v211, v211, v231\ns_waitcnt lgkmcnt(1)\nv_add_f32 v212, v212, v232\nv_add_f32 v213, v213, v233\nv_add_f32 v214, v214, v234\n" \
  "v_add_f32 v215, v215, v235\ns_waitcnt lgkmcnt(0)\nv_add_f32 v216, v216, v236\nv_add_f32 v217, v217, v237\nv_add_f32 v218, v218, v238\n" \
  "v_add_f32 v219, v219, v239\n.LY%=_0S1:\nds_write_b128 %[pw], v[200:203] offset:0\nds_write_b128 %[pw], v[204:207] offset:1024\n" \
  "ds_write_b128 %[pw], v[208:211] offset:2048\nds_write_b128 %[pw], v[212:215] offset:3072\nds_write_b128 %[pw], v[216:219] offset:4096\n" \
  "ds_write_b32 %[sa], v241\nv_xor_b32 %[pw], %[px], %[pw]\n" \
  YTS(2) \
  "s_add_u32 s46, s46, 1\ns_add_u32 s47, s47, 1\ns_cmp_lg_u32 s48, 0\ns_cbranch_scc1 .LY%=_0J0\n" \
  YTP(-1) \
  "s_add_u32 s48, %[one], 0\ns_add_u32 s49, %[ct], 0\ns_and_b32 s49, s49, 7\ns_lshl_b32 s52, s49, 10\ns_add_u32 s52, s52, %[a5]\n" \
  "s_cmp_ge_u32 s49, %[nb]\ns_cselect_b32 s52, s48, s52\nbuffer_load_dwordx4 v[8:11], %[hv], %[rs], s52 offen sc1\ns_cselect_b32 s60, 0, 110592\n" \
  "s_add_u32 s49, %[ct], 1\ns_and_b32 s49, s49, 7\ns_lshl_b32 s53, s49, 10\ns_add_u32 s53, s53, %[a5]\ns_cmp_ge_u32 s49, %[nb]\n" \
  "s_cselect_b32 s53, s48, s53\nbuffer_load_dwordx4 v[12:15], %[hv], %[rs], s53 offen sc1\ns_cselect_b32 s61, 0, 110592\ns_add_u32 s49, %[ct], 2\n" \
  "s_and_b32 s49, s49, 7\ns_lshl_b32 s54, s49, 10\ns_add_u32 s54, s54, %[a5]\ns_cmp_ge_u32 s49, %[nb]\ns_cselect_b32 s54, s48, s54\n" \
  "buffer_load_dwordx4 v[16:19], %[hv], %[rs], s54 offen sc1\ns_cselect_b32 s62, 0, 110592\ns_add_u32 s49, %[ct], 3\ns_and_b32 s49, s49, 7\n" \
  "s_lshl_b32 s55, s49, 10\ns_add_u32 s55, s55, %[a5]\ns_cmp_ge_u32 s49, %[nb]\ns_cselect_b32 s55, s48, s55\n" \
  "buffer_load_dwordx4 v[20:23], %[hv], %[rs], s55 offen sc1\ns_cselect_b32 s63, 0, 110592\ns_add_u32 s49, %[ct], 4\ns_and_b32 s49, s49, 7\n" \
  "s_lshl_b32 s56, s49, 10\ns_add_u32 s56, s56, %[a5]\ns_cmp_ge_u32 s49, %[nb]\ns_cselect_b32 s56, s48, s56\n" \
  "buffer_load_dwordx4 v[24:27], %[hv], %[rs], s56 offen sc1\ns_cselect_b32 s64, 0, 110592\ns_add_u32 s49, %[ct], 5\ns_and_b32 s49, s49, 7\n" \
  "s_lshl_b32 s57, s49, 10\ns_add_u32 s57, s57, %[a5]\ns_cmp_ge_u32 s49, %[nb]\ns_cselect_b32 s57, s48, s57\n" \
  "buffer_load_dwordx4 v[28:31], %[hv], %[rs], s57 offen sc1\ns_cselect_b32 s65, 0, 110592\ns_add_u32 s49, %[ct], 6\ns_and_b32 s49, s49, 7\n" \
  "s_lshl_b32 s58, s49, 10\ns_add_u32 s58, s58, %[a5]\ns_cmp_ge_u32 s49, %[nb]\ns_cselect_b32 s58, s48, s58\n" \
  "buffer_load_dwordx4 v[32:35], %[hv], %[rs], s58 offen sc1\ns_cselect_b32 s66, 0, 110592\ns_add_u32 s49, %[ct], 7\ns_and_b32 s49, s49, 7\n" \
  "s_lshl_b32 s59, s49, 10\ns_add_u32 s59, s59, %[a5]\ns_cmp_ge_u32 s49, %[nb]\ns_cselect_b32 s59, s48, s59\n" \
  "buffer_load_dwordx4 v[36:39], %[hv], %[rs], s59 offen sc1\ns_cselect_b32 s67, 0, 110592\ns_mov_b32 s48, 8\n.LY%=_1J0:\ns_sub_u32 s48, s48, 1\n" \
  "s_cmp_eq_u32 s48, 0\ns_cselect_b32 s49, s50, 0\nv_or_b32 v245, s49, %[hv]\nv_mov_b32 v241, s46\n.LY%=_1T0:\ns_waitcnt vmcnt(7)\n" \
  "v_cmp_gt_i32 vcc, 0, v8\ns_cbranch_vccnz .LY%=_1R0\n.LY%=_1B0:\nv_mfma_f32_16x16x32_bf16 v[200:203], v[40:43], v[8:11], 0\ns_sub_u32 s52, s52, s60\n" \
  "v_mfma_f32_16x16x32_bf16 v[204:207], v[44:47], v[8:11], 0\nv_mfma_f32_16x16x32_bf16 v[208:211], v[48:51], v[8:11], 0\n" \
  "v_mfma_f32_16x16x32_bf16 v[212:215], v[52:55], v[8:11], 0\nv_mfma_f32_16x16x32_bf16 v[216:219], v[56:59], v[8:11], 0\n" \
  "buffer_load_dwordx4 v[8:11], v245, %[rs], s52 offen sc1\n" \
  YTS(1) \
  ".LY%=_1T1:\ns_waitcnt vmcnt(7)\nv_cmp_gt_i32 vcc, 0, v12\ns_cbranch_vccnz .LY%=_1R1\n.LY%=_1B1:\n" \
  "v_mfma_f32_16x16x32_bf16 v[200:203], v[60:63], v[12:15], v[200:203]\ns_sub_u32 s53, s53, s61\n" \
  "v_mfma_f32_16x16x32_bf16 v[204:207], v[64:67], v[12:15], v[204:207]\nv_mfma_f32_16x16x32_bf16 v[208:211], v[68:71], v[12:15], v[208:211]\n" \
  "v_mfma_f32_16x16x32_bf16 v[212:215], v[72:75], v[12:15], v[212:215]\nv_mfma_f32_16x16x32_bf16 v[216:219], v[76:79], v[12:15], v[216:219]\n" \
  "buffer_load_dwordx4 v[12:15], v245, %[rs], s53 offen sc1\n.LY%=_1T2:\ns_waitcnt vmcnt(7)\nv_cmp_gt_i32 vcc, 0, v16\ns_cbranch_vccnz .LY%=_1R2\n" \
  ".LY%=_1B2:\nv_mfma_f32_16x16x32_bf16 v[200:203], v[80:83], v[16:19], v[200:203]\ns_sub_u32 s54, s54, s62\n" \
  "v_mfma_f32_16x16x32_bf16 v[204:207], v[84:87], v[16:19], v[204:207]\nv_mfma_f32_16x16x32_bf16 v[208:211], v[88:91], v[16:19], v[208:211]\n" \
  "v_mfma_f32_16x16x32_bf16 v[212:215], v[92:95], v[16:19], v[212:215]\nv_mfma_f32_16x16x32_bf16 v[216:219], v[96:99], v[16:19], v[216:219]\n" \
  "buffer_load_dwordx4 v[16:19], v245, %[rs], s54 offen sc1\n.LY%=_1T3:\ns_waitcnt vmcnt(7)\nv_cmp_gt_i32 vcc, 0, v20\ns_cbranch_vccnz .LY%=_1R3\n" \
  ".LY%=_1B3:\nv_mfma_f32_16x16x32_bf16 v[200:203], v[100:103], v[20:23], v[200:203]\ns_sub_u32 s55, s55, s63\n" \
  "v_mfma_f32_16x16x32_bf16 v[204:207], v[104:107], v[20:23], v[204:207]\nv_mfma_f32_16x16x32_bf16 v[208:211], v[108:111], v[20:23], v[208:211]\n" \
  "v_mfma_f32_16x16x32_bf16 v[212:215], v[112:115], v[20:23], v[212:215]\nv_mfma_f32_16x16x32_bf16 v[216:219], v[116:119], v[20:23], v[216:219]\n" \
  "buffer_load_dwordx4 v[20:23], v245, %[rs], s55 offen sc1\n.LY%=_1T4:\ns_waitcnt vmcnt(7)\nv_cmp_gt_i32 vcc, 0, v24\ns_cbranch_vccnz .LY%=_1R4\n" \
  ".LY%=_1B4:\nv_mfma_f32_16x16x32_bf16 v[200:203], v[120:123], v[24:27], v[200:203]\ns_sub_u32 s56, s56, s64\n" \
  "v_mfma_f32_16x16x32_bf16 v[204:207], v[124:127], v[24:27], v[204:207]\nv_mfma_f32_16x16x32_bf16 v[208:211], v[128:131], v[24:27], v[208:211]\n" \
  "v_mfma_f32_16x16x32_bf16 v[212:215], v[132:135], v[24:27], v[212:215]\nv_mfma_f32_16x16x32_bf16 v[216:219], v[136:139], v[24:27], v[216:219]\n" \
  "buffer_load_dwordx4 v[24:27], v245, %[rs], s56 offen sc1\n.LY%=_1T5:\ns_waitcnt vmcnt(7)\nv_cmp_gt_i32 vcc, 0, v28\ns_cbranch_vccnz .LY%=_1R5\n" \
  ".LY%=_1B5:\nv_mfma_f32_16x16x32_bf16 v[200:203], v[140:143], v[28:31], v[200:203]\ns_sub_u32 s57, s57, s65\n" \
  "v_mfma_f32_16x16x32_bf16 v[204:207], v[144:147], v[28:31], v[204:207]\nv_mfma_f32_16x16x32_bf16 v[208:211], v[148:151], v[28:31], v[208:211]\n" \
  "v_mfma_f32_16x16x32_bf16 v[212:215], v[152:155], v[28:31], v[212:215]\nv_mfma_f32_16x16x32_bf16 v[216:219], v[156:159], v[28:31], v[216:219]\n" \
  "buffer_load_dwordx4 v[28:31], v245, %[rs], s57 offen sc1\n.LY%=_1T6:\ns_waitcnt vmcnt(7)\nv_cmp_gt_i32 vcc, 0, v32\ns_cbranch_vccnz .LY%=_1R6\n" \
  ".LY%=_1B6:\nv_mfma_f32_16x16x32_bf16 v[200:203], v[160:163], v[32:35], v[200:203]\ns_sub_u32 s58, s58, s66\n" \
  "v_mfma_f32_16x16x32_bf16 v[204:207], v[164:167], v[32:35], v[204:207]\nv_mfma_f32_16x16x32_bf16 v[208:211], v[168:171], v[32:35], v[208:211]\n" \
  "v_mfma_f32_16x16x32_bf16 v[212:215], v[172:175], v[32:35], v[212:215]\nv_mfma_f32_16x16x32_bf16 v[216:219], v[176:179], v[32:35], v[216:219]\n" \
  "buffer_load_dwordx4 v[32:35], v245, %[rs], s58 offen sc1\nds_read_b32 v240, %[ga]\n.LY%=_1T7:\ns_waitcnt vmcnt(7)\nv_cmp_gt_i32 vcc, 0, v36\n" \
  "s_cbranch_vccnz .LY%=_1R7\n.LY%=_1B7:\nv_mfma_f32_16x16x32_bf16 v[200:203], v[180:183], v[36:39], v[200:203]\ns_sub_u32 s59, s59, s67\n" \
  "v_mfma_f32_16x16x32_bf16 v[204:207], v[184:187], v[36:39], v[204:207]\nv_mfma_f32_16x16x32_bf16 v[208:211], v[188:191], v[36:39], v[208:211]\n" \
  "v_mfma_f32_16x16x32_bf16 v[212:215], v[192:195], v[36:39], v[212:215]\nv_mfma_f32_16x16x32_bf16 v[216:219], v[196:199], v[36:39], v[216:219]\n" \
  "buffer_load_dwordx4 v[36:39], v245, %[rs], s59 offen sc1\n.LY%=_1S0:\ns_waitcnt lgkmcnt(0)\nv_readfirstlane_b32 s49, v240\nds_read_b32 v240, %[ga]\n" \
  "s_cmp_lt_i32 s49, s47\ns_cselect_b32 s49, %[bud], 0\ns_subb_u32 %[bud], %[bud], 0\ns_cmp_gt_i32 s49, 0\ns_cbranch_scc1 .LY%=_1S0\n" \
  "s_cmp_eq_u32 %[hi], 0\ns_cbranch_scc1 .LY%=_1S1\nds_read_b128 v[220:223], %[pw] offset:0\nds_read_b128 v[224:227], %[pw] offset:1024\n" \
  "ds_read_b128 v[228:231], %[pw] offset:2048\nds_read_b128 v[232:235], %[pw] offset:3072\nds_read_b128 v[236:239], %[pw] offset:4096\n" \
  "s_waitcnt lgkmcnt(4)\nv_add_f32 v200, v200, v220\nv_add_f32 v201, v201, v221\nv_add_f32 v202, v202, v222\nv_add_f32 v203, v203, v223\n" \
  "s_waitcnt lgkmcnt(3)\nv_add_f32 v204, v204, v224\nv_add_f32 v205, v205, v225\nv_add_f32 v206, v206, v226\nv_add_f32 v207, v207, v227\n" \
  "s_waitcnt lgkmcnt(2)\nv_add_f32 v208, v208, v228\nv_add_f32 v209, v209, v229\nv_add_f32 v210, v210, v230\nv_add_f32 v211, v211, v231\n" \
  "s_waitcnt lgkmcnt(1)\nv_add_f32 v212, v212, v232\nv_add_f32 v213, v213, v233\nv_add_f32 v214, v214, v234\nv_add_f32 v215, v215, v235\n" \
  "s_waitcnt lgkmcnt(0)\nv_add_f32 v216, v216, v236\nv_add_f32 v217, v217, v237\nv_add_f32 v218, v218, v238\nv_add_f32 v219, v219, v239\n.LY%=_1S1:\n" \
  "ds_write_b128 %[pw], v[200:203] offset:0\nds_write_b128 %[pw], v[204:207] offset:1024\nds_write_b128 %[pw], v[208:211] offset:2048\n" \
  "ds_write_b128 %[pw], v[212:215] offset:3072\nds_write_b128 %[pw], v[216:219] offset:4096\nds_write_b32 %[sa], v241\nv_xor_b32 %[pw], %[px], %[pw]\n" \
  YTS(2) \
  "s_add_u32 s46, s46, 1\ns_add_u32 s47, s47, 1\ns_cmp_lg_u32 s48, 0\ns_cbranch_scc1 .LY%=_1J0\n" \
  YTP(-1) \
  "s_add_u32 s52, %[a3], 1769472\ns_add_u32 s53, s52, 1024\ns_add_u32 s54, s52, %[d3]\nbuffer_load_dwordx4 v[8:11], %[hv], %[rs], s52 offen sc1\n" \
  "buffer_load_dwordx4 v[12:15], %[hv], %[rs], s53 offen sc1\nbuffer_load_dwordx4 v[16:19], %[hv], %[rs], s54 offen sc1\ns_mov_b32 s48, 8\n" \
  "s_waitcnt vmcnt(3)\nds_read_b128 v[20:23], %[wr2] offset:0\nds_read_b128 v[24:27], %[wr2] offset:1024\nds_read_b128 v[28:31], %[wr2] offset:2048\n" \
  "ds_read_b128 v[32:35], %[wr2] offset:3072\nds_read_b128 v[36:39], %[wr2] offset:4096\nv_bfrev_b32 v244, 1\nv_xor_b32 v40, v40, v244\n" \
  "v_xor_b32 v44, v44, v244\nv_xor_b32 v48, v48, v244\nv_xor_b32 v52, v52, v244\nv_xor_b32 v56, v56, v244\nv_xor_b32 v60, v60, v244\n" \
  "v_xor_b32 v64, v64, v244\nv_xor_b32 v68, v68, v244\nv_xor_b32 v72, v72, v244\nv_xor_b32 v76, v76, v244\nv_xor_b32 v80, v80, v244\n" \
  "v_xor_b32 v84, v84, v244\nv_xor_b32 v88, v88, v244\nv_xor_b32 v92, v92, v244\nv_xor_b32 v96, v96, v244\nv_xor_b32 v100, v100, v244\n" \
  "v_xor_b32 v104, v104, v244\nv_xor_b32 v108, v108, v244\nv_xor_b32 v112, v112, v244\nv_xor_b32 v116, v116, v244\nv_xor_b32 v120, v120, v244\n" \
  "v_xor_b32 v124, v124, v244\nv_xor_b32 v128, v128, v244\nv_xor_b32 v132, v132, v244\nv_xor_b32 v136, v136, v244\nv_xor_b32 v140, v140, v244\n" \
  "v_xor_b32 v144, v144, v244\nv_xor_b32 v148, v148, v244\nv_xor_b32 v152, v152, v244\nv_xor_b32 v156, v156, v244\nv_xor_b32 v160, v160, v244\n" \
  "v_xor_b32 v164, v164, v244\nv_xor_b32 v168, v168, v244\nv_xor_b32 v172, v172, v244\nv_xor_b32 v176, v176, v244\nv_xor_b32 v180, v180, v244\n" \
  "v_xor_b32 v184, v184, v244\nv_xor_b32 v188, v188, v244\nv_xor_b32 v192, v192, v244\nv_xor_b32 v196, v196, v244\n.LY%=_2J0:\n" \
  "ds_read_b128 v[200:203], %[wr0] offset:0\nds_read_b128 v[204:207], %[wr0] offset:1024\nds_read_b128 v[208:211], %[wr0] offset:2048\n" \
  "ds_read_b128 v[212:215], %[wr0] offset:3072\nds_read_b128 v[216:219], %[wr0] offset:4096\nds_read_b128 v[220:223], %[wr0] offset:5120\n" \
  "ds_read_b128 v[224:227], %[wr0] offset:6144\nds_read_b128 v[228:231], %[wr0] offset:7168\nds_read_b128 v[232:235], %[wr0] offset:8192\n" \
  "ds_read_b128 v[236:239], %[wr0] offset:9216\ns_sub_u32 s48, s48, 1\ns_cmp_eq_u32 s48, 0\ns_cselect_b32 s49, s50, 0\nv_or_b32 v245, s49, %[hv]\n" \
  "v_mov_b32 v241, s46\n.LY%=_2T0:\ns_waitcnt vmcnt(2) lgkmcnt(5)\nv_cmp_eq_u32 vcc, -1, v8\ns_cbranch_vccnz .LY%=_2R0\n.LY%=_2B0:\n" \
  "v_mfma_f32_16x16x32_bf16 v[200:203], v[200:203], v[8:11], 0\ns_sub_u32 s52, s52, 110592\nv_mfma_f32_16x16x32_bf16 v[204:207], v[204:207], v[8:11], 0\n" \
  "v_mfma_f32_16x16x32_bf16 v[208:211], v[208:211], v[8:11], 0\nv_mfma_f32_16x16x32_bf16 v[212:215], v[212:215], v[8:11], 0\n" \
  "v_mfma_f32_16x16x32_bf16 v[216:219], v[216:219], v[8:11], 0\nbuffer_load_dwordx4 v[8:11], v245, %[rs], s52 offen sc1\n" \
  YTS(1) \
  ".LY%=_2T1:\ns_waitcnt vmcnt(2) lgkmcnt(0)\nv_cmp_eq_u32 vcc, -1, v12\ns_cbranch_vccnz .LY%=_2R1\n.LY%=_2B1:\n" \
  "v_mfma_f32_16x16x32_bf16 v[200:203], v[220:223], v[12:15], v[200:203]\ns_sub_u32 s53, s53, 110592\n" \
  "v_mfma_f32_16x16x32_bf16 v[204:207], v[224:227], v[12:15], v[204:207]\nv_mfma_f32_16x16x32_bf16 v[208:211], v[228:231], v[12:15], v[208:211]\n" \
  "v_mfma_f32_16x16x32_bf16 v[212:215], v[232:235], v[12:15], v[212:215]\nv_mfma_f32_16x16x32_bf16 v[216:219], v[236:239], v[12:15], v[216:219]\n" \
  "buffer_load_dwordx4 v[12:15], v245, %[rs], s53 offen sc1\nds_read_b32 v240, %[ga]\n.LY%=_2T2:\ns_waitcnt vmcnt(2)\nv_cmp_eq_u32 vcc, -1, v16\n" \
  "s_cbranch_vccnz .LY%=_2R2\n.LY%=_2B2:\nv_mfma_f32_16x16x32_bf16 v[200:203], v[20:23], v[16:19], v[200:203]\ns_sub_u32 s54, s54, 110592\n" \
  "v_mfma_f32_16x16x32_bf16 v[204:207], v[24:27], v[16:19], v[204:207]\nv_mfma_f32_16x16x32_bf16 v[208:211], v[28:31], v[16:19], v[208:211]\n" \
  "v_mfma_f32_16x16x32_bf16 v[212:215], v[32:35], v[16:19], v[212:215]\nv_mfma_f32_16x16x32_bf16 v[216:219], v[36:39], v[16:19], v[216:219]\n" \
  "buffer_load_dwordx4 v[16:19], v245, %[rs], s54 offen sc1\n.LY%=_2S0:\ns_waitcnt lgkmcnt(0)\nv_readfirstlane_b32 s49, v240\nds_read_b32 v240, %[ga]\n" \
  "s_cmp_lt_i32 s49, s47\ns_cselect_b32 s49, %[bud], 0\ns_subb_u32 %[bud], %[bud], 0\ns_cmp_gt_i32 s49, 0\ns_cbranch_scc1 .LY%=_2S0\n" \
  "s_cmp_eq_u32 %[hi], 0\ns_cbranch_scc1 .LY%=_2S1\nds_read_b128 v[220:223], %[pw] offset:0\nds_read_b128 v[224:227], %[pw] offset:1024\n" \
  "ds_read_b128 v[228:231], %[pw] offset:2048\nds_read_b128 v[232:235], %[pw] offset:3072\nds_read_b128 v[236:239], %[pw] offset:4096\n" \
  "s_waitcnt lgkmcnt(4)\nv_add_f32 v200, v200, v220\nv_add_f32 v201, v201, v221\nv_add_f32 v202, v202, v222\nv_add_f32 v203, v203, v223\n" \
  "s_waitcnt lgkmcnt(3)\nv_add_f32 v204, v204, v224\nv_add_f32 v205, v205, v225\nv_add_f32 v206, v206, v226\nv_add_f32 v207, v207, v227\n" \
  "s_waitcnt lgkmcnt(2)\nv_add_f32 v208, v208, v228\nv_add_f32 v209, v209, v229\nv_add_f32 v210, v210, v230\nv_add_f32 v211, v211, v231\n" \
  "s_waitcnt lgkmcnt(1)\nv_add_f32 v212, v212, v232\nv_add_f32 v213, v213, v233\nv_add_f32 v214, v214, v234\nv_add_f32 v215, v215, v235\n" \
  "s_waitcnt lgkmcnt(0)\nv_add_f32 v216, v216, v236\nv_add_f32 v217, v217, v237\nv_add_f32 v218, v218, v238\nv_add_f32 v219, v219, v239\n.LY%=_2S1:\n" \
  "ds_write_b128 %[pw], v[200:203] offset:0\nds_write_b128 %[pw], v[204:207] offset:1024\nds_write_b128 %[pw], v[208:211] offset:2048\n" \
  "ds_write_b128 %[pw], v[212:215] offset:3072\nds_write_b128 %[pw], v[216:219] offset:4096\nds_write_b32 %[sa], v241\nv_xor_b32 %[pw], %[px], %[pw]\n" \
  YTS(2) \
  "s_add_u32 s46, s46, 1\ns_add_u32 s47, s47, 1\ns_cmp_lg_u32 s48, 0\ns_cbranch_scc1 .LY%=_2J0\n" \
  YTP(-1) \
  "s_add_u32 s48, %[one], 1024\ns_add_u32 s49, %[ct], 0\ns_and_b32 s49, s49, 7\ns_lshl_b32 s52, s49, 10\ns_add_u32 s52, s52, %[a5]\n" \
  "s_cmp_ge_u32 s49, %[nb]\ns_cselect_b32 s52, s48, s52\nbuffer_load_dwordx4 v[8:11], %[hv], %[rs], s52 offen sc1\ns_add_u32 s49, %[ct], 1\n" \
  "s_and_b32 s49, s49, 7\ns_lshl_b32 s53, s49, 10\ns_add_u32 s53, s53, %[a5]\ns_cmp_ge_u32 s49, %[nb]\ns_cselect_b32 s53, s48, s53\n" \
  "buffer_load_dwordx4 v[12:15], %[hv], %[rs], s53 offen sc1\ns_add_u32 s49, %[ct], 2\ns_and_b32 s49, s49, 7\ns_lshl_b32 s54, s49, 10\n" \
  "s_add_u32 s54, s54, %[a5]\ns_cmp_ge_u32 s49, %[nb]\ns_cselect_b32 s54, s48, s54\nbuffer_load_dwordx4 v[16:19], %[hv], %[rs], s54 offen sc1\n" \
  "s_add_u32 s49, %[ct], 3\ns_and_b32 s49, s49, 7\ns_lshl_b32 s55, s49, 10\ns_add_u32 s55, s55, %[a5]\ns_cmp_ge_u32 s49, %[nb]\n" \
  "s_cselect_b32 s55, s48, s55\nbuffer_load_dwordx4 v[20:23], %[hv], %[rs], s55 offen sc1\ns_add_u32 s49, %[ct], 4\ns_and_b32 s49, s49, 7\n" \
  "s_lshl_b32 s56, s49, 10\ns_add_u32 s56, s56, %[a5]\ns_cmp_ge_u32 s49, %[nb]\ns_cselect_b32 s56, s48, s56\n" \
  "buffer_load_dwordx4 v[24:27], %[hv], %[rs], s56 offen sc1\ns_add_u32 s49, %[ct], 5\ns_and_b32 s49, s49, 7\ns_lshl_b32 s57, s49, 10\n" \
  "s_add_u32 s57, s57, %[a5]\ns_cmp_ge_u32 s49, %[nb]\ns_cselect_b32 s57, s48, s57\nbuffer_load_dwordx4 v[28:31], %[hv], %[rs], s57 offen sc1\n" \
  "s_add_u32 s49, %[ct], 6\ns_and_b32 s49, s49, 7\ns_lshl_b32 s58, s49, 10\ns_add_u32 s58, s58, %[a5]\ns_cmp_ge_u32 s49, %[nb]\n" \
  "s_cselect_b32 s58, s48, s58\nbuffer_load_dwordx4 v[32:35], %[hv], %[rs], s58 offen sc1\ns_add_u32 s49, %[ct], 7\ns_and_b32 s49, s49, 7\n" \
  "s_lshl_b32 s59, s49, 10\ns_add_u32 s59, s59, %[a5]\ns_cmp_ge_u32 s49, %[nb]\ns_cselect_b32 s59, s48, s59\n" \
  "buffer_load_dwordx4 v[36:39], %[hv], %[rs], s59 offen sc1\ns_mov_b32 s48, 8\n.LY%=_3J0:\ns_sub_u32 s48, s48, 1\ns_cmp_eq_u32 s48, 0\n" \
  "s_cselect_b32 s49, s50, 0\nv_or_b32 v245, s49, %[hv]\nv_mov_b32 v241, s46\n.LY%=_3T0:\ns_waitcnt vmcnt(7)\nv_cmp_lt_i32 vcc, -1, v8\n" \
  "s_cbranch_vccnz .LY%=_3R0\n.LY%=_3B0:\nv_mfma_f32_16x16x32_bf16 v[200:203], v[40:43], v[8:11], 0\ns_sub_u32 s52, s52, s60\n" \
  "v_mfma_f32_16x16x32_bf16 v[204:207], v[44:47], v[8:11], 0\nv_mfma_f32_16x16x32_bf16 v[208:211], v[48:51], v[8:11], 0\n" \
  "v_mfma_f32_16x16x32_bf16 v[212:215], v[52:55], v[8:11], 0\nv_mfma_f32_16x16x32_bf16 v[216:219], v[56:59], v[8:11], 0\n" \
  "buffer_load_dwordx4 v[8:11], v245, %[rs], s52 offen sc1\n" \
  YTS(1) \
  ".LY%=_3T1:\ns_waitcnt vmcnt(7)\nv_cmp_lt_i32 vcc, -1, v12\ns_cbranch_vccnz .LY%=_3R1\n.LY%=_3B1:\n" \
  "v_mfma_f32_16x16x32_bf16 v[200:203], v[60:63], v[12:15], v[200:203]\ns_sub_u32 s53, s53, s61\n" \
  "v_mfma_f32_16x16x32_bf16 v[204:207], v[64:67], v[12:15], v[204:207]\nv_mfma_f32_16x16x32_bf16 v[208:211], v[68:71], v[12:15], v[208:211]\n" \
  "v_mfma_f32_16x16x32_bf16 v[212:215], v[72:75], v[12:15], v[212:215]\nv_mfma_f32_16x16x32_bf16 v[216:219], v[76:79], v[12:15], v[216:219]\n" \
  "buffer_load_dwordx4 v[12:15], v245, %[rs], s53 offen sc1\n.LY%=_3T2:\ns_waitcnt vmcnt(7)\nv_cmp_lt_i32 vcc, -1, v16\ns_cbranch_vccnz .LY%=_3R2\n" \
  ".LY%=_3B2:\nv_mfma_f32_16x16x32_bf16 v[200:203], v[80:83], v[16:19], v[200:203]\ns_sub_u32 s54, s54, s62\n" \
  "v_mfma_f32_16x16x32_bf16 v[204:207], v[84:87], v[16:19], v[204:207]\nv_mfma_f32_16x16x32_bf16 v[208:211], v[88:91], v[16:19], v[208:211]\n" \
  "v_mfma_f32_16x16x32_bf16 v[212:215], v[92:95], v[16:19], v[212:215]\nv_mfma_f32_16x16x32_bf16 v[216:219], v[96:99], v[16:19], v[216:219]\n" \
  "buffer_load_dwordx4 v[16:19], v245, %[rs], s54 offen sc1\n.LY%=_3T3:\ns_waitcnt vmcnt(7)\nv_cmp_lt_i32 vcc, -1, v20\ns_cbranch_vccnz .LY%=_3R3\n" \
  ".LY%=_3B3:\nv_mfma_f32_16x16x32_bf16 v[200:203], v[100:103], v[20:23], v[200:203]\ns_sub_u32 s55, s55, s63\n" \
  "v_mfma_f32_16x16x32_bf16 v[204:207], v[104:107], v[20:23], v[204:207]\nv_mfma_f32_16x16x32_bf16 v[208:211], v[108:111], v[20:23], v[208:211]\n" \
  "v_mfma_f32_16x16x32_bf16 v[212:215], v[112:115], v[20:23], v[212:215]\nv_mfma_f32_16x16x32_bf16 v[216:219], v[116:119], v[20:23], v[216:219]\n" \
  "buffer_load_dwordx4 v[20:23], v245, %[rs], s55 offen sc1\n.LY%=_3T4:\ns_waitcnt vmcnt(7)\nv_cmp_lt_i32 vcc, -1, v24\ns_cbranch_vccnz .LY%=_3R4\n" \
  ".LY%=_3B4:\nv_mfma_f32_16x16x32_bf16 v[200:203], v[120:123], v[24:27], v[200:203]\ns_sub_u32 s56, s56, s64\n" \
  "v_mfma_f32_16x16x32_bf16 v[204:207], v[124:127], v[24:27], v[204:207]\nv_mfma_f32_16x16x32_bf16 v[208:211], v[128:131], v[24:27], v[208:211]\n" \
  "v_mfma_f32_16x16x32_bf16 v[212:215], v[132:135], v[24:27], v[212:215]\nv_mfma_f32_16x16x32_bf16 v[216:219], v[136:139], v[24:27], v[216:219]\n" \
  "buffer_load_dwordx4 v[24:27], v245, %[rs], s56 offen sc1\n.LY%=_3T5:\ns_waitcnt vmcnt(7)\nv_cmp_lt_i32 vcc, -1, v28\ns_cbranch_vccnz .LY%=_3R5\n" \
  ".LY%=_3B5:\nv_mfma_f32_16x16x32_bf16 v[200:203], v[140:143], v[28:31], v[200:203]\ns_sub_u32 s57, s57, s65\n" \
  "v_mfma_f32_16x16x32_bf16 v[204:207], v[144:147], v[28:31], v[204:207]\nv_mfma_f32_16x16x32_bf16 v[208:211], v[148:151], v[28:31], v[208:211]\n" \
  "v_mfma_f32_16x16x32_bf16 v[212:215], v[152:155], v[28:31], v[212:215]\nv_mfma_f32_16x16x32_bf16 v[216:219], v[156:159], v[28:31], v[216:219]\n" \
  "buffer_load_dwordx4 v[28:31], v245, %[rs], s57 offen sc1\n.LY%=_3T6:\ns_waitcnt vmcnt(7)\nv_cmp_lt_i32 vcc, -1, v32\ns_cbranch_vccnz .LY%=_3R6\n" \
  ".LY%=_3B6:\nv_mfma_f32_16x16x32_bf16 v[200:203], v[160:163], v[32:35], v[200:203]\ns_sub_u32 s58, s58, s66\n" \
  "v_mfma_f32_16x16x32_bf16 v[204:207], v[164:167], v[32:35], v[204:207]\nv_mfma_f32_16x16x32_bf16 v[208:211], v[168:171], v[32:35], v[208:211]\n" \
  "v_mfma_f32_16x16x32_bf16 v[212:215], v[172:175], v[32:35], v[212:215]\nv_mfma_f32_16x16x32_bf16 v[216:219], v[176:179], v[32:35], v[216:219]\n" \
  "buffer_load_dwordx4 v[32:35], v245, %[rs], s58 offen sc1\nds_read_b32 v240, %[ga]\n.LY%=_3T7:\ns_waitcnt vmcnt(7)\nv_cmp_lt_i32 vcc, -1, v36\n" \
  "s_cbranch_vccnz .LY%=_3R7\n.LY%=_3B7:\nv_mfma_f32_16x16x32_bf16 v[200:203], v[180:183], v[36:39], v[200:203]\ns_sub_u32 s59, s59, s67\n" \
  "v_mfma_f32_16x16x32_bf16 v[204:207], v[184:187], v[36:39], v[204:207]\nv_mfma_f32_16x16x32_bf16 v[208:211], v[188:191], v[36:39], v[208:211]\n" \
  "v_mfma_f32_16x16x32_bf16 v[212:215], v[192:195], v[36:39], v[212:215]\nv_mfma_f32_16x16x32_bf16 v[216:219], v[196:199], v[36:39], v[216:219]\n" \
  "buffer_load_dwordx4 v[36:39], v245, %[rs], s59 offen sc1\n.LY%=_3S0:\ns_waitcnt lgkmcnt(0)\nv_readfirstlane_b32 s49, v240\nds_read_b32 v240, %[ga]\n" \
  "s_cmp_lt_i32 s49, s47\ns_cselect_b32 s49, %[bud], 0\ns_subb_u32 %[bud], %[bud], 0\ns_cmp_gt_i32 s49, 0\ns_cbranch_scc1 .LY%=_3S0\n" \
  "s_cmp_eq_u32 %[hi], 0\ns_cbranch_scc1 .LY%=_3S1\nds_read_b128 v[220:223], %[pw] offset:0\nds_read_b128 v[224:227], %[pw] offset:1024\n" \
  "ds_read_b128 v[228:231], %[pw] offset:2048\nds_read_b128 v[232:235], %[pw] offset:3072\nds_read_b128 v[236:239], %[pw] offset:4096\n" \
  "s_waitcnt lgkmcnt(4)\nv_add_f32 v200, v200, v220\nv_add_f32 v201, v201, v221\nv_add_f32 v202, v202, v222\nv_add_f32 v203, v203, v223\n" \
  "s_waitcnt lgkmcnt(3)\nv_add_f32 v204, v204, v224\nv_add_f32 v205, v205, v225\nv_add_f32 v206, v206, v226\nv_add_f32 v207, v207, v227\n" \
  "s_waitcnt lgkmcnt(2)\nv_add_f32 v208, v208, v228\nv_add_f32 v209, v209, v229\nv_add_f32 v210, v210, v230\nv_add_f32 v211, v211, v231\n" \
  "s_waitcnt lgkmcnt(1)\nv_add_f32 v212, v212, v232\nv_add_f32 v213, v213, v233\nv_add_f32 v214, v214, v234\nv_add_f32 v215, v215, v235\n" \
  "s_waitcnt lgkmcnt(0)\nv_add_f32 v216, v216, v236\nv_add_f32 v217, v217, v237\nv_add_f32 v218, v218, v238\nv_add_f32 v219, v219, v239\n.LY%=_3S1:\n" \
  "ds_write_b128 %[pw], v[200:203] offset:0\nds_write_b128 %[pw], v[204:207] offset:1024\nds_write_b128 %[pw], v[208:211] offset:2048\n" \
  "ds_write_b128 %[pw], v[212:215] offset:3072\nds_write_b128 %[pw], v[216:219] offset:4096\nds_write_b32 %[sa], v241\nv_xor_b32 %[pw], %[px], %[pw]\n" \
  YTS(2) \
  "s_add_u32 s46, s46, 1\ns_add_u32 s47, s47, 1\ns_cmp_lg_u32 s48, 0\ns_cbranch_scc1 .LY%=_3J0\n" \
  YTP(-1) \
  "s_branch .LY%=_E\n.LY%=_0R0:\ns_sub_u32 %[bud], %[bud], 4\ns_cmp_gt_i32 %[bud], 0\ns_cbranch_scc0 .LY%=_0B0\ns_sleep 1\n" \
  "buffer_load_dwordx4 v[8:11], %[hv], %[rs], s52 offen sc1\nbuffer_load_dwordx4 v[12:15], %[hv], %[rs], s53 offen sc1\n" \
  "buffer_load_dwordx4 v[16:19], %[hv], %[rs], s54 offen sc1\nbuffer_load_dword v244, %[hv], %[rs], s50 offen\n" \
  "buffer_load_dword v244, %[hv], %[rs], s50 offen\nbuffer_load_dword v244, %[hv], %[rs], s50 offen\nbuffer_load_dword v244, %[hv], %[rs], s50 offen\n" \
  "buffer_load_dword v244, %[hv], %[rs], s50 offen\nbuffer_load_dword v244, %[hv], %[rs], s50 offen\nbuffer_load_dword v244, %[hv], %[rs], s50 offen\n" \
  "s_branch .LY%=_0T0\n.LY%=_0R1:\ns_sub_u32 %[bud], %[bud], 4\ns_cmp_gt_i32 %[bud], 0\ns_cbranch_scc0 .LY%=_0B1\ns_sleep 1\n" \
  "buffer_load_dwordx4 v[12:15], %[hv], %[rs], s53 offen sc1\nbuffer_load_dwordx4 v[16:19], %[hv], %[rs], s54 offen sc1\n" \
  "buffer_load_dword v244, %[hv], %[rs], s50 offen\nbuffer_load_dword v244, %[hv], %[rs], s50 offen\nbuffer_load_dword v244, %[hv], %[rs], s50 offen\n" \
  "buffer_load_dword v244, %[hv], %[rs], s50 offen\nbuffer_load_dword v244, %[hv], %[rs], s50 offen\nbuffer_load_dword v244, %[hv], %[rs], s50 offen\n" \
  "buffer_load_dword v244, %[hv], %[rs], s50 offen\nbuffer_load_dword v244, %[hv], %[rs], s50 offen\ns_branch .LY%=_0T1\n.LY%=_0R2:\n" \
  "s_sub_u32 %[bud], %[bud], 4\ns_cmp_gt_i32 %[bud], 0\ns_cbranch_scc0 .LY%=_0B2\ns_sleep 1\nbuffer_load_dwordx4 v[16:19], %[hv], %[rs], s54 offen sc1\n" \
  "buffer_load_dword v244, %[hv], %[rs], s50 offen\nbuffer_load_dword v244, %[hv], %[rs], s50 offen\nbuffer_load_dword v244, %[hv], %[rs], s50 offen\n" \
  "buffer_load_dword v244, %[hv], %[rs], s50 offen\nbuffer_load_dword v244, %[hv], %[rs], s50 offen\nbuffer_load_dword v244, %[hv], %[rs], s50 offen\n" \
  "buffer_load_dword v244, %[hv], %[rs], s50 offen\nbuffer_load_dword v244, %[hv], %[rs], s50 offen\nbuffer_load_dword v244, %[hv], %[rs], s50 offen\n" \
  "s_branch .LY%=_0T2\n.LY%=_W:\nbuffer_load_dwordx4 v[120:123], %[vo], %[rs], s64 offen offset:0 sc1\n" \
  "buffer_load_dwordx4 v[124:127], %[vo], %[rs], s64 offen offset:256 sc1\nbuffer_load_dwordx4 v[128:131], %[vo], %[rs], s64 offen offset:512 sc1\n" \
  "buffer_load_dwordx4 v[132:135], %[vo], %[rs], s64 offen offset:768 sc1\nbuffer_load_dwordx4 v[136:139], %[vo], %[rs], s64 offen offset:1024 sc1\n" \
  "s_branch .LY%=_Q\nbuffer_load_dwordx4 v[140:143], %[vo], %[rs], s65 offen offset:0 sc1\n" \
  "buffer_load_dwordx4 v[144:147], %[vo], %[rs], s65 offen offset:256 sc1\nbuffer_load_dwordx4 v[148:151], %[vo], %[rs], s65 offen offset:512 sc1\n" \
  "buffer_load_dwordx4 v[152:155], %[vo], %[rs], s65 offen offset:768 sc1\nbuffer_load_dwordx4 v[156:159], %[vo], %[rs], s65 offen offset:1024 sc1\n" \
  "s_branch .LY%=_Q\nbuffer_load_dwordx4 v[160:163], %[vo], %[rs], s66 offen offset:0 sc1\n" \
  "buffer_load_dwordx4 v[164:167], %[vo], %[rs], s66 offen offset:256 sc1\nbuffer_load_dwordx4 v[168:171], %[vo], %[rs], s66 offen offset:512 sc1\n" \
  "buffer_load_dwordx4 v[172:175], %[vo], %[rs], s66 offen offset:768 sc1\nbuffer_load_dwordx4 v[176:179], %[vo], %[rs], s66 offen offset:1024 sc1\n" \
  "s_branch .LY%=_Q\nbuffer_load_dwordx4 v[180:183], %[vo], %[rs], s67 offen offset:0 sc1\n" \
  "buffer_load_dwordx4 v[184:187], %[vo], %[rs], s67 offen offset:256 sc1\nbuffer_load_dwordx4 v[188:191], %[vo], %[rs], s67 offen offset:512 sc1\n" \
  "buffer_load_dwordx4 v[192:195], %[vo], %[rs], s67 offen offset:768 sc1\nbuffer_load_dwordx4 v[196:199], %[vo], %[rs], s67 offen offset:1024 sc1\n" \
  "s_branch .LY%=_Q\nbuffer_load_dword v244, %[hv], %[rs], s50 offen\nbuffer_load_dword v244, %[hv], %[rs], s50 offen\n" \
  "buffer_load_dword v244, %[hv], %[rs], s50 offen\nbuffer_load_dword v244, %[hv], %[rs], s50 offen\nbuffer_load_dword v244, %[hv], %[rs], s50 offen\n" \
  "s_branch .LY%=_Q\nbuffer_load_dword v244, %[hv], %[rs], s50 offen\nbuffer_load_dword v244, %[hv], %[rs], s50 offen\n" \
  "buffer_load_dword v244, %[hv], %[rs], s50 offen\nbuffer_load_dword v244, %[hv], %[rs], s50 offen\nbuffer_load_dword v244, %[hv], %[rs], s50 offen\n" \
  "s_branch .LY%=_Q\nbuffer_load_dword v244, %[hv], %[rs], s50 offen\nbuffer_load_dword v244, %[hv], %[rs], s50 offen\n" \
  "buffer_load_dword v244, %[hv], %[rs], s50 offen\nbuffer_load_dword v244, %[hv], %[rs], s50 offen\nbuffer_load_dword v244, %[hv], %[rs], s50 offen\n" \
  "s_branch .LY%=_Q\nbuffer_load_dword v244, %[hv], %[rs], s50 offen\nbuffer_load_dword v244, %[hv], %[rs], s50 offen\n" \
  "buffer_load_dword v244, %[hv], %[rs], s50 offen\nbuffer_load_dword v244, %[hv], %[rs], s50 offen\nbuffer_load_dword v244, %[hv], %[rs], s50 offen\n" \
  "s_branch .LY%=_Q\n.LY%=_1R0:\ns_sub_u32 %[bud], %[bud], 4\ns_cmp_gt_i32 %[bud], 0\ns_cbranch_scc0 .LY%=_1B0\ns_sleep 1\n" \
  "buffer_load_dwordx4 v[8:11], %[hv], %[rs], s52 offen sc1\ns_waitcnt vmcnt(0)\nv_cmp_gt_i32 vcc, 0, v8\ns_cbranch_vccnz .LY%=_1R0\n" \
  "v_cmp_gt_i32 s[68:69], 0, v12\nv_cmp_gt_i32 s[70:71], 0, v16\nv_cmp_gt_i32 s[72:73], 0, v20\nv_cmp_gt_i32 s[74:75], 0, v24\n" \
  "v_cmp_gt_i32 s[76:77], 0, v28\nv_cmp_gt_i32 s[78:79], 0, v32\nv_cmp_gt_i32 s[80:81], 0, v36\ns_or_b64 exec, s[68:69], 1\n" \
  "buffer_load_dwordx4 v[12:15], %[hv], %[rs], s53 offen sc1\ns_or_b64 exec, s[70:71], 1\nbuffer_load_dwordx4 v[16:19], %[hv], %[rs], s54 offen sc1\n" \
  "s_or_b64 exec, s[72:73], 1\nbuffer_load_dwordx4 v[20:23], %[hv], %[rs], s55 offen sc1\ns_or_b64 exec, s[74:75], 1\n" \
  "buffer_load_dwordx4 v[24:27], %[hv], %[rs], s56 offen sc1\ns_or_b64 exec, s[76:77], 1\nbuffer_load_dwordx4 v[28:31], %[hv], %[rs], s57 offen sc1\n" \
  "s_or_b64 exec, s[78:79], 1\nbuffer_load_dwordx4 v[32:35], %[hv], %[rs], s58 offen sc1\ns_or_b64 exec, s[80:81], 1\n" \
  "buffer_load_dwordx4 v[36:39], %[hv], %[rs], s59 offen sc1\ns_mov_b64 exec, -1\ns_branch .LY%=_1B0\n.LY%=_1R1:\ns_sub_u32 %[bud], %[bud], 4\n" \
  "s_cmp_gt_i32 %[bud], 0\ns_cbranch_scc0 .LY%=_1B1\ns_sleep 1\nbuffer_load_dwordx4 v[12:15], %[hv], %[rs], s53 offen sc1\ns_waitcnt vmcnt(0)\n" \
  "v_cmp_gt_i32 vcc, 0, v12\ns_cbranch_vccnz .LY%=_1R1\nv_cmp_gt_i32 s[70:71], 0, v16\nv_cmp_gt_i32 s[72:73], 0, v20\nv_cmp_gt_i32 s[74:75], 0, v24\n" \
  "v_cmp_gt_i32 s[76:77], 0, v28\nv_cmp_gt_i32 s[78:79], 0, v32\nv_cmp_gt_i32 s[80:81], 0, v36\ns_or_b64 exec, s[70:71], 1\n" \
  "buffer_load_dwordx4 v[16:19], %[hv], %[rs], s54 offen sc1\ns_or_b64 exec, s[72:73], 1\nbuffer_load_dwordx4 v[20:23], %[hv], %[rs], s55 offen sc1\n" \
  "s_or_b64 exec, s[74:75], 1\nbuffer_load_dwordx4 v[24:27], %[hv], %[rs], s56 offen sc1\ns_or_b64 exec, s[76:77], 1\n" \
  "buffer_load_dwordx4 v[28:31], %[hv], %[rs], s57 offen sc1\ns_or_b64 exec, s[78:79], 1\nbuffer_load_dwordx4 v[32:35], %[hv], %[rs], s58 offen sc1\n" \
  "s_or_b64 exec, s[80:81], 1\nbuffer_load_dwordx4 v[36:39], %[hv], %[rs], s59 offen sc1\ns_mov_b64 exec, -1\n" \
  "buffer_load_dword v244, %[hv], %[rs], s50 offen\ns_branch .LY%=_1B1\n.LY%=_1R2:\ns_sub_u32 %[bud], %[bud], 4\ns_cmp_gt_i32 %[bud], 0\n" \
  "s_cbranch_scc0 .LY%=_1B2\ns_sleep 1\nbuffer_load_dwordx4 v[16:19], %[hv], %[rs], s54 offen sc1\ns_waitcnt vmcnt(0)\nv_cmp_gt_i32 vcc, 0, v16\n" \
  "s_cbranch_vccnz .LY%=_1R2\nv_cmp_gt_i32 s[72:73], 0, v20\nv_cmp_gt_i32 s[74:75], 0, v24\nv_cmp_gt_i32 s[76:77], 0, v28\n" \
  "v_cmp_gt_i32 s[78:79], 0, v32\nv_cmp_gt_i32 s[80:81], 0, v36\ns_or_b64 exec, s[72:73], 1\nbuffer_load_dwordx4 v[20:23], %[hv], %[rs], s55 offen sc1\n" \
  "s_or_b64 exec, s[74:75], 1\nbuffer_load_dwordx4 v[24:27], %[hv], %[rs], s56 offen sc1\ns_or_b64 exec, s[76:77], 1\n" \
  "buffer_load_dwordx4 v[28:31], %[hv], %[rs], s57 offen sc1\ns_or_b64 exec, s[78:79], 1\nbuffer_load_dwordx4 v[32:35], %[hv], %[rs], s58 offen sc1\n" \
  "s_or_b64 exec, s[80:81], 1\nbuffer_load_dwordx4 v[36:39], %[hv], %[rs], s59 offen sc1\ns_mov_b64 exec, -1\n" \
  "buffer_load_dword v244, %[hv], %[rs], s50 offen\nbuffer_load_dword v244, %[hv], %[rs], s50 offen\ns_branch .LY%=_1B2\n.LY%=_1R3:\n" \
  "s_sub_u32 %[bud], %[bud], 4\ns_cmp_gt_i32 %[bud], 0\ns_cbranch_scc0 .LY%=_1B3\ns_sleep 1\nbuffer_load_dwordx4 v[20:23], %[hv], %[rs], s55 offen sc1\n" \
  "s_waitcnt vmcnt(0)\nv_cmp_gt_i32 vcc, 0, v20\ns_cbranch_vccnz .LY%=_1R3\nv_cmp_gt_i32 s[74:75], 0, v24\nv_cmp_gt_i32 s[76:77], 0, v28\n" \
  "v_cmp_gt_i32 s[78:79], 0, v32\nv_cmp_gt_i32 s[80:81], 0, v36\ns_or_b64 exec, s[74:75], 1\nbuffer_load_dwordx4 v[24:27], %[hv], %[rs], s56 offen sc1\n" \
  "s_or_b64 exec, s[76:77], 1\nbuffer_load_dwordx4 v[28:31], %[hv], %[rs], s57 offen sc1\ns_or_b64 exec, s[78:79], 1\n" \
  "buffer_load_dwordx4 v[32:35], %[hv], %[rs], s58 offen sc1\ns_or_b64 exec, s[80:81], 1\nbuffer_load_dwordx4 v[36:39], %[hv], %[rs], s59 offen sc1\n" \
  "s_mov_b64 exec, -1\nbuffer_load_dword v244, %[hv], %[rs], s50 offen\nbuffer_load_dword v244, %[hv], %[rs], s50 offen\n" \
  "buffer_load_dword v244, %[hv], %[rs], s50 offen\ns_branch .LY%=_1B3\n.LY%=_1R4:\ns_sub_u32 %[bud], %[bud], 4\ns_cmp_gt_i32 %[bud], 0\n" \
  "s_cbranch_scc0 .LY%=_1B4\ns_sleep 1\nbuffer_load_dwordx4 v[24:27], %[hv], %[rs], s56 offen sc1\ns_waitcnt vmcnt(0)\nv_cmp_gt_i32 vcc, 0, v24\n" \
  "s_cbranch_vccnz .LY%=_1R4\nv_cmp_gt_i32 s[76:77], 0, v28\nv_cmp_gt_i32 s[78:79], 0, v32\nv_cmp_gt_i32 s[80:81], 0, v36\ns_or_b64 exec, s[76:77], 1\n" \
  "buffer_load_dwordx4 v[28:31], %[hv], %[rs], s57 offen sc1\ns_or_b64 exec, s[78:79], 1\nbuffer_load_dwordx4 v[32:35], %[hv], %[rs], s58 offen sc1\n" \
  "s_or_b64 exec, s[80:81], 1\nbuffer_load_dwordx4 v[36:39], %[hv], %[rs], s59 offen sc1\ns_mov_b64 exec, -1\n" \
  "buffer_load_dword v244, %[hv], %[rs], s50 offen\nbuffer_load_dword v244, %[hv], %[rs], s50 offen\nbuffer_load_dword v244, %[hv], %[rs], s50 offen\n" \
  "buffer_load_dword v244, %[hv], %[rs], s50 offen\ns_branch .LY%=_1B4\n.LY%=_1R5:\ns_sub_u32 %[bud], %[bud], 4\ns_cmp_gt_i32 %[bud], 0\n" \
  "s_cbranch_scc0 .LY%=_1B5\ns_sleep 1\nbuffer_load_dwordx4 v[28:31], %[hv], %[rs], s57 offen sc1\ns_waitcnt vmcnt(0)\nv_cmp_gt_i32 vcc, 0, v28\n" \
  "s_cbranch_vccnz .LY%=_1R5\nv_cmp_gt_i32 s[78:79], 0, v32\nv_cmp_gt_i32 s[80:81], 0, v36\ns_or_b64 exec, s[78:79], 1\n" \
  "buffer_load_dwordx4 v[32:35], %[hv], %[rs], s58 offen sc1\ns_or_b64 exec, s[80:81], 1\nbuffer_load_dwordx4 v[36:39], %[hv], %[rs], s59 offen sc1\n" \
  "s_mov_b64 exec, -1\nbuffer_load_dword v244, %[hv], %[rs], s50 offen\nbuffer_load_dword v244, %[hv], %[rs], s50 offen\n" \
  "buffer_load_dword v244, %[hv], %[rs], s50 offen\nbuffer_load_dword v244, %[hv], %[rs], s50 offen\nbuffer_load_dword v244, %[hv], %[rs], s50 offen\n" \
  "s_branch .LY%=_1B5\n.LY%=_1R6:\ns_sub_u32 %[bud], %[bud], 4\ns_cmp_gt_i32 %[bud], 0\ns_cbranch_scc0 .LY%=_1B6\ns_sleep 1\n" \
  "buffer_load_dwordx4 v[32:35], %[hv], %[rs], s58 offen sc1\ns_waitcnt vmcnt(0)\nv_cmp_gt_i32 vcc, 0, v32\ns_cbranch_vccnz .LY%=_1R6\n" \
  "v_cmp_gt_i32 s[80:81], 0, v36\ns_or_b64 exec, s[80:81], 1\nbuffer_load_dwordx4 v[36:39], %[hv], %[rs], s59 offen sc1\ns_mov_b64 exec, -1\n" \
  "buffer_load_dword v244, %[hv], %[rs], s50 offen\nbuffer_load_dword v244, %[hv], %[rs], s50 offen\nbuffer_load_dword v244, %[hv], %[rs], s50 offen\n" \
  "buffer_load_dword v244, %[hv], %[rs], s50 offen\nbuffer_load_dword v244, %[hv], %[rs], s50 offen\nbuffer_load_dword v244, %[hv], %[rs], s50 offen\n" \
  "s_branch .LY%=_1B6\n.LY%=_1R7:\ns_sub_u32 %[bud], %[bud], 4\ns_cmp_gt_i32 %[bud], 0\ns_cbranch_scc0 .LY%=_1B7\ns_sleep 1\n" \
  "buffer_load_dwordx4 v[36:39], %[hv], %[rs], s59 offen sc1\ns_waitcnt vmcnt(0)\nv_cmp_gt_i32 vcc, 0, v36\ns_cbranch_vccnz .LY%=_1R7\n" \
  "s_mov_b64 exec, -1\ns_branch .LY%=_1B7\n.LY%=_2R0:\ns_sub_u32 %[bud], %[bud], 4\ns_cmp_gt_i32 %[bud], 0\ns_cbranch_scc0 .LY%=_2B0\ns_sleep 1\n" \
  "buffer_load_dwordx4 v[8:11], %[hv], %[rs], s52 offen sc1\nbuffer_load_dwordx4 v[12:15], %[hv], %[rs], s53 offen sc1\n" \
  "buffer_load_dwordx4 v[16:19], %[hv], %[rs], s54 offen sc1\ns_branch .LY%=_2T0\n.LY%=_2R1:\ns_sub_u32 %[bud], %[bud], 4\ns_cmp_gt_i32 %[bud], 0\n" \
  "s_cbranch_scc0 .LY%=_2B1\ns_sleep 1\nbuffer_load_dwordx4 v[12:15], %[hv], %[rs], s53 offen sc1\n" \
  "buffer_load_dwordx4 v[16:19], %[hv], %[rs], s54 offen sc1\nbuffer_load_dword v244, %[hv], %[rs], s50 offen\ns_branch .LY%=_2T1\n.LY%=_2R2:\n" \
  "s_sub_u32 %[bud], %[bud], 4\ns_cmp_gt_i32 %[bud], 0\ns_cbranch_scc0 .LY%=_2B2\ns_sleep 1\nbuffer_load_dwordx4 v[16:19], %[hv], %[rs], s54 offen sc1\n" \
  "buffer_load_dword v244, %[hv], %[rs], s50 offen\nbuffer_load_dword v244, %[hv], %[rs], s50 offen\ns_branch .LY%=_2T2\n.LY%=_3R0:\n" \
  "s_sub_u32 %[bud], %[bud], 4\ns_cmp_gt_i32 %[bud], 0\ns_cbranch_scc0 .LY%=_3B0\ns_sleep 1\nbuffer_load_dwordx4 v[8:11], %[hv], %[rs], s52 offen sc1\n" \
  "s_waitcnt vmcnt(0)\nv_cmp_lt_i32 vcc, -1, v8\ns_cbranch_vccnz .LY%=_3R0\nv_cmp_lt_i32 s[68:69], -1, v12\nv_cmp_lt_i32 s[70:71], -1, v16\n" \
  "v_cmp_lt_i32 s[72:73], -1, v20\nv_cmp_lt_i32 s[74:75], -1, v24\nv_cmp_lt_i32 s[76:77], -1, v28\nv_cmp_lt_i32 s[78:79], -1, v32\n" \
  "v_cmp_lt_i32 s[80:81], -1, v36\ns_or_b64 exec, s[68:69], 1\nbuffer_load_dwordx4 v[12:15], %[hv], %[rs], s53 offen sc1\ns_or_b64 exec, s[70:71], 1\n" \
  "buffer_load_dwordx4 v[16:19], %[hv], %[rs], s54 offen sc1\ns_or_b64 exec, s[72:73], 1\nbuffer_load_dwordx4 v[20:23], %[hv], %[rs], s55 offen sc1\n" \
  "s_or_b64 exec, s[74:75], 1\nbuffer_load_dwordx4 v[24:27], %[hv], %[rs], s56 offen sc1\ns_or_b64 exec, s[76:77], 1\n" \
  "buffer_load_dwordx4 v[28:31], %[hv], %[rs], s57 offen sc1\ns_or_b64 exec, s[78:79], 1\nbuffer_load_dwordx4 v[32:35], %[hv], %[rs], s58 offen sc1\n" \
  "s_or_b64 exec, s[80:81], 1\nbuffer_load_dwordx4 v[36:39], %[hv], %[rs], s59 offen sc1\ns_mov_b64 exec, -1\ns_branch .LY%=_3B0\n.LY%=_3R1:\n" \
  "s_sub_u32 %[bud], %[bud], 4\ns_cmp_gt_i32 %[bud], 0\ns_cbranch_scc0 .LY%=_3B1\ns_sleep 1\nbuffer_load_dwordx4 v[12:15], %[hv], %[rs], s53 offen sc1\n" \
  "s_waitcnt vmcnt(0)\nv_cmp_lt_i32 vcc, -1, v12\ns_cbranch_vccnz .LY%=_3R1\nv_cmp_lt_i32 s[70:71], -1, v16\nv_cmp_lt_i32 s[72:73], -1, v20\n" \
  "v_cmp_lt_i32 s[74:75], -1, v24\nv_cmp_lt_i32 s[76:77], -1, v28\nv_cmp_lt_i32 s[78:79], -1, v32\nv_cmp_lt_i32 s[80:81], -1, v36\n" \
  "s_or_b64 exec, s[70:71], 1\nbuffer_load_dwordx4 v[16:19], %[hv], %[rs], s54 offen sc1\ns_or_b64 exec, s[72:73], 1\n" \
  "buffer_load_dwordx4 v[20:23], %[hv], %[rs], s55 offen sc1\ns_or_b64 exec, s[74:75], 1\nbuffer_load_dwordx4 v[24:27], %[hv], %[rs], s56 offen sc1\n" \
  "s_or_b64 exec, s[76:77], 1\nbuffer_load_dwordx4 v[28:31], %[hv], %[rs], s57 offen sc1\ns_or_b64 exec, s[78:79], 1\n" \
  "buffer_load_dwordx4 v[32:35], %[hv], %[rs], s58 offen sc1\ns_or_b64 exec, s[80:81], 1\nbuffer_load_dwordx4 v[36:39], %[hv], %[rs], s59 offen sc1\n" \
  "s_mov_b64 exec, -1\nbuffer_load_dword v244, %[hv], %[rs], s50 offen\ns_branch .LY%=_3B1\n.LY%=_3R2:\ns_sub_u32 %[bud], %[bud], 4\n" \
  "s_cmp_gt_i32 %[bud], 0\ns_cbranch_scc0 .LY%=_3B2\ns_sleep 1\nbuffer_load_dwordx4 v[16:19], %[hv], %[rs], s54 offen sc1\ns_waitcnt vmcnt(0)\n" \
  "v_cmp_lt_i32 vcc, -1, v16\ns_cbranch_vccnz .LY%=_3R2\nv_cmp_lt_i32 s[72:73], -1, v20\nv_cmp_lt_i32 s[74:75], -1, v24\nv_cmp_lt_i32 s[76:77], -1, v28\n" \
  "v_cmp_lt_i32 s[78:79], -1, v32\nv_cmp_lt_i32 s[80:81], -1, v36\ns_or_b64 exec, s[72:73], 1\n" \
  "buffer_load_dwordx4 v[20:23], %[hv], %[rs], s55 offen sc1\ns_or_b64 exec, s[74:75], 1\nbuffer_load_dwordx4 v[24:27], %[hv], %[rs], s56 offen sc1\n" \
  "s_or_b64 exec, s[76:77], 1\nbuffer_load_dwordx4 v[28:31], %[hv], %[rs], s57 offen sc1\ns_or_b64 exec, s[78:79], 1\n" \
  "buffer_load_dwordx4 v[32:35], %[hv], %[rs], s58 offen sc1\ns_or_b64 exec, s[80:81], 1\nbuffer_load_dwordx4 v[36:39], %[hv], %[rs], s59 offen sc1\n" \
  "s_mov_b64 exec, -1\nbuffer_load_dword v244, %[hv], %[rs], s50 offen\nbuffer_load_dword v244, %[hv], %[rs], s50 offen\ns_branch .LY%=_3B2\n.LY%=_3R3:\n" \
  "s_sub_u32 %[bud], %[bud], 4\ns_cmp_gt_i32 %[bud], 0\ns_cbranch_scc0 .LY%=_3B3\ns_sleep 1\nbuffer_load_dwordx4 v[20:23], %[hv], %[rs], s55 offen sc1\n" \
  "s_waitcnt vmcnt(0)\nv_cmp_lt_i32 vcc, -1, v20\ns_cbranch_vccnz .LY%=_3R3\nv_cmp_lt_i32 s[74:75], -1, v24\nv_cmp_lt_i32 s[76:77], -1, v28\n" \
  "v_cmp_lt_i32 s[78:79], -1, v32\nv_cmp_lt_i32 s[80:81], -1, v36\ns_or_b64 exec, s[74:75], 1\n" \
  "buffer_load_dwordx4 v[24:27], %[hv], %[rs], s56 offen sc1\ns_or_b64 exec, s[76:77], 1\nbuffer_load_dwordx4 v[28:31], %[hv], %[rs], s57 offen sc1\n" \
  "s_or_b64 exec, s[78:79], 1\nbuffer_load_dwordx4 v[32:35], %[hv], %[rs], s58 offen sc1\ns_or_b64 exec, s[80:81], 1\n" \
  "buffer_load_dwordx4 v[36:39], %[hv], %[rs], s59 offen sc1\ns_mov_b64 exec, -1\nbuffer_load_dword v244, %[hv], %[rs], s50 offen\n" \
  "buffer_load_dword v244, %[hv], %[rs], s50 offen\nbuffer_load_dword v244, %[hv], %[rs], s50 offen\ns_branch .LY%=_3B3\n.LY%=_3R4:\n" \
  "s_sub_u32 %[bud], %[bud], 4\ns_cmp_gt_i32 %[bud], 0\ns_cbranch_scc0 .LY%=_3B4\ns_sleep 1\nbuffer_load_dwordx4 v[24:27], %[hv], %[rs], s56 offen sc1\n" \
  "s_waitcnt vmcnt(0)\nv_cmp_lt_i32 vcc, -1, v24\ns_cbranch_vccnz .LY%=_3R4\nv_cmp_lt_i32 s[76:77], -1, v28\nv_cmp_lt_i32 s[78:79], -1, v32\n" \
  "v_cmp_lt_i32 s[80:81], -1, v36\ns_or_b64 exec, s[76:77], 1\nbuffer_load_dwordx4 v[28:31], %[hv], %[rs], s57 offen sc1\ns_or_b64 exec, s[78:79], 1\n" \
  "buffer_load_dwordx4 v[32:35], %[hv], %[rs], s58 offen sc1\ns_or_b64 exec, s[80:81], 1\nbuffer_load_dwordx4 v[36:39], %[hv], %[rs], s59 offen sc1\n" \
  "s_mov_b64 exec, -1\nbuffer_load_dword v244, %[hv], %[rs], s50 offen\nbuffer_load_dword v244, %[hv], %[rs], s50 offen\n" \
  "buffer_load_dword v244, %[hv], %[rs], s50 offen\nbuffer_load_dword v244, %[hv], %[rs], s50 offen\ns_branch .LY%=_3B4\n.LY%=_3R5:\n" \
  "s_sub_u32 %[bud], %[bud], 4\ns_cmp_gt_i32 %[bud], 0\ns_cbranch_scc0 .LY%=_3B5\ns_sleep 1\nbuffer_load_dwordx4 v[28:31], %[hv], %[rs], s57 offen sc1\n" \
  "s_waitcnt vmcnt(0)\nv_cmp_lt_i32 vcc, -1, v28\ns_cbranch_vccnz .LY%=_3R5\nv_cmp_lt_i32 s[78:79], -1, v32\nv_cmp_lt_i32 s[80:81], -1, v36\n" \
  "s_or_b64 exec, s[78:79], 1\nbuffer_load_dwordx4 v[32:35], %[hv], %[rs], s58 offen sc1\ns_or_b64 exec, s[80:81], 1\n" \
  "buffer_load_dwordx4 v[36:39], %[hv], %[rs], s59 offen sc1\ns_mov_b64 exec, -1\nbuffer_load_dword v244, %[hv], %[rs], s50 offen\n" \
  "buffer_load_dword v244, %[hv], %[rs], s50 offen\nbuffer_load_dword v244, %[hv], %[rs], s50 offen\nbuffer_load_dword v244, %[hv], %[rs], s50 offen\n" \
  "buffer_load_dword v244, %[hv], %[rs], s50 offen\ns_branch .LY%=_3B5\n.LY%=_3R6:\ns_sub_u32 %[bud], %[bud], 4\ns_cmp_gt_i32 %[bud], 0\n" \
  "s_cbranch_scc0 .LY%=_3B6\ns_sleep 1\nbuffer_load_dwordx4 v[32:35], %[hv], %[rs], s58 offen sc1\ns_waitcnt vmcnt(0)\nv_cmp_lt_i32 vcc, -1, v32\n" \
  "s_cbranch_vccnz .LY%=_3R6\nv_cmp_lt_i32 s[80:81], -1, v36\ns_or_b64 exec, s[80:81], 1\nbuffer_load_dwordx4 v[36:39], %[hv], %[rs], s59 offen sc1\n" \
  "s_mov_b64 exec, -1\nbuffer_load_dword v244, %[hv], %[rs], s50 offen\nbuffer_load_dword v244, %[hv], %[rs], s50 offen\n" \
  "buffer_load_dword v244, %[hv], %[rs], s50 offen\nbuffer_load_dword v244, %[hv], %[rs], s50 offen\nbuffer_load_dword v244, %[hv], %[rs], s50 offen\n" \
  "buffer_load_dword v244, %[hv], %[rs], s50 offen\ns_branch .LY%=_3B6\n.LY%=_3R7:\ns_sub_u32 %[bud], %[bud], 4\ns_cmp_gt_i32 %[bud], 0\n" \
  "s_cbranch_scc0 .LY%=_3B7\ns_sleep 1\nbuffer_load_dwordx4 v[36:39], %[hv], %[rs], s59 offen sc1\ns_waitcnt vmcnt(0)\nv_cmp_lt_i32 vcc, -1, v36\n" \
  "s_cbranch_vccnz .LY%=_3R7\ns_mov_b64 exec, -1\ns_branch .LY%=_3B7\n.LY%=_E:\ns_waitcnt vmcnt(0) lgkmcnt(0)\n"
#define YCLOB \
  "v8", "v9", "v10", "v11", "v12", "v13", "v14", "v15", "v16", "v17", "v18", "v19", "v20", "v21", "v22", "v23", "v24", "v25", "v26", "v27", "v28", \
  "v29", "v30", "v31", "v32", "v33", "v34", "v35", "v36", "v37", "v38", "v39", "v40", "v41", "v42", "v43", "v44", "v45", "v46", "v47", "v48", "v49", \
  "v50", "v51", "v52", "v53", "v54", "v55", "v56", "v57", "v58", "v59", "v60", "v61", "v62", "v63", "v64", "v65", "v66", "v67", "v68", "v69", "v70", \
  "v71", "v72", "v73", "v74", "v75", "v76", "v77", "v78", "v79", "v80", "v81", "v82", "v83", "v84", "v85", "v86", "v87", "v88", "v89", "v90", "v91", \
  "v92", "v93", "v94", "v95", "v96", "v97", "v98", "v99", "v100", "v101", "v102", "v103", "v104", "v105", "v106", "v107", "v108", "v109", "v110", \
  "v111", "v112", "v113", "v114", "v115", "v116", "v117", "v118", "v119", "v120", "v121", "v122", "v123", "v124", "v125", "v126", "v127", "v128", \
  "v129", "v130", "v131", "v132", "v133", "v134", "v135", "v136", "v137", "v138", "v139", "v140", "v141", "v142", "v143", "v144", "v145", "v146", \
  "v147", "v148", "v149", "v150", "v151", "v152", "v153", "v154", "v155", "v156", "v157", "v158", "v159", "v160", "v161", "v162", "v163", "v164", \
  "v165", "v166", "v167", "v168", "v169", "v170", "v171", "v172", "v173", "v174", "v175", "v176", "v177", "v178", "v179", "v180", "v181", "v182", \
  "v183", "v184", "v185", "v186", "v187", "v188", "v189", "v190", "v191", "v192", "v193", "v194", "v195", "v196", "v197", "v198", "v199", "v200", \
  "v201", "v202", "v203", "v204", "v205", "v206", "v207", "v208", "v209", "v210", "v211", "v212", "v213", "v214", "v215", "v216", "v217", "v218", \
  "v219", "v220", "v221", "v222", "v223", "v224", "v225", "v226", "v227", "v228", "v229", "v230", "v231", "v232", "v233", "v234", "v235", "v236", \
  "v237", "v238", "v239", "v240", "v241", "v242", "v243", "v244", "v245", "v246", "v247", "v248", "v249", "s46", "s47", "s48", "s49", "s50", "s51", \
  "s52", "s53", "s54", "s55", "s56", "s57", "s58", "s59", "s60", "s61", "s62", "s63", "s64", "s65", "s66", "s67", "s68", "s69", "s70", "s71", "s72", \
  "s73", "s74", "s75", "s76", "s77", "s78", "s79", "s80", "s81"
#define FPROG \
  "s_mov_b32 s52, m0\ns_mov_b32 s46, 1\ns_mov_b32 s47, 0\ns_mov_b32 s48, 0x40000000\ns_mov_b32 s51, %[os]\nv_mov_b32 v89, %[tx]\nv_mov_b32 v91, %[ty]\n" \
  ".LF%=_T:\nds_read_b32 v95, %[pf] offset:12\nds_read_b128 v[68:71], %[pa] offset:30720\nds_read_b128 v[72:75], %[pa] offset:31744\n" \
  "ds_read_b128 v[76:79], %[pa] offset:32768\nds_read_b128 v[80:83], %[pa] offset:33792\nds_read_b128 v[84:87], %[pa] offset:34816\n" \
  "s_lshl_b32 s49, s46, 1\ns_sub_u32 s49, s49, 2\ns_xor_b32 s49, s49, 14\ns_mul_i32 s50, s49, 20480\ns_add_u32 s50, s50, %[xo]\ns_waitcnt lgkmcnt(5)\n" \
  "v_cmp_gt_u32 vcc, s46, v95\ns_cbranch_vccnz .LF%=_W3\n.LF%=_U:\nds_read_b96 v[92:94], %[pf]\nds_read_b128 v[28:31], %[pa] offset:0\n" \
  "ds_read_b128 v[32:35], %[pa] offset:1024\nds_read_b128 v[36:39], %[pa] offset:2048\nds_read_b128 v[40:43], %[pa] offset:3072\n" \
  "ds_read_b128 v[44:47], %[pa] offset:4096\nds_read_b128 v[48:51], %[pa] offset:10240\nds_read_b128 v[52:55], %[pa] offset:11264\n" \
  "ds_read_b128 v[56:59], %[pa] offset:12288\nds_read_b128 v[60:63], %[pa] offset:13312\ns_set_gpr_idx_on s47, gpr_idx(SRC0)\ns_waitcnt lgkmcnt(10)\n" \
  "v_add_f32 v8, v96, v68\nv_add_f32 v9, v97, v69\nv_add_f32 v10, v98, v70\nv_add_f32 v11, v99, v71\nv_add_f32 v12, v100, v72\nv_add_f32 v13, v101, v73\n" \
  "v_add_f32 v14, v102, v74\nv_add_f32 v15, v103, v75\nv_add_f32 v16, v104, v76\nv_add_f32 v17, v105, v77\nv_add_f32 v18, v106, v78\n" \
  "v_add_f32 v19, v107, v79\nv_add_f32 v20, v108, v80\nv_add_f32 v21, v109, v81\nv_add_f32 v22, v110, v82\nv_add_f32 v23, v111, v83\n" \
  "v_add_f32 v24, v112, v84\nv_add_f32 v25, v113, v85\nv_add_f32 v26, v114, v86\nv_add_f32 v27, v115, v87\ns_set_gpr_idx_off\n.LF%=_X:\n" \
  "s_waitcnt lgkmcnt(9)\nds_read_b128 v[64:67], %[pa] offset:14336\nds_read_b128 v[68:71], %[pa] offset:20480\n" \
  "ds_read_b128 v[72:75], %[pa] offset:21504\nds_read_b128 v[76:79], %[pa] offset:22528\nds_read_b128 v[80:83], %[pa] offset:23552\n" \
  "ds_read_b128 v[84:87], %[pa] offset:24576\nv_min3_u32 v92, v92, v93, v94\nv_cmp_gt_u32 vcc, s46, v92\ns_cbranch_vccnz .LF%=_W2\n.LF%=_V:\n" \
  FTS(1) \
  "v_mov_b32 v92, s46\ns_waitcnt lgkmcnt(10)\nv_pk_add_f32 v[8:9], v[8:9], v[28:29]\nv_pk_add_f32 v[10:11], v[10:11], v[30:31]\n" \
  "v_pk_add_f32 v[12:13], v[12:13], v[32:33]\nv_pk_add_f32 v[14:15], v[14:15], v[34:35]\nv_pk_add_f32 v[16:17], v[16:17], v[36:37]\n" \
  "v_pk_add_f32 v[18:19], v[18:19], v[38:39]\nv_pk_add_f32 v[20:21], v[20:21], v[40:41]\nv_pk_add_f32 v[22:23], v[22:23], v[42:43]\n" \
  "v_pk_add_f32 v[24:25], v[24:25], v[44:45]\nv_pk_add_f32 v[26:27], v[26:27], v[46:47]\nds_write_b32 %[pf], v92 offset:32\n" \
  "v_xor_b32 %[pa], 13312, %[pa]\ns_waitcnt lgkmcnt(6)\nv_pk_add_f32 v[8:9], v[8:9], v[48:49]\nv_pk_add_f32 v[10:11], v[10:11], v[50:51]\n" \
  "v_pk_add_f32 v[12:13], v[12:13], v[52:53]\nv_pk_add_f32 v[14:15], v[14:15], v[54:55]\nv_pk_add_f32 v[16:17], v[16:17], v[56:57]\n" \
  "v_pk_add_f32 v[18:19], v[18:19], v[58:59]\nv_pk_add_f32 v[20:21], v[20:21], v[60:61]\nv_pk_add_f32 v[22:23], v[22:23], v[62:63]\n" \
  "v_pk_add_f32 v[24:25], v[24:25], v[64:65]\nv_pk_add_f32 v[26:27], v[26:27], v[66:67]\ns_waitcnt lgkmcnt(1)\n" \
  "v_pk_add_f32 v[24:25], v[24:25], v[84:85]\nv_pk_add_f32 v[26:27], v[26:27], v[86:87]\nv_pk_add_f32 v[8:9], v[8:9], v[68:69]\n" \
  "v_pk_add_f32 v[10:11], v[10:11], v[70:71]\nv_pk_add_f32 v[12:13], v[12:13], v[72:73]\nv_pk_add_f32 v[14:15], v[14:15], v[74:75]\n" \
  "v_pk_add_f32 v[16:17], v[16:17], v[76:77]\nv_pk_add_f32 v[18:19], v[18:19], v[78:79]\nv_pk_add_f32 v[20:21], v[20:21], v[80:81]\n" \
  "v_pk_add_f32 v[22:23], v[22:23], v[82:83]\nv_cvt_pk_bf16_f32 v36, v24, v25\nv_cvt_pk_bf16_f32 v37, v26, v27\nv_cvt_pk_bf16_f32 v38, v24, v25\n" \
  "v_cvt_pk_bf16_f32 v39, v26, v27\nv_cvt_pk_bf16_f32 v28, v8, v9\nv_cvt_pk_bf16_f32 v29, v10, v11\nv_cvt_pk_bf16_f32 v30, v12, v13\n" \
  "v_cvt_pk_bf16_f32 v31, v14, v15\nv_cvt_pk_bf16_f32 v32, v16, v17\nv_cvt_pk_bf16_f32 v33, v18, v19\nv_cvt_pk_bf16_f32 v34, v20, v21\n" \
  "v_cvt_pk_bf16_f32 v35, v22, v23\nv_permlane16_swap_b32 v36, v38\nv_permlane16_swap_b32 v37, v39\ns_cmp_gt_u32 s46, 24\ns_waitcnt vmcnt(0)\n" \
  "buffer_store_dwordx4 v[88:91], %[rv], %[rs], s48 offen sc1\ns_cbranch_scc1 .LF%=_O\nbuffer_store_dwordx4 v[28:31], %[hv], %[rs], s50 offen sc1\n" \
  "buffer_store_dwordx4 v[32:35], %[hv], %[rs], s50 offen offset:1024 sc1\nbuffer_store_dwordx4 v[36:39], %[v2], %[rs], s50 offen sc1\n" \
  FTS(2) \
  "v_pk_add_f32 v[48:49], v[8:9], v[10:11]\nv_pk_add_f32 v[50:51], v[12:13], v[14:15]\nv_pk_add_f32 v[48:49], v[48:49], v[16:17]\n" \
  "v_pk_add_f32 v[50:51], v[50:51], v[18:19]\nv_pk_add_f32 v[48:49], v[48:49], v[20:21]\nv_pk_add_f32 v[50:51], v[50:51], v[22:23]\n" \
  "v_pk_add_f32 v[48:49], v[48:49], v[24:25]\nv_pk_add_f32 v[50:51], v[50:51], v[26:27]\nv_pk_add_f32 v[48:49], v[48:49], v[50:51]\n" \
  "s_lshl_b32 s48, s49, 11\nv_add_f32 v48, v48, v49\nv_mov_b32 v49, v48\ns_nop 1\nv_permlane16_swap_b32 v48, v49\nv_add_f32 v48, v48, v49\n" \
  "v_mov_b32 v49, v48\ns_nop 1\nv_permlane32_swap_b32 v48, v49\nv_add_f32 v88, v48, v49\nv_mul_f32 v48, 0xbc4ccccd, v88\ns_add_u32 s48, s48, %[ro]\n" \
  "v_pk_add_f32 v[68:69], v[8:9], v[48:49] op_sel_hi:[1,0]\nv_pk_add_f32 v[70:71], v[10:11], v[48:49] op_sel_hi:[1,0]\n" \
  "v_pk_add_f32 v[72:73], v[12:13], v[48:49] op_sel_hi:[1,0]\nv_pk_add_f32 v[74:75], v[14:15], v[48:49] op_sel_hi:[1,0]\n" \
  "v_pk_add_f32 v[76:77], v[16:17], v[48:49] op_sel_hi:[1,0]\nv_pk_add_f32 v[78:79], v[18:19], v[48:49] op_sel_hi:[1,0]\n" \
  "v_pk_add_f32 v[80:81], v[20:21], v[48:49] op_sel_hi:[1,0]\nv_pk_add_f32 v[82:83], v[22:23], v[48:49] op_sel_hi:[1,0]\n" \
  "v_pk_add_f32 v[84:85], v[24:25], v[48:49] op_sel_hi:[1,0]\nv_pk_add_f32 v[86:87], v[26:27], v[48:49] op_sel_hi:[1,0]\n" \
  "v_pk_mul_f32 v[48:49], v[68:69], v[68:69]\nv_pk_mul_f32 v[50:51], v[70:71], v[70:71]\nv_pk_fma_f32 v[48:49], v[72:73], v[72:73], v[48:49]\n" \
  "v_pk_fma_f32 v[50:51], v[74:75], v[74:75], v[50:51]\nv_pk_fma_f32 v[48:49], v[76:77], v[76:77], v[48:49]\n" \
  "v_pk_fma_f32 v[50:51], v[78:79], v[78:79], v[50:51]\nv_pk_fma_f32 v[48:49], v[80:81], v[80:81], v[48:49]\n" \
  "v_pk_fma_f32 v[50:51], v[82:83], v[82:83], v[50:51]\nv_pk_fma_f32 v[48:49], v[84:85], v[84:85], v[48:49]\n" \
  "v_pk_fma_f32 v[50:51], v[86:87], v[86:87], v[50:51]\nv_pk_add_f32 v[48:49], v[48:49], v[50:51]\nv_add_f32 v48, v48, v49\nv_mov_b32 v49, v48\ns_nop 1\n" \
  "v_permlane16_swap_b32 v48, v49\nv_add_f32 v48, v48, v49\nv_mov_b32 v49, v48\ns_nop 1\nv_permlane32_swap_b32 v48, v49\nv_add_f32 v90, v48, v49\n" \
  "s_set_gpr_idx_on s47, gpr_idx(DST)\nv_mov_b32 v96, v8\nv_mov_b32 v97, v9\nv_mov_b32 v98, v10\nv_mov_b32 v99, v11\nv_mov_b32 v100, v12\n" \
  "v_mov_b32 v101, v13\nv_mov_b32 v102, v14\nv_mov_b32 v103, v15\nv_mov_b32 v104, v16\nv_mov_b32 v105, v17\nv_mov_b32 v106, v18\nv_mov_b32 v107, v19\n" \
  "v_mov_b32 v108, v20\nv_mov_b32 v109, v21\nv_mov_b32 v110, v22\nv_mov_b32 v111, v23\nv_mov_b32 v112, v24\nv_mov_b32 v113, v25\nv_mov_b32 v114, v26\n" \
  "v_mov_b32 v115, v27\ns_set_gpr_idx_off\n.LF%=_N:\n" \
  FTP(3) \
  "s_add_u32 s47, s47, 20\ns_cmp_eq_u32 s47, 160\ns_cselect_b32 s47, 0, s47\ns_add_u32 s46, s46, 1\ns_cmp_eq_u32 s46, 25\ns_cbranch_scc1 .LF%=_Z\n" \
  "s_cmp_le_u32 s46, 32\ns_cbranch_scc1 .LF%=_T\ns_branch .LF%=_E\n.LF%=_Z:\nv_and_b32 v48, 240, %[hv]\nv_mul_u32_u24 v48, 160, v48\n" \
  "v_lshrrev_b32 v49, 9, %[hv]\nv_lshl_add_u32 v49, v49, 4, v48\nv_and_b32 v50, 256, %[hv]\nv_lshl_add_u32 %[v2], v50, 22, v49\n" \
  "v_lshrrev_b32 v49, 8, %[hv]\nv_lshl_add_u32 %[hv], v49, 4, v48\ns_branch .LF%=_T\n.LF%=_O:\n" \
  "buffer_store_dwordx4 v[28:31], %[hv], %[rout], s51 offen sc1\nbuffer_store_dwordx4 v[32:35], %[hv], %[rout], s51 offen offset:64 sc1\n" \
  "buffer_store_dwordx4 v[36:39], %[v2], %[rout], s51 offen offset:128 sc1\ns_mov_b32 s48, 0x40000000\ns_sub_u32 s51, s51, 81920\ns_branch .LF%=_N\n" \
  ".LF%=_W3:\ns_waitcnt vmcnt(0)\nbuffer_store_dwordx4 v[88:91], %[rv], %[rs], s48 offen sc1\ns_mov_b32 s48, 0x40000000\n.LF%=_W3a:\n" \
  "s_sub_u32 %[bud], %[bud], 1\ns_cmp_gt_i32 %[bud], 0\ns_cbranch_scc0 .LF%=_U\ns_sleep 1\nds_read_b32 v95, %[pf] offset:12\ns_waitcnt lgkmcnt(0)\n" \
  "v_cmp_gt_u32 vcc, s46, v95\ns_cbranch_vccnz .LF%=_W3a\ns_branch .LF%=_T\n.LF%=_W2:\ns_waitcnt vmcnt(0)\n" \
  "buffer_store_dwordx4 v[88:91], %[rv], %[rs], s48 offen sc1\ns_mov_b32 s48, 0x40000000\n.LF%=_W2a:\ns_sub_u32 %[bud], %[bud], 1\n" \
  "s_cmp_gt_i32 %[bud], 0\ns_cbranch_scc0 .LF%=_V\ns_sleep 1\nds_read_b96 v[92:94], %[pf]\ns_waitcnt lgkmcnt(0)\nv_min3_u32 v92, v92, v93, v94\n" \
  "v_cmp_gt_u32 vcc, s46, v92\ns_cbranch_vccnz .LF%=_W2a\nds_read_b96 v[92:94], %[pf]\nds_read_b128 v[28:31], %[pa] offset:0\n" \
  "ds_read_b128 v[32:35], %[pa] offset:1024\nds_read_b128 v[36:39], %[pa] offset:2048\nds_read_b128 v[40:43], %[pa] offset:3072\n" \
  "ds_read_b128 v[44:47], %[pa] offset:4096\nds_read_b128 v[48:51], %[pa] offset:10240\nds_read_b128 v[52:55], %[pa] offset:11264\n" \
  "ds_read_b128 v[56:59], %[pa] offset:12288\nds_read_b128 v[60:63], %[pa] offset:13312\ns_branch .LF%=_X\n.LF%=_E:\ns_mov_b32 m0, s52\n" \
  "s_waitcnt vmcnt(0) lgkmcnt(0)\n"
#define FCLOB \
  "v8", "v9", "v10", "v11", "v12", "v13", "v14", "v15", "v16", "v17", "v18", "v19", "v20", "v21", "v22", "v23", "v24", "v25", "v26", "v27", "v28", \
  "v29", "v30", "v31", "v32", "v33", "v34", "v35", "v36", "v37", "v38", "v39", "v40", "v41", "v42", "v43", "v44", "v45", "v46", "v47", "v48", "v49", \
  "v50", "v51", "v52", "v53", "v54", "v55", "v56", "v57", "v58", "v59", "v60", "v61", "v62", "v63", "v64", "v65", "v66", "v67", "v68", "v69", "v70", \
  "v71", "v72", "v73", "v74", "v75", "v76", "v77", "v78", "v79", "v80", "v81", "v82", "v83", "v84", "v85", "v86", "v87", "v88", "v89", "v90", "v91", \
  "v92", "v93", "v94", "v95", "s46", "s47", "s48", "s49", "s50", "s51", "s52"
#define APROG \
  "s_mov_b32 s67, 55296\ns_mov_b32 s60, %[rc]\ns_mov_b32 s61, %[rp]\ns_mov_b32 s62, %[sg]\ns_mov_b32 s63, %[so]\ns_mov_b32 s64, %[need]\n" \
  "s_mov_b32 s65, %[sv]\nv_mad_u32_u24 v76, %[mz], s60, %[kq]\nv_add_u32 v78, s61, %[kl]\nv_add_u32 v77, s60, %[kl]\nv_add_u32 v82, s61, %[v1]\n" \
  "v_add_u32 v81, s60, %[v1]\nv_add_u32 v84, s61, %[v2]\nv_add_u32 v83, s60, %[v2]\nv_add_u32 v80, s61, %[v0]\nv_add_u32 v79, s60, %[v0]\n" \
  "s_getpc_b64 s[68:69]\n.LA%=_G:\ns_mul_i32 s66, %[wc], 100\ns_sub_u32 s66, .LA%=_C-.LA%=_G+500, s66\ns_add_u32 s68, s68, s66\ns_addc_u32 s69, s69, 0\n" \
  "v_mov_b32 v33, %[go]\ns_setprio 1\n.LA%=_T:\nds_read_b128 v[88:91], %[md]\n.LA%=_D:\nds_read_b128 v[36:39], v76 offset:0\n" \
  "ds_read_b128 v[44:47], v78 offset:80\nds_read_b128 v[48:51], v77 offset:80\nds_read_b128 v[40:43], v76 offset:32\n" \
  "ds_read_b128 v[52:55], v78 offset:112\nds_read_b128 v[56:59], v77 offset:112\nds_read_b64_tr_b16 v[64:65], v82 offset:0\n" \
  "ds_read_b64_tr_b16 v[66:67], v81 offset:0\nds_read_b64_tr_b16 v[72:73], v84 offset:0\nds_read_b64_tr_b16 v[74:75], v83 offset:0\n" \
  "ds_read_b64_tr_b16 v[60:61], v80 offset:0\nds_read_b64_tr_b16 v[62:63], v79 offset:0\nds_read_b64_tr_b16 v[68:69], v80 offset:40\n" \
  "ds_read_b64_tr_b16 v[70:71], v79 offset:40\ns_waitcnt lgkmcnt(13)\nv_min3_u32 v88, v88, v89, v90\nv_min_u32 v88, v88, v91\n" \
  "v_cmp_gt_u32 vcc, s64, v88\nv_and_b32 v38, %[mqa], v38\nv_and_b32 v39, %[mqa], v39\ns_cbranch_vccnz .LA%=_W\n" \
  ATS(0) \
  "s_waitcnt lgkmcnt(10)\nv_and_b32 v40, %[mqb], v40\nv_and_b32 v41, %[mqb], v41\ns_setprio 3\n" \
  "v_mfma_f32_16x16x32_bf16 v[44:47], v[44:47], v[36:39], %[cp]\nv_mfma_f32_16x16x32_bf16 v[48:51], v[48:51], v[36:39], %[cq]\ns_waitcnt lgkmcnt(8)\n" \
  "v_mfma_f32_16x16x32_bf16 v[52:55], v[52:55], v[40:43], %[cp]\nv_mfma_f32_16x16x32_bf16 v[56:59], v[56:59], v[40:43], %[cq]\ns_setprio 1\n" \
  "ds_read_b32 v87, v33\nds_read_b32 v32, %[va]\ns_add_u32 s60, s60, 8704\ns_cmp_ge_u32 s60, 148992\ns_cselect_b32 s60, %[c0], s60\n" \
  "s_add_u32 s61, s61, 8704\nv_max_f32 v44, v44, v48\nv_max_f32 v45, v45, v49\nv_max_f32 v46, v46, v50\nv_max_f32 v47, v47, v51\n" \
  "v_max3_f32 v85, v44, v45, v46\nv_max_f32 v52, v52, v56\nv_max_f32 v53, v53, v57\nv_max_f32 v54, v54, v58\nv_max_f32 v55, v55, v59\n" \
  "v_max_f32 v85, v85, v47\nv_max3_f32 v86, v52, v53, v54\nv_max_f32 v86, v86, v55\ns_cmp_ge_u32 s61, 148992\ns_cselect_b32 s61, %[p0], s61\n" \
  "v_permlane16_swap_b32 v85, v86\nv_max_f32 v85, v85, v86\nv_mov_b32 v86, v85\nv_mad_u32_u24 v76, %[mz], s60, %[kq]\nv_add_u32 v78, s61, %[kl]\n" \
  "v_permlane32_swap_b32 v85, v86\nv_max_f32 v86, v85, v86\nv_mul_f32 v85, 0xbea52b5b, v86\nv_mul_f32 v86, 0xbea52b5b, v86\nv_add_u32 v77, s60, %[kl]\n" \
  "v_add_u32 v82, s61, %[v1]\nv_permlane16_swap_b32 v85, v86\nv_fmamk_f32 v44, v44, 0x3ea52b5b, v85\nv_fmamk_f32 v45, v45, 0x3ea52b5b, v85\n" \
  "v_fmamk_f32 v46, v46, 0x3ea52b5b, v85\nv_fmamk_f32 v47, v47, 0x3ea52b5b, v85\nv_exp_f32 v44, v44\nv_fmamk_f32 v52, v52, 0x3ea52b5b, v86\n" \
  "v_exp_f32 v45, v45\nv_fmamk_f32 v53, v53, 0x3ea52b5b, v86\nv_exp_f32 v46, v46\nv_fmamk_f32 v54, v54, 0x3ea52b5b, v86\nv_exp_f32 v47, v47\n" \
  "v_fmamk_f32 v55, v55, 0x3ea52b5b, v86\nv_exp_f32 v52, v52\nv_cvt_pk_bf16_f32 v38, v44, v45\nv_exp_f32 v53, v53\nv_cvt_pk_bf16_f32 v39, v46, v47\n" \
  "v_exp_f32 v54, v54\nv_and_b32 v36, %[mp0], v38\nv_exp_f32 v55, v55\nv_and_b32 v37, %[mp1], v39\nv_xor_b32 v38, v38, v36\nv_xor_b32 v39, v39, v37\n" \
  "v_cvt_pk_bf16_f32 v42, v52, v53\nv_cvt_pk_bf16_f32 v43, v54, v55\nv_and_b32 v40, %[mp0], v42\nv_and_b32 v41, %[mp1], v43\nv_xor_b32 v42, v42, v40\n" \
  "v_xor_b32 v43, v43, v41\ns_waitcnt lgkmcnt(0)\ns_setprio 3\nv_mfma_f32_16x16x32_bf16 v[64:67], v[64:67], v[36:39], 0\n" \
  "v_mfma_f32_16x16x32_bf16 v[72:75], v[72:75], v[40:43], 0\nv_mfma_f32_16x16x32_bf16 v[60:63], v[60:63], v[36:39], 0\n" \
  "v_mfma_f32_16x16x32_bf16 v[68:71], v[68:71], v[40:43], 0\ns_setprio 1\ns_cmp_lt_u32 %[m], 5\ns_cselect_b32 s66, 9, %[sb]\ns_add_u32 s66, s66, s65\n" \
  "v_mov_b32 v88, s62\nv_mov_b32 v89, s66\nv_mov_b32 v85, v64\nds_write_b32 v88, v89\nv_mov_b32 v86, v72\nv_permlane16_swap_b32 v65, v85\n" \
  "v_cmp_ne_u32 vcc, 0, v87\nv_permlane16_swap_b32 v73, v86\nv_rcp_f32 v85, v85\nv_rcp_f32 v86, v86\nv_mul_f32 v60, v60, v85\nv_mul_f32 v61, v61, v85\n" \
  "v_mul_f32 v62, v62, v85\nv_mul_f32 v63, v63, v85\nv_mul_f32 v68, v68, v86\nv_mul_f32 v69, v69, v86\nv_mul_f32 v70, v70, v86\nv_mul_f32 v71, v71, v86\n" \
  "v_cvt_pk_bf16_f32 v48, v60, v61\nv_cvt_pk_bf16_f32 v49, v62, v63\nv_cvt_pk_bf16_f32 v50, v68, v69\nv_cvt_pk_bf16_f32 v51, v70, v71\n" \
  "buffer_store_dwordx4 v[48:51], %[s0], %[rs], s63 offen sc1\nv_mul_f32 v64, v64, v85\nv_mul_f32 v65, v65, v85\nv_mul_f32 v66, v66, v85\n" \
  "v_mul_f32 v67, v67, v85\nv_mul_f32 v72, v72, v86\nv_mul_f32 v73, v73, v86\nv_mul_f32 v74, v74, v86\nv_mul_f32 v75, v75, v86\n" \
  "v_cvt_pk_bf16_f32 v56, v64, v65\nv_cvt_pk_bf16_f32 v57, v66, v67\nv_cvt_pk_bf16_f32 v58, v72, v73\nv_cvt_pk_bf16_f32 v59, v74, v75\n" \
  "buffer_store_dwordx4 v[56:59], %[s1], %[rs], s63 offen sc1\n" \
  ATS(2) \
  "v_add_u32 v81, s60, %[v1]\nv_add_u32 v84, s61, %[v2]\nv_add_u32 v83, s60, %[v2]\nv_add_u32 v80, s61, %[v0]\nv_add_u32 v79, s60, %[v0]\n" \
  "s_add_u32 s62, s62, 16\ns_cmp_ge_u32 s62, 159808\ns_cselect_b32 s62, %[g0], s62\ns_sub_u32 s63, s63, 110592\ns_add_u32 s64, s64, 1\n" \
  "s_add_u32 s65, s65, 2\ns_add_u32 %[m], %[m], 1\ns_cbranch_vccnz .LA%=_J\n.LA%=_K:\ns_cmp_lt_u32 %[m], %[m1]\ns_cbranch_scc1 .LA%=_T\n" \
  "s_cmp_lt_u32 %[m], 7\ns_cbranch_scc1 .LA%=_E\n.LA%=_R:\ns_cmp_eq_u32 %[wc], 0\ns_cbranch_scc1 .LA%=_E\nds_read_b32 v87, v33\nds_read_b32 v32, %[va]\n" \
  "s_waitcnt lgkmcnt(0)\nv_cmp_ne_u32 vcc, 0, v87\ns_cbranch_vccnz .LA%=_J\ns_sub_u32 %[bud], %[bud], 1\ns_cmp_gt_i32 %[bud], 0\ns_cbranch_scc0 .LA%=_E\n" \
  "s_sleep 1\ns_branch .LA%=_R\n.LA%=_J:\ns_setpc_b64 s[68:69]\n.LA%=_C:\nbuffer_load_dwordx4 v[96:99], v32, %[rs], %[wo] offen sc1\n" \
  "s_add_u32 %[wo], %[wo], s67\nbuffer_load_dwordx4 v[100:103], v32, %[rs], %[wo] offen sc1\ns_add_u32 %[wo], %[wo], s67\n" \
  "buffer_load_dwordx4 v[104:107], v32, %[rs], %[wo] offen sc1\ns_add_u32 %[wo], %[wo], s67\n" \
  "buffer_load_dwordx4 v[108:111], v32, %[rs], %[wo] offen sc1\ns_add_u32 %[wo], %[wo], s67\n" \
  "buffer_load_dwordx4 v[112:115], v32, %[rs], %[wo] offen sc1\ns_add_u32 %[wo], %[wo], s67\n" \
  "buffer_load_dwordx4 v[116:119], v32, %[rs], %[wo] offen sc1\ns_add_u32 %[wo], %[wo], s67\n" \
  "buffer_load_dwordx4 v[120:123], v32, %[rs], %[wo] offen sc1\ns_add_u32 %[wo], %[wo], s67\n" \
  "buffer_load_dwordx4 v[124:127], v32, %[rs], %[wo] offen sc1\ns_add_u32 %[wo], %[wo], s67\ns_branch .LA%=_Q\n" \
  "buffer_load_dwordx4 v[128:131], v32, %[rs], %[wo] offen sc1\ns_add_u32 %[wo], %[wo], s67\n" \
  "buffer_load_dwordx4 v[132:135], v32, %[rs], %[wo] offen sc1\ns_add_u32 %[wo], %[wo], s67\n" \
  "buffer_load_dwordx4 v[136:139], v32, %[rs], %[wo] offen sc1\ns_add_u32 %[wo], %[wo], s67\n" \
  "buffer_load_dwordx4 v[140:143], v32, %[rs], %[wo] offen sc1\ns_add_u32 %[wo], %[wo], s67\n" \
  "buffer_load_dwordx4 v[144:147], v32, %[rs], %[wo] offen sc1\ns_add_u32 %[wo], %[wo], s67\n" \
  "buffer_load_dwordx4 v[148:151], v32, %[rs], %[wo] offen sc1\ns_add_u32 %[wo], %[wo], s67\n" \
  "buffer_load_dwordx4 v[152:155], v32, %[rs], %[wo] offen sc1\ns_add_u32 %[wo], %[wo], s67\n" \
  "buffer_load_dwordx4 v[156:159], v32, %[rs], %[wo] offen sc1\ns_add_u32 %[wo], %[wo], s67\ns_branch .LA%=_Q\n" \
  "buffer_load_dwordx4 v[160:163], v32, %[rs], %[wo] offen sc1\ns_add_u32 %[wo], %[wo], s67\n" \
  "buffer_load_dwordx4 v[164:167], v32, %[rs], %[wo] offen sc1\ns_add_u32 %[wo], %[wo], s67\n" \
  "buffer_load_dwordx4 v[168:171], v32, %[rs], %[wo] offen sc1\ns_add_u32 %[wo], %[wo], s67\n" \
  "buffer_load_dwordx4 v[172:175], v32, %[rs], %[wo] offen sc1\ns_add_u32 %[wo], %[wo], s67\n" \
  "buffer_load_dwordx4 v[176:179], v32, %[rs], %[wo] offen sc1\ns_add_u32 %[wo], %[wo], s67\n" \
  "buffer_load_dwordx4 v[180:183], v32, %[rs], %[wo] offen sc1\ns_add_u32 %[wo], %[wo], s67\n" \
  "buffer_load_dwordx4 v[184:187], v32, %[rs], %[wo] offen sc1\ns_add_u32 %[wo], %[wo], s67\n" \
  "buffer_load_dwordx4 v[188:191], v32, %[rs], %[wo] offen sc1\ns_add_u32 %[wo], %[wo], s67\ns_branch .LA%=_Q\n" \
  "buffer_load_dwordx4 v[192:195], v32, %[rs], %[wo] offen sc1\ns_add_u32 %[wo], %[wo], s67\n" \
  "buffer_load_dwordx4 v[196:199], v32, %[rs], %[wo] offen sc1\ns_add_u32 %[wo], %[wo], s67\n" \
  "buffer_load_dwordx4 v[200:203], v32, %[rs], %[wo] offen sc1\ns_add_u32 %[wo], %[wo], s67\n" \
  "buffer_load_dwordx4 v[204:207], v32, %[rs], %[wo] offen sc1\ns_add_u32 %[wo], %[wo], s67\n" \
  "buffer_load_dwordx4 v[208:211], v32, %[rs], %[wo] offen sc1\ns_add_u32 %[wo], %[wo], s67\n" \
  "buffer_load_dwordx4 v[212:215], v32, %[rs], %[wo] offen sc1\ns_add_u32 %[wo], %[wo], s67\n" \
  "buffer_load_dwordx4 v[216:219], v32, %[rs], %[wo] offen sc1\ns_add_u32 %[wo], %[wo], s67\n" \
  "buffer_load_dwordx4 v[220:223], v32, %[rs], %[wo] offen sc1\ns_add_u32 %[wo], %[wo], s67\ns_branch .LA%=_Q\n" \
  "buffer_load_dwordx4 v[224:227], v32, %[rs], %[wo] offen sc1\ns_add_u32 %[wo], %[wo], s67\n" \
  "buffer_load_dwordx4 v[228:231], v32, %[rs], %[wo] offen sc1\ns_add_u32 %[wo], %[wo], s67\n" \
  "buffer_load_dwordx4 v[232:235], v32, %[rs], %[wo] offen sc1\ns_add_u32 %[wo], %[wo], s67\n" \
  "buffer_load_dwordx4 v[236:239], v32, %[rs], %[wo] offen sc1\ns_add_u32 %[wo], %[wo], s67\n" \
  "buffer_load_dwordx4 v[240:243], v32, %[rs], %[wo] offen sc1\ns_add_u32 %[wo], %[wo], s67\n" \
  "buffer_load_dwordx4 v[244:247], v32, %[rs], %[wo] offen sc1\ns_add_u32 %[wo], %[wo], s67\n" \
  "buffer_load_dwordx4 v[248:251], v32, %[rs], %[wo] offen sc1\ns_add_u32 %[wo], %[wo], s67\n" \
  "buffer_load_dwordx4 v[252:255], v32, %[rs], %[wo] offen sc1\ns_add_u32 %[wo], %[wo], s67\ns_branch .LA%=_Q\n.LA%=_Q:\ns_add_u32 s68, s68, 100\n" \
  "s_addc_u32 s69, s69, 0\ns_sub_u32 %[wc], %[wc], 1\ns_cmp_lg_u32 %[wc], 0\ns_cbranch_scc1 .LA%=_K\nv_mov_b32 v33, 159872\ns_branch .LA%=_K\n.LA%=_W:\n" \
  "s_sub_u32 %[bud], %[bud], 1\ns_cmp_gt_i32 %[bud], 0\ns_cselect_b32 s64, s64, 0\ns_sleep 1\nds_read_b128 v[88:91], %[md]\ns_waitcnt lgkmcnt(0)\n" \
  "v_min3_u32 v88, v88, v89, v90\nv_min_u32 v88, v88, v91\nv_cmp_gt_u32 vcc, s64, v88\ns_cbranch_vccnz .LA%=_W\ns_branch .LA%=_D\n.LA%=_E:\ns_setprio 3\n"
#define XPROG0 \
  "s_mov_b32 s60, %[ra]\ns_mov_b32 s63, 7\ns_lshl_b32 s61, %[base], 1\ns_add_u32 s64, s61, 1\ns_add_u32 s61, s61, 2\nv_mov_b32 v89, s60\n" \
  "s_mul_i32 s65, s60, 2560\nv_mov_b32 v27, %[base]\nv_add_u32 v88, s65, %[xl]\n" \
  XTI(0) \
  "s_setprio 2\ns_branch .LX%=_N\n.LX%=_T:\n" \
  XTS(0) \
  "s_waitcnt vmcnt(39) lgkmcnt(3)\nv_mfma_f32_32x32x16_bf16 v[28:43], v[96:99], v[44:47], 0\nds_read_b128 v[60:63], v88 offset:2048\n" \
  "s_waitcnt vmcnt(38) lgkmcnt(3)\nv_mfma_f32_32x32x16_bf16 v[28:43], v[100:103], v[48:51], v[28:43]\nds_read_b128 v[44:47], v88 offset:2560\n" \
  "v_mul_f32 v64, v64, v83\nv_mul_f32 v65, v65, v83\nv_mul_f32 v66, v66, v83\ns_waitcnt vmcnt(37) lgkmcnt(3)\n" \
  "v_mfma_f32_32x32x16_bf16 v[28:43], v[104:107], v[52:55], v[28:43]\nds_read_b128 v[48:51], v88 offset:3072\nv_cvt_pk_bf16_f32 v64, v64, v65\n" \
  "v_mul_f32 v67, v67, v83\nv_mul_f32 v68, v68, v83\ns_waitcnt vmcnt(36) lgkmcnt(3)\nv_mfma_f32_32x32x16_bf16 v[28:43], v[108:111], v[56:59], v[28:43]\n" \
  "ds_read_b128 v[52:55], v88 offset:3584\nv_cvt_pk_bf16_f32 v65, v66, v67\nv_mul_f32 v69, v69, v83\nv_mul_f32 v70, v70, v83\n" \
  "s_waitcnt vmcnt(35) lgkmcnt(3)\nv_mfma_f32_32x32x16_bf16 v[28:43], v[112:115], v[60:63], v[28:43]\nds_read_b128 v[56:59], v88 offset:4096\n" \
  "v_cvt_pk_bf16_f32 v66, v68, v69\nv_mul_f32 v71, v71, v83\nv_mul_f32 v72, v72, v83\ns_waitcnt vmcnt(34) lgkmcnt(3)\n" \
  "v_mfma_f32_32x32x16_bf16 v[28:43], v[116:119], v[44:47], v[28:43]\nds_read_b128 v[60:63], v88 offset:4608\nv_cvt_pk_bf16_f32 v67, v70, v71\n" \
  "v_mul_f32 v73, v73, v83\nds_write_b128 v90, v[64:67]\ns_waitcnt vmcnt(33) lgkmcnt(4)\n" \
  "v_mfma_f32_32x32x16_bf16 v[28:43], v[120:123], v[48:51], v[28:43]\nds_read_b128 v[44:47], v88 offset:5120\nv_mul_f32 v74, v74, v83\n" \
  "v_cvt_pk_bf16_f32 v68, v72, v73\nv_mul_f32 v75, v75, v83\ns_waitcnt vmcnt(32) lgkmcnt(4)\n" \
  "v_mfma_f32_32x32x16_bf16 v[28:43], v[124:127], v[52:55], v[28:43]\nds_read_b128 v[48:51], v88 offset:5632\nv_mul_f32 v76, v76, v83\n" \
  "v_cvt_pk_bf16_f32 v69, v74, v75\nv_mul_f32 v77, v77, v83\ns_waitcnt vmcnt(31) lgkmcnt(4)\n" \
  "v_mfma_f32_32x32x16_bf16 v[28:43], v[128:131], v[56:59], v[28:43]\nds_read_b128 v[52:55], v88 offset:6144\nv_mul_f32 v78, v78, v83\n" \
  "v_cvt_pk_bf16_f32 v70, v76, v77\nv_mul_f32 v79, v79, v83\ns_waitcnt vmcnt(30) lgkmcnt(4)\n" \
  "v_mfma_f32_32x32x16_bf16 v[28:43], v[132:135], v[60:63], v[28:43]\nds_read_b128 v[56:59], v88 offset:6656\nv_cvt_pk_bf16_f32 v71, v78, v79\n" \
  "ds_write_b128 v90, v[68:71] offset:32\nds_write_b32 %[mda], v27\ns_waitcnt vmcnt(29) lgkmcnt(5)\n" \
  "v_mfma_f32_32x32x16_bf16 v[28:43], v[136:139], v[44:47], v[28:43]\nds_read_b128 v[60:63], v88 offset:7168\ns_waitcnt vmcnt(28) lgkmcnt(5)\n" \
  "v_mfma_f32_32x32x16_bf16 v[28:43], v[140:143], v[48:51], v[28:43]\nds_read_b128 v[44:47], v88 offset:7680\ns_waitcnt vmcnt(27) lgkmcnt(5)\n" \
  "v_mfma_f32_32x32x16_bf16 v[28:43], v[144:147], v[52:55], v[28:43]\nds_read_b128 v[48:51], v88 offset:8192\ns_waitcnt vmcnt(26) lgkmcnt(5)\n" \
  "v_mfma_f32_32x32x16_bf16 v[28:43], v[148:151], v[56:59], v[28:43]\nds_read_b128 v[52:55], v88 offset:8704\ns_waitcnt vmcnt(25) lgkmcnt(3)\n" \
  "v_mfma_f32_32x32x16_bf16 v[28:43], v[152:155], v[60:63], v[28:43]\nds_read_b128 v[56:59], v88 offset:9216\nds_read_b128 v[84:87], v89\n" \
  "s_waitcnt vmcnt(24) lgkmcnt(4)\nv_mfma_f32_32x32x16_bf16 v[28:43], v[156:159], v[44:47], v[28:43]\nds_read_b128 v[60:63], v88 offset:9728\n" \
  "s_waitcnt vmcnt(23) lgkmcnt(4)\nv_mfma_f32_32x32x16_bf16 v[28:43], v[160:163], v[48:51], v[28:43]\nds_read_b128 v[44:47], v88 offset:10240\n" \
  "s_waitcnt vmcnt(22) lgkmcnt(4)\nv_mfma_f32_32x32x16_bf16 v[28:43], v[164:167], v[52:55], v[28:43]\nds_read_b128 v[48:51], v88 offset:10752\n" \
  "s_waitcnt vmcnt(21) lgkmcnt(4)\nv_mfma_f32_32x32x16_bf16 v[28:43], v[168:171], v[56:59], v[28:43]\nds_read_b128 v[52:55], v88 offset:11264\n" \
  "s_waitcnt vmcnt(20) lgkmcnt(3)\nv_mfma_f32_32x32x16_bf16 v[28:43], v[172:175], v[60:63], v[28:43]\nds_read_b128 v[56:59], v88 offset:11776\n" \
  "v_cmp_gt_u32 vcc, s61, v84\ns_cbranch_vccnz .LX%=_H\n.LX%=_Hb:\ns_waitcnt vmcnt(19) lgkmcnt(3)\n" \
  "v_mfma_f32_32x32x16_bf16 v[28:43], v[176:179], v[44:47], v[28:43]\nds_read_b128 v[60:63], v88 offset:12288\nv_lshl_add_u32 v91, s60, 5, %[hl]\n" \
  "s_mul_i32 s64, s60, 544\ns_waitcnt vmcnt(18) lgkmcnt(3)\nv_mfma_f32_32x32x16_bf16 v[28:43], v[180:183], v[48:51], v[28:43]\n" \
  "ds_read_b128 v[44:47], v88 offset:12800\nv_add_u32 v90, s64, %[ql]\ns_add_u32 s60, s60, 16\ns_waitcnt vmcnt(17) lgkmcnt(3)\n" \
  "v_mfma_f32_32x32x16_bf16 v[28:43], v[184:187], v[52:55], v[28:43]\nds_read_b128 v[48:51], v88 offset:13312\ns_cmp_eq_u32 s60, 159808\n" \
  "s_cselect_b32 s60, 159760, s60\ns_waitcnt vmcnt(16) lgkmcnt(3)\nv_mfma_f32_32x32x16_bf16 v[28:43], v[188:191], v[56:59], v[28:43]\n" \
  "ds_read_b128 v[52:55], v88 offset:13824\nv_mov_b32 v89, s60\ns_mul_i32 s65, s60, 2560\ns_waitcnt vmcnt(15) lgkmcnt(3)\n" \
  "v_mfma_f32_32x32x16_bf16 v[28:43], v[192:195], v[60:63], v[28:43]\nds_read_b128 v[56:59], v88 offset:14336\nv_add_u32 v27, 1, v27\n" \
  "s_add_u32 s64, s61, 1\ns_waitcnt vmcnt(14) lgkmcnt(3)\nv_mfma_f32_32x32x16_bf16 v[28:43], v[196:199], v[44:47], v[28:43]\n" \
  "ds_read_b128 v[60:63], v88 offset:14848\ns_add_u32 s61, s61, 2\ns_sub_u32 s63, s63, 1\ns_waitcnt vmcnt(13) lgkmcnt(3)\n" \
  "v_mfma_f32_32x32x16_bf16 v[28:43], v[200:203], v[48:51], v[28:43]\nds_read_b128 v[44:47], v88 offset:15360\ns_cselect_b32 s62, -1, s64\n" \
  "s_waitcnt vmcnt(12) lgkmcnt(3)\nv_mfma_f32_32x32x16_bf16 v[28:43], v[204:207], v[52:55], v[28:43]\nds_read_b128 v[48:51], v88 offset:15872\n" \
  "s_waitcnt vmcnt(11) lgkmcnt(3)\nv_mfma_f32_32x32x16_bf16 v[28:43], v[208:211], v[56:59], v[28:43]\nds_read_b128 v[52:55], v88 offset:16384\n" \
  "s_waitcnt vmcnt(10) lgkmcnt(3)\nv_mfma_f32_32x32x16_bf16 v[28:43], v[212:215], v[60:63], v[28:43]\nds_read_b128 v[56:59], v88 offset:16896\n" \
  "s_waitcnt vmcnt(9) lgkmcnt(3)\nv_mfma_f32_32x32x16_bf16 v[28:43], v[216:219], v[44:47], v[28:43]\nds_read_b128 v[60:63], v88 offset:17408\n" \
  "s_waitcnt vmcnt(8) lgkmcnt(3)\nv_mfma_f32_32x32x16_bf16 v[28:43], v[220:223], v[48:51], v[28:43]\nds_read_b128 v[44:47], v88 offset:17920\n" \
  "s_waitcnt vmcnt(7) lgkmcnt(3)\nv_mfma_f32_32x32x16_bf16 v[28:43], v[224:227], v[52:55], v[28:43]\nds_read_b128 v[48:51], v88 offset:18432\n" \
  "s_waitcnt vmcnt(6) lgkmcnt(3)\nv_mfma_f32_32x32x16_bf16 v[28:43], v[228:231], v[56:59], v[28:43]\nds_read_b128 v[52:55], v88 offset:18944\n" \
  "s_waitcnt vmcnt(5) lgkmcnt(3)\nv_mfma_f32_32x32x16_bf16 v[28:43], v[232:235], v[60:63], v[28:43]\nds_read_b128 v[56:59], v88 offset:19456\n" \
  "ds_read_b128 v[84:87], v89\ns_waitcnt vmcnt(4) lgkmcnt(4)\nv_mfma_f32_32x32x16_bf16 v[28:43], v[236:239], v[44:47], v[28:43]\n" \
  "ds_read_b128 v[60:63], v88 offset:19968\ns_waitcnt vmcnt(3) lgkmcnt(4)\nv_mfma_f32_32x32x16_bf16 v[28:43], v[240:243], v[48:51], v[28:43]\n" \
  "ds_read_b128 v[80:83], v91\nds_write_b32 %[mda], v27 offset:32\nv_add_u32 v88, s65, %[xl]\ns_waitcnt vmcnt(2) lgkmcnt(5)\n" \
  "v_mfma_f32_32x32x16_bf16 v[28:43], v[244:247], v[52:55], v[28:43]\nds_read_b128 v[44:47], v88 offset:0\ns_waitcnt vmcnt(1) lgkmcnt(5)\n" \
  "v_mfma_f32_32x32x16_bf16 v[28:43], v[248:251], v[56:59], v[28:43]\nds_read_b128 v[48:51], v88 offset:512\ns_waitcnt vmcnt(0) lgkmcnt(4)\n" \
  "v_mfma_f32_32x32x16_bf16 v[28:43], v[252:255], v[60:63], v[28:43]\nds_read_b128 v[52:55], v88 offset:1024\nv_min3_u32 v84, v84, v86, v87\n" \
  "v_cmp_gt_u32 vcc, s62, v84\n" \
  XTS(2) \
  "s_waitcnt lgkmcnt(4)\nv_mfma_f32_32x32x16_bf16 v[64:79], v[92:95], v[80:83], v[28:43]\nds_read_b128 v[56:59], v88 offset:1536\n" \
  "s_cbranch_vccz .LX%=_T\n.LX%=_Q:\nds_read_b64 v[84:85], v89 offset:8\ns_waitcnt lgkmcnt(0)\nv_min_u32 v84, v84, v85\nv_cmp_gt_u32 vcc, s64, v84\n" \
  "s_cbranch_vccz .LX%=_Qd\ns_sub_u32 %[bud], %[bud], 1\ns_cmp_gt_i32 %[bud], 0\ns_cbranch_scc0 .LX%=_Qd\ns_sleep 1\ns_branch .LX%=_Q\n.LX%=_Qd:\n" \
  "s_nop 7\ns_nop 7\nv_pk_mul_f32 v[64:65], v[64:65], v[82:83] op_sel:[0,1]\nv_cvt_pk_bf16_f32 v64, v64, v65\n" \
  "v_pk_mul_f32 v[66:67], v[66:67], v[82:83] op_sel:[0,1]\nv_cvt_pk_bf16_f32 v65, v66, v67\nv_pk_mul_f32 v[68:69], v[68:69], v[82:83] op_sel:[0,1]\n" \
  "v_cvt_pk_bf16_f32 v66, v68, v69\nv_pk_mul_f32 v[70:71], v[70:71], v[82:83] op_sel:[0,1]\nv_cvt_pk_bf16_f32 v67, v70, v71\n" \
  "ds_write_b128 v90, v[64:67]\nv_pk_mul_f32 v[72:73], v[72:73], v[82:83] op_sel:[0,1]\nv_cvt_pk_bf16_f32 v68, v72, v73\n" \
  "v_pk_mul_f32 v[74:75], v[74:75], v[82:83] op_sel:[0,1]\nv_cvt_pk_bf16_f32 v69, v74, v75\nv_pk_mul_f32 v[76:77], v[76:77], v[82:83] op_sel:[0,1]\n" \
  "v_cvt_pk_bf16_f32 v70, v76, v77\nv_pk_mul_f32 v[78:79], v[78:79], v[82:83] op_sel:[0,1]\nv_cvt_pk_bf16_f32 v71, v78, v79\n" \
  "ds_write_b128 v90, v[68:71] offset:32\nds_write_b32 %[mda], v27\ns_cmp_lt_i32 s63, 0\ns_cbranch_scc1 .LX%=_E\n.LX%=_N:\nv_mov_b32 v90, %[dl]\n" \
  ".LX%=_P:\nds_read_b32 v84, v89\ns_waitcnt lgkmcnt(0)\nv_cmp_gt_u32 vcc, s64, v84\ns_cbranch_vccz .LX%=_Pd\ns_sub_u32 %[bud], %[bud], 1\n" \
  "s_cmp_gt_i32 %[bud], 0\ns_cbranch_scc0 .LX%=_Pd\ns_sleep 1\ns_branch .LX%=_P\n.LX%=_Pd:\nds_read_b128 v[44:47], v88 offset:0\n" \
  "ds_read_b128 v[48:51], v88 offset:512\nds_read_b128 v[52:55], v88 offset:1024\nds_read_b128 v[56:59], v88 offset:1536\n" \
  XTP(0) \
  "s_branch .LX%=_T\n.LX%=_H:\nds_read_b32 v84, v89\ns_waitcnt lgkmcnt(0)\nv_cmp_gt_u32 vcc, s61, v84\ns_cbranch_vccz .LX%=_Hd\n" \
  "s_sub_u32 %[bud], %[bud], 1\ns_cmp_gt_i32 %[bud], 0\ns_cbranch_scc0 .LX%=_Hd\ns_sleep 1\ns_branch .LX%=_H\n.LX%=_Hd:\ns_branch .LX%=_Hb\n.LX%=_E:\n" \
  "s_setprio 3\ns_waitcnt lgkmcnt(0)\n"
#define XPROG1 \
  "s_mov_b32 s60, %[ra]\ns_mov_b32 s63, 7\ns_lshl_b32 s61, %[base], 1\ns_add_u32 s64, s61, 1\ns_add_u32 s61, s61, 2\nv_mov_b32 v89, s60\n" \
  "s_mul_i32 s65, s60, 2560\nv_mov_b32 v27, %[base]\nv_add_u32 v88, s65, %[xl]\ns_mov_b32 s66, %[h1]\n" \
  XTI(0) \
  "s_setprio 2\ns_branch .LX%=_N\n.LX%=_T:\n" \
  XTS(0) \
  "s_waitcnt lgkmcnt(3)\nv_mfma_f32_32x32x16_bf16 v[28:43], v[96:99], v[44:47], 0\nds_read_b128 v[60:63], v88 offset:2048\ns_waitcnt lgkmcnt(3)\n" \
  "v_mfma_f32_32x32x16_bf16 v[28:43], v[100:103], v[48:51], v[28:43]\nds_read_b128 v[44:47], v88 offset:2560\nv_mul_f32 v64, v64, v83\n" \
  "v_mul_f32 v65, v65, v83\nv_mul_f32 v64, v64, |v64|\ns_waitcnt lgkmcnt(3)\nv_mfma_f32_32x32x16_bf16 v[28:43], v[104:107], v[52:55], v[28:43]\n" \
  "ds_read_b128 v[48:51], v88 offset:3072\nv_mul_f32 v65, v65, |v65|\nv_cvt_pk_bf16_f32 v64, v64, v65\nv_mul_f32 v66, v66, v83\ns_waitcnt lgkmcnt(3)\n" \
  "v_mfma_f32_32x32x16_bf16 v[28:43], v[108:111], v[56:59], v[28:43]\nds_read_b128 v[52:55], v88 offset:3584\nv_mul_f32 v67, v67, v83\n" \
  "v_pk_max_i16 v64, v64, 0\nv_mul_f32 v66, v66, |v66|\ns_waitcnt lgkmcnt(3)\nv_mfma_f32_32x32x16_bf16 v[28:43], v[112:115], v[60:63], v[28:43]\n" \
  "ds_read_b128 v[56:59], v88 offset:4096\nv_mul_f32 v67, v67, |v67|\nv_or_b32 v64, %[mark], v64\nv_cvt_pk_bf16_f32 v65, v66, v67\ns_waitcnt lgkmcnt(3)\n" \
  "v_mfma_f32_32x32x16_bf16 v[28:43], v[116:119], v[44:47], v[28:43]\nds_read_b128 v[60:63], v88 offset:4608\nv_mul_f32 v68, v68, v83\n" \
  "v_mul_f32 v69, v69, v83\nv_pk_max_i16 v65, v65, 0\ns_waitcnt lgkmcnt(3)\nv_mfma_f32_32x32x16_bf16 v[28:43], v[120:123], v[48:51], v[28:43]\n" \
  "ds_read_b128 v[44:47], v88 offset:5120\nv_mul_f32 v68, v68, |v68|\nv_mul_f32 v69, v69, |v69|\nv_cvt_pk_bf16_f32 v66, v68, v69\ns_waitcnt lgkmcnt(3)\n" \
  "v_mfma_f32_32x32x16_bf16 v[28:43], v[124:127], v[52:55], v[28:43]\nds_read_b128 v[48:51], v88 offset:5632\nv_mul_f32 v70, v70, v83\n" \
  "v_mul_f32 v71, v71, v83\nv_pk_max_i16 v66, v66, 0\ns_waitcnt lgkmcnt(3)\nv_mfma_f32_32x32x16_bf16 v[28:43], v[128:131], v[56:59], v[28:43]\n" \
  "ds_read_b128 v[52:55], v88 offset:6144\nv_mul_f32 v70, v70, |v70|\nv_mul_f32 v71, v71, |v71|\nv_cvt_pk_bf16_f32 v67, v70, v71\ns_waitcnt lgkmcnt(3)\n" \
  "v_mfma_f32_32x32x16_bf16 v[28:43], v[132:135], v[60:63], v[28:43]\nds_read_b128 v[56:59], v88 offset:6656\nv_mul_f32 v72, v72, v83\n" \
  "v_mul_f32 v73, v73, v83\nv_pk_max_i16 v67, v67, 0\ns_waitcnt lgkmcnt(3)\nv_mfma_f32_32x32x16_bf16 v[28:43], v[136:139], v[44:47], v[28:43]\n" \
  "ds_read_b128 v[60:63], v88 offset:7168\nbuffer_store_dwordx4 v[64:67], %[ql], %[rs], s67 offen sc1\nv_mul_f32 v72, v72, |v72|\n" \
  "v_mul_f32 v73, v73, |v73|\ns_waitcnt lgkmcnt(3)\nv_mfma_f32_32x32x16_bf16 v[28:43], v[140:143], v[48:51], v[28:43]\n" \
  "ds_read_b128 v[44:47], v88 offset:7680\nv_cvt_pk_bf16_f32 v68, v72, v73\nv_mul_f32 v74, v74, v83\nv_mul_f32 v75, v75, v83\ns_waitcnt lgkmcnt(3)\n" \
  "v_mfma_f32_32x32x16_bf16 v[28:43], v[144:147], v[52:55], v[28:43]\nds_read_b128 v[48:51], v88 offset:8192\nv_pk_max_i16 v68, v68, 0\n" \
  "v_mul_f32 v74, v74, |v74|\nv_mul_f32 v75, v75, |v75|\ns_waitcnt lgkmcnt(3)\nv_mfma_f32_32x32x16_bf16 v[28:43], v[148:151], v[56:59], v[28:43]\n" \
  "ds_read_b128 v[52:55], v88 offset:8704\nv_or_b32 v68, %[mark], v68\nv_cvt_pk_bf16_f32 v69, v74, v75\nv_mul_f32 v76, v76, v83\ns_waitcnt lgkmcnt(3)\n" \
  "v_mfma_f32_32x32x16_bf16 v[28:43], v[152:155], v[60:63], v[28:43]\nds_read_b128 v[56:59], v88 offset:9216\nds_read_b128 v[84:87], v89\n" \
  "v_mul_f32 v77, v77, v83\nv_pk_max_i16 v69, v69, 0\ns_waitcnt lgkmcnt(4)\nv_mfma_f32_32x32x16_bf16 v[28:43], v[156:159], v[44:47], v[28:43]\n" \
  "ds_read_b128 v[60:63], v88 offset:9728\nv_mul_f32 v76, v76, |v76|\nv_mul_f32 v77, v77, |v77|\nv_cvt_pk_bf16_f32 v70, v76, v77\ns_waitcnt lgkmcnt(4)\n" \
  "v_mfma_f32_32x32x16_bf16 v[28:43], v[160:163], v[48:51], v[28:43]\nds_read_b128 v[44:47], v88 offset:10240\nv_mul_f32 v78, v78, v83\n" \
  "v_mul_f32 v79, v79, v83\nv_pk_max_i16 v70, v70, 0\ns_waitcnt lgkmcnt(4)\nv_mfma_f32_32x32x16_bf16 v[28:43], v[164:167], v[52:55], v[28:43]\n" \
  "ds_read_b128 v[48:51], v88 offset:10752\nv_mul_f32 v78, v78, |v78|\nv_mul_f32 v79, v79, |v79|\nv_cvt_pk_bf16_f32 v71, v78, v79\ns_waitcnt lgkmcnt(4)\n" \
  "v_mfma_f32_32x32x16_bf16 v[28:43], v[168:171], v[56:59], v[28:43]\nds_read_b128 v[52:55], v88 offset:11264\nv_pk_max_i16 v71, v71, 0\n" \
  "buffer_store_dwordx4 v[68:71], %[ql], %[rs], s67 offen offset:512 sc1\ns_waitcnt lgkmcnt(3)\n" \
  "v_mfma_f32_32x32x16_bf16 v[28:43], v[172:175], v[60:63], v[28:43]\nds_read_b128 v[56:59], v88 offset:11776\nv_cmp_gt_u32 vcc, s61, v84\n" \
  "s_cbranch_vccnz .LX%=_H\n.LX%=_Hb:\ns_waitcnt lgkmcnt(3)\nv_mfma_f32_32x32x16_bf16 v[28:43], v[176:179], v[44:47], v[28:43]\n" \
  "ds_read_b128 v[60:63], v88 offset:12288\nv_lshl_add_u32 v91, s60, 5, %[hl]\ns_sub_u32 s66, s66, 110592\ns_waitcnt lgkmcnt(3)\n" \
  "v_mfma_f32_32x32x16_bf16 v[28:43], v[180:183], v[48:51], v[28:43]\nds_read_b128 v[44:47], v88 offset:12800\ns_mov_b32 s67, s66\n" \
  "s_add_u32 s60, s60, 16\ns_waitcnt lgkmcnt(3)\nv_mfma_f32_32x32x16_bf16 v[28:43], v[184:187], v[52:55], v[28:43]\n" \
  "ds_read_b128 v[48:51], v88 offset:13312\ns_cmp_eq_u32 s60, 159808\ns_cselect_b32 s60, 159760, s60\ns_waitcnt lgkmcnt(3)\n" \
  "v_mfma_f32_32x32x16_bf16 v[28:43], v[188:191], v[56:59], v[28:43]\nds_read_b128 v[52:55], v88 offset:13824\nv_mov_b32 v89, s60\n" \
  "s_mul_i32 s65, s60, 2560\ns_waitcnt lgkmcnt(3)\nv_mfma_f32_32x32x16_bf16 v[28:43], v[192:195], v[60:63], v[28:43]\n" \
  "ds_read_b128 v[56:59], v88 offset:14336\nv_add_u32 v27, 1, v27\ns_add_u32 s64, s61, 1\ns_waitcnt lgkmcnt(3)\n" \
  "v_mfma_f32_32x32x16_bf16 v[28:43], v[196:199], v[44:47], v[28:43]\nds_read_b128 v[60:63], v88 offset:14848\ns_add_u32 s61, s61, 2\n" \
  "s_sub_u32 s63, s63, 1\ns_waitcnt lgkmcnt(3)\nv_mfma_f32_32x32x16_bf16 v[28:43], v[200:203], v[48:51], v[28:43]\n" \
  "ds_read_b128 v[44:47], v88 offset:15360\ns_cselect_b32 s62, -1, s64\ns_waitcnt lgkmcnt(3)\n" \
  "v_mfma_f32_32x32x16_bf16 v[28:43], v[204:207], v[52:55], v[28:43]\nds_read_b128 v[48:51], v88 offset:15872\ns_waitcnt lgkmcnt(3)\n" \
  "v_mfma_f32_32x32x16_bf16 v[28:43], v[208:211], v[56:59], v[28:43]\nds_read_b128 v[52:55], v88 offset:16384\ns_waitcnt lgkmcnt(3)\n" \
  "v_mfma_f32_32x32x16_bf16 v[28:43], v[212:215], v[60:63], v[28:43]\nds_read_b128 v[56:59], v88 offset:16896\ns_waitcnt lgkmcnt(3)\n" \
  "v_mfma_f32_32x32x16_bf16 v[28:43], v[216:219], v[44:47], v[28:43]\nds_read_b128 v[60:63], v88 offset:17408\ns_waitcnt lgkmcnt(3)\n" \
  "v_mfma_f32_32x32x16_bf16 v[28:43], v[220:223], v[48:51], v[28:43]\nds_read_b128 v[44:47], v88 offset:17920\ns_waitcnt lgkmcnt(3)\n" \
  "v_mfma_f32_32x32x16_bf16 v[28:43], v[224:227], v[52:55], v[28:43]\nds_read_b128 v[48:51], v88 offset:18432\ns_waitcnt lgkmcnt(3)\n" \
  "v_mfma_f32_32x32x16_bf16 v[28:43], v[228:231], v[56:59], v[28:43]\nds_read_b128 v[52:55], v88 offset:18944\ns_waitcnt lgkmcnt(3)\n" \
  "v_mfma_f32_32x32x16_bf16 v[28:43], v[232:235], v[60:63], v[28:43]\nds_read_b128 v[56:59], v88 offset:19456\nds_read_b128 v[84:87], v89\n" \
  "s_waitcnt lgkmcnt(4)\nv_mfma_f32_32x32x16_bf16 v[28:43], v[236:239], v[44:47], v[28:43]\nds_read_b128 v[60:63], v88 offset:19968\n" \
  "s_waitcnt lgkmcnt(4)\nv_mfma_f32_32x32x16_bf16 v[28:43], v[240:243], v[48:51], v[28:43]\nds_read_b128 v[80:83], v91\n" \
  "ds_write_b32 %[mda], v27 offset:32\nv_add_u32 v88, s65, %[xl]\ns_waitcnt lgkmcnt(5)\n" \
  "v_mfma_f32_32x32x16_bf16 v[28:43], v[244:247], v[52:55], v[28:43]\nds_read_b128 v[44:47], v88 offset:0\ns_waitcnt lgkmcnt(5)\n" \
  "v_mfma_f32_32x32x16_bf16 v[28:43], v[248:251], v[56:59], v[28:43]\nds_read_b128 v[48:51], v88 offset:512\ns_waitcnt lgkmcnt(4)\n" \
  "v_mfma_f32_32x32x16_bf16 v[28:43], v[252:255], v[60:63], v[28:43]\nds_read_b128 v[52:55], v88 offset:1024\nv_cmp_gt_u32 vcc, s62, v84\n" \
  XTS(2) \
  "s_waitcnt lgkmcnt(4)\nv_mfma_f32_32x32x16_bf16 v[64:79], v[92:95], v[80:83], v[28:43]\nds_read_b128 v[56:59], v88 offset:1536\n" \
  "s_cbranch_vccz .LX%=_T\ns_nop 7\ns_nop 7\nv_pk_mul_f32 v[64:65], v[64:65], v[82:83] op_sel:[0,1]\nv_mul_f32 v64, v64, |v64|\n" \
  "v_mul_f32 v65, v65, |v65|\nv_cvt_pk_bf16_f32 v64, v64, v65\nv_pk_max_i16 v64, v64, 0\nv_or_b32 v64, %[mark], v64\n" \
  "v_pk_mul_f32 v[66:67], v[66:67], v[82:83] op_sel:[0,1]\nv_mul_f32 v66, v66, |v66|\nv_mul_f32 v67, v67, |v67|\nv_cvt_pk_bf16_f32 v65, v66, v67\n" \
  "v_pk_max_i16 v65, v65, 0\nv_pk_mul_f32 v[68:69], v[68:69], v[82:83] op_sel:[0,1]\nv_mul_f32 v68, v68, |v68|\nv_mul_f32 v69, v69, |v69|\n" \
  "v_cvt_pk_bf16_f32 v66, v68, v69\nv_pk_max_i16 v66, v66, 0\nv_pk_mul_f32 v[70:71], v[70:71], v[82:83] op_sel:[0,1]\nv_mul_f32 v70, v70, |v70|\n" \
  "v_mul_f32 v71, v71, |v71|\nv_cvt_pk_bf16_f32 v67, v70, v71\nv_pk_max_i16 v67, v67, 0\nbuffer_store_dwordx4 v[64:67], %[ql], %[rs], s67 offen sc1\n" \
  "v_pk_mul_f32 v[72:73], v[72:73], v[82:83] op_sel:[0,1]\nv_mul_f32 v72, v72, |v72|\nv_mul_f32 v73, v73, |v73|\nv_cvt_pk_bf16_f32 v68, v72, v73\n" \
  "v_pk_max_i16 v68, v68, 0\nv_or_b32 v68, %[mark], v68\nv_pk_mul_f32 v[74:75], v[74:75], v[82:83] op_sel:[0,1]\nv_mul_f32 v74, v74, |v74|\n" \
  "v_mul_f32 v75, v75, |v75|\nv_cvt_pk_bf16_f32 v69, v74, v75\nv_pk_max_i16 v69, v69, 0\nv_pk_mul_f32 v[76:77], v[76:77], v[82:83] op_sel:[0,1]\n" \
  "v_mul_f32 v76, v76, |v76|\nv_mul_f32 v77, v77, |v77|\nv_cvt_pk_bf16_f32 v70, v76, v77\nv_pk_max_i16 v70, v70, 0\n" \
  "v_pk_mul_f32 v[78:79], v[78:79], v[82:83] op_sel:[0,1]\nv_mul_f32 v78, v78, |v78|\nv_mul_f32 v79, v79, |v79|\nv_cvt_pk_bf16_f32 v71, v78, v79\n" \
  "v_pk_max_i16 v71, v71, 0\nbuffer_store_dwordx4 v[68:71], %[ql], %[rs], s67 offen offset:512 sc1\ns_cmp_lt_i32 s63, 0\ns_cbranch_scc1 .LX%=_E\n" \
  ".LX%=_N:\ns_mov_b32 s67, 0x40000000\n.LX%=_P:\nds_read_b32 v84, v89\ns_waitcnt lgkmcnt(0)\nv_cmp_gt_u32 vcc, s64, v84\ns_cbranch_vccz .LX%=_Pd\n" \
  "s_sub_u32 %[bud], %[bud], 1\ns_cmp_gt_i32 %[bud], 0\ns_cbranch_scc0 .LX%=_Pd\ns_sleep 1\ns_branch .LX%=_P\n.LX%=_Pd:\n" \
  "ds_read_b128 v[44:47], v88 offset:0\nds_read_b128 v[48:51], v88 offset:512\nds_read_b128 v[52:55], v88 offset:1024\n" \
  "ds_read_b128 v[56:59], v88 offset:1536\n" \
  XTP(0) \
  "s_branch .LX%=_T\n.LX%=_H:\nds_read_b32 v84, v89\ns_waitcnt lgkmcnt(0)\nv_cmp_gt_u32 vcc, s61, v84\ns_cbranch_vccz .LX%=_Hd\n" \
  "s_sub_u32 %[bud], %[bud], 1\ns_cmp_gt_i32 %[bud], 0\ns_cbranch_scc0 .LX%=_Hd\ns_sleep 1\ns_branch .LX%=_H\n.LX%=_Hd:\ns_branch .LX%=_Hb\n.LX%=_E:\n" \
  "s_setprio 3\ns_waitcnt lgkmcnt(0)\n"
#define XLOAD \
  "buffer_load_dwordx4 v[96:99], %[vo], %[rs], %[so] offen sc1\ns_add_u32 %[so], %[so], %[st]\n" \
  "buffer_load_dwordx4 v[100:103], %[vo], %[rs], %[so] offen sc1\ns_add_u32 %[so], %[so], %[st]\n" \
  "buffer_load_dwordx4 v[104:107], %[vo], %[rs], %[so] offen sc1\ns_add_u32 %[so], %[so], %[st]\n" \
  "buffer_load_dwordx4 v[108:111], %[vo], %[rs], %[so] offen sc1\ns_add_u32 %[so], %[so], %[st]\n" \
  "buffer_load_dwordx4 v[112:115], %[vo], %[rs], %[so] offen sc1\ns_add_u32 %[so], %[so], %[st]\n" \
  "buffer_load_dwordx4 v[116:119], %[vo], %[rs], %[so] offen sc1\ns_add_u32 %[so], %[so], %[st]\n" \
  "buffer_load_dwordx4 v[120:123], %[vo], %[rs], %[so] offen sc1\ns_add_u32 %[so], %[so], %[st]\n" \
  "buffer_load_dwordx4 v[124:127], %[vo], %[rs], %[so] offen sc1\ns_add_u32 %[so], %[so], %[st]\n" \
  "buffer_load_dwordx4 v[128:131], %[vo], %[rs], %[so] offen sc1\ns_add_u32 %[so], %[so], %[st]\n" \
  "buffer_load_dwordx4 v[132:135], %[vo], %[rs], %[so] offen sc1\ns_add_u32 %[so], %[so], %[st]\n" \
  "buffer_load_dwordx4 v[136:139], %[vo], %[rs], %[so] offen sc1\ns_add_u32 %[so], %[so], %[st]\n" \
  "buffer_load_dwordx4 v[140:143], %[vo], %[rs], %[so] offen sc1\ns_add_u32 %[so], %[so], %[st]\n" \
  "buffer_load_dwordx4 v[144:147], %[vo], %[rs], %[so] offen sc1\ns_add_u32 %[so], %[so], %[st]\n" \
  "buffer_load_dwordx4 v[148:151], %[vo], %[rs], %[so] offen sc1\ns_add_u32 %[so], %[so], %[st]\n" \
  "buffer_load_dwordx4 v[152:155], %[vo], %[rs], %[so] offen sc1\ns_add_u32 %[so], %[so], %[st]\n" \
  "buffer_load_dwordx4 v[156:159], %[vo], %[rs], %[so] offen sc1\ns_add_u32 %[so], %[so], %[st]\n" \
  "buffer_load_dwordx4 v[160:163], %[vo], %[rs], %[so] offen sc1\ns_add_u32 %[so], %[so], %[st]\n" \
  "buffer_load_dwordx4 v[164:167], %[vo], %[rs], %[so] offen sc1\ns_add_u32 %[so], %[so], %[st]\n" \
  "buffer_load_dwordx4 v[168:171], %[vo], %[rs], %[so] offen sc1\ns_add_u32 %[so], %[so], %[st]\n" \
  "buffer_load_dwordx4 v[172:175], %[vo], %[rs], %[so] offen sc1\ns_add_u32 %[so], %[so], %[st]\n" \
  "buffer_load_dwordx4 v[176:179], %[vo], %[rs], %[so] offen sc1\ns_add_u32 %[so], %[so], %[st]\n" \
  "buffer_load_dwordx4 v[180:183], %[vo], %[rs], %[so] offen sc1\ns_add_u32 %[so], %[so], %[st]\n" \
  "buffer_load_dwordx4 v[184:187], %[vo], %[rs], %[so] offen sc1\ns_add_u32 %[so], %[so], %[st]\n" \
  "buffer_load_dwordx4 v[188:191], %[vo], %[rs], %[so] offen sc1\ns_add_u32 %[so], %[so], %[st]\n" \
  "buffer_load_dwordx4 v[192:195], %[vo], %[rs], %[so] offen sc1\ns_add_u32 %[so], %[so], %[st]\n" \
  "buffer_load_dwordx4 v[196:199], %[vo], %[rs], %[so] offen sc1\ns_add_u32 %[so], %[so], %[st]\n" \
  "buffer_load_dwordx4 v[200:203], %[vo], %[rs], %[so] offen sc1\ns_add_u32 %[so], %[so], %[st]\n" \
  "buffer_load_dwordx4 v[204:207], %[vo], %[rs], %[so] offen sc1\ns_add_u32 %[so], %[so], %[st]\n" \
  "buffer_load_dwordx4 v[208:211], %[vo], %[rs], %[so] offen sc1\ns_add_u32 %[so], %[so], %[st]\n" \
  "buffer_load_dwordx4 v[212:215], %[vo], %[rs], %[so] offen sc1\ns_add_u32 %[so], %[so], %[st]\n" \
  "buffer_load_dwordx4 v[216:219], %[vo], %[rs], %[so] offen sc1\ns_add_u32 %[so], %[so], %[st]\n" \
  "buffer_load_dwordx4 v[220:223], %[vo], %[rs], %[so] offen sc1\ns_add_u32 %[so], %[so], %[st]\n" \
  "buffer_load_dwordx4 v[224:227], %[vo], %[rs], %[so] offen sc1\ns_add_u32 %[so], %[so], %[st]\n" \
  "buffer_load_dwordx4 v[228:231], %[vo], %[rs], %[so] offen sc1\ns_add_u32 %[so], %[so], %[st]\n" \
  "buffer_load_dwordx4 v[232:235], %[vo], %[rs], %[so] offen sc1\ns_add_u32 %[so], %[so], %[st]\n" \
  "buffer_load_dwordx4 v[236:239], %[vo], %[rs], %[so] offen sc1\ns_add_u32 %[so], %[so], %[st]\n" \
  "buffer_load_dwordx4 v[240:243], %[vo], %[rs], %[so] offen sc1\ns_add_u32 %[so], %[so], %[st]\n" \
  "buffer_load_dwordx4 v[244:247], %[vo], %[rs], %[so] offen sc1\ns_add_u32 %[so], %[so], %[st]\n" \
  "buffer_load_dwordx4 v[248:251], %[vo], %[rs], %[so] offen sc1\ns_add_u32 %[so], %[so], %[st]\n" \
  "buffer_load_dwordx4 v[252:255], %[vo], %[rs], %[so] offen sc1\n"
#define XLOAD0 \
  "buffer_load_dwordx4 v[96:99], %[vo], %[rs], %[so] offen sc1\ns_add_u32 %[so], %[so], %[st]\n" \
  "buffer_load_dwordx4 v[100:103], %[vo], %[rs], %[so] offen sc1\ns_add_u32 %[so], %[so], %[st]\n" \
  "buffer_load_dwordx4 v[104:107], %[vo], %[rs], %[so] offen sc1\ns_add_u32 %[so], %[so], %[st]\n" \
  "buffer_load_dwordx4 v[108:111], %[vo], %[rs], %[so] offen sc1\ns_add_u32 %[so], %[so], %[st]\n" \
  "buffer_load_dwordx4 v[112:115], %[vo], %[rs], %[so] offen sc1\ns_add_u32 %[so], %[so], %[st]\n" \
  "buffer_load_dwordx4 v[116:119], %[vo], %[rs], %[so] offen sc1\ns_add_u32 %[so], %[so], %[st]\n" \
  "buffer_load_dwordx4 v[120:123], %[vo], %[rs], %[so] offen sc1\ns_add_u32 %[so], %[so], %[st]\n" \
  "buffer_load_dwordx4 v[124:127], %[vo], %[rs], %[so] offen sc1\ns_add_u32 %[so], %[so], %[st]\n" \
  "buffer_load_dwordx4 v[128:131], %[vo], %[rs], %[so] offen sc1\ns_add_u32 %[so], %[so], %[st]\n" \
  "buffer_load_dwordx4 v[132:135], %[vo], %[rs], %[so] offen sc1\ns_add_u32 %[so], %[so], %[st]\n" \
  "buffer_load_dwordx4 v[136:139], %[vo], %[rs], %[so] offen sc1\ns_add_u32 %[so], %[so], %[st]\n" \
  "buffer_load_dwordx4 v[140:143], %[vo], %[rs], %[so] offen sc1\ns_add_u32 %[so], %[so], %[st]\n" \
  "buffer_load_dwordx4 v[144:147], %[vo], %[rs], %[so] offen sc1\ns_add_u32 %[so], %[so], %[st]\n" \
  "buffer_load_dwordx4 v[148:151], %[vo], %[rs], %[so] offen sc1\ns_add_u32 %[so], %[so], %[st]\n" \
  "buffer_load_dwordx4 v[152:155], %[vo], %[rs], %[so] offen sc1\ns_add_u32 %[so], %[so], %[st]\n" \
  "buffer_load_dwordx4 v[156:159], %[vo], %[rs], %[so] offen sc1\n"
#define XLOAD1 \
  "buffer_load_dwordx4 v[160:163], %[vo], %[rs], %[so] offen sc1\ns_add_u32 %[so], %[so], %[st]\n" \
  "buffer_load_dwordx4 v[164:167], %[vo], %[rs], %[so] offen sc1\ns_add_u32 %[so], %[so], %[st]\n" \
  "buffer_load_dwordx4 v[168:171], %[vo], %[rs], %[so] offen sc1\ns_add_u32 %[so], %[so], %[st]\n" \
  "buffer_load_dwordx4 v[172:175], %[vo], %[rs], %[so] offen sc1\ns_add_u32 %[so], %[so], %[st]\n" \
  "buffer_load_dwordx4 v[176:179], %[vo], %[rs], %[so] offen sc1\ns_add_u32 %[so], %[so], %[st]\n" \
  "buffer_load_dwordx4 v[180:183], %[vo], %[rs], %[so] offen sc1\ns_add_u32 %[so], %[so], %[st]\n" \
  "buffer_load_dwordx4 v[184:187], %[vo], %[rs], %[so] offen sc1\ns_add_u32 %[so], %[so], %[st]\n" \
  "buffer_load_dwordx4 v[188:191], %[vo], %[rs], %[so] offen sc1\ns_add_u32 %[so], %[so], %[st]\n" \
  "buffer_load_dwordx4 v[192:195], %[vo], %[rs], %[so] offen sc1\ns_add_u32 %[so], %[so], %[st]\n" \
  "buffer_load_dwordx4 v[196:199], %[vo], %[rs], %[so] offen sc1\ns_add_u32 %[so], %[so], %[st]\n" \
  "buffer_load_dwordx4 v[200:203], %[vo], %[rs], %[so] offen sc1\ns_add_u32 %[so], %[so], %[st]\n" \
  "buffer_load_dwordx4 v[204:207], %[vo], %[rs], %[so] offen sc1\ns_add_u32 %[so], %[so], %[st]\n" \
  "buffer_load_dwordx4 v[208:211], %[vo], %[rs], %[so] offen sc1\ns_add_u32 %[so], %[so], %[st]\n" \
  "buffer_load_dwordx4 v[212:215], %[vo], %[rs], %[so] offen sc1\ns_add_u32 %[so], %[so], %[st]\n" \
  "buffer_load_dwordx4 v[216:219], %[vo], %[rs], %[so] offen sc1\ns_add_u32 %[so], %[so], %[st]\n" \
  "buffer_load_dwordx4 v[220:223], %[vo], %[rs], %[so] offen sc1\n"
#define XLOAD2 \
  "buffer_load_dwordx4 v[224:227], %[vo], %[rs], %[so] offen sc1\ns_add_u32 %[so], %[so], %[st]\n" \
  "buffer_load_dwordx4 v[228:231], %[vo], %[rs], %[so] offen sc1\ns_add_u32 %[so], %[so], %[st]\n" \
  "buffer_load_dwordx4 v[232:235], %[vo], %[rs], %[so] offen sc1\ns_add_u32 %[so], %[so], %[st]\n" \
  "buffer_load_dwordx4 v[236:239], %[vo], %[rs], %[so] offen sc1\ns_add_u32 %[so], %[so], %[st]\n" \
  "buffer_load_dwordx4 v[240:243], %[vo], %[rs], %[so] offen sc1\ns_add_u32 %[so], %[so], %[st]\n" \
  "buffer_load_dwordx4 v[244:247], %[vo], %[rs], %[so] offen sc1\ns_add_u32 %[so], %[so], %[st]\n" \
  "buffer_load_dwordx4 v[248:251], %[vo], %[rs], %[so] offen sc1\ns_add_u32 %[so], %[so], %[st]\n" \
  "buffer_load_dwordx4 v[252:255], %[vo], %[rs], %[so] offen sc1\n"
#define LPROG \
  "s_mov_b32 %[k], m0\ns_mov_b32 m0, %[dst]\nds_read_b128 v[88:91], %[xa]\ns_branch .LL%=_C\n.LL%=_P:\n" \
  "buffer_load_dwordx4 v[72:75], %[vo], %[rs], %[ro] offen offset:0 sc1\nbuffer_load_dwordx4 v[76:79], %[vo], %[rs], %[ro] offen offset:1024 sc1\n" \
  "buffer_load_dwordx4 v[80:83], %[vo], %[rs], %[ro] offen offset:2048 sc1\nbuffer_load_dwordx4 v[84:87], %[vo], %[rs], %[ro] offen offset:3072 sc1\n" \
  "s_waitcnt vmcnt(3)\nds_read_b128 v[88:91], %[xa]\ns_waitcnt vmcnt(0)\n.LL%=_C:\nv_cmpx_eq_u32 vcc, %[tx], v73\nv_cmpx_eq_u32 vcc, %[ty], v75\n" \
  "v_cmpx_eq_u32 vcc, %[tx], v77\nv_cmpx_eq_u32 vcc, %[ty], v79\nv_cmpx_eq_u32 vcc, %[tx], v81\nv_cmpx_eq_u32 vcc, %[ty], v83\n" \
  "v_cmpx_eq_u32 vcc, %[tx], v85\nv_cmpx_eq_u32 vcc, %[ty], v87\ns_cmp_eq_u64 exec, -1\ns_mov_b64 exec, -1\ns_cbranch_scc1 .LL%=_S\n" \
  "s_sub_u32 %[bud], %[bud], 8\ns_cmp_gt_i32 %[bud], 0\ns_sleep 1\ns_cbranch_scc1 .LL%=_P\n.LL%=_S:\ns_waitcnt lgkmcnt(0)\n" \
  "v_min3_u32 v88, v88, v89, v90\nv_min_u32 v88, v88, v91\nv_cmp_gt_u32 vcc, %[need], v88\ns_cbranch_vccz .LL%=_G\ns_sub_u32 %[bud], %[bud], 1\n" \
  "s_cmp_gt_i32 %[bud], 0\ns_cbranch_scc0 .LL%=_G\ns_sleep 1\nds_read_b128 v[88:91], %[xa]\ns_branch .LL%=_S\n.LL%=_G:\n" \
  "buffer_load_dwordx4 %[vo], %[rs], %[sx] offen offset:0 sc1 lds\nbuffer_load_dwordx4 %[vo], %[rs], %[sx] offen offset:1024 sc1 lds\n" \
  "buffer_load_dwordx4 %[vo], %[rs], %[sx] offen offset:2048 sc1 lds\nbuffer_load_dwordx4 %[vo], %[rs], %[sx] offen offset:3072 sc1 lds\n" \
  LTS(0) \
  "s_add_u32 m0, m0, 20480\ns_add_u32 %[sx], %[sx], 20480\nbuffer_load_dwordx4 %[vo], %[rs], %[sx] offen offset:0 sc1 lds\n" \
  "buffer_load_dwordx4 %[vo], %[rs], %[sx] offen offset:1024 sc1 lds\nbuffer_load_dwordx4 %[vo], %[rs], %[sx] offen offset:2048 sc1 lds\n" \
  "buffer_load_dwordx4 %[vo], %[rs], %[sx] offen offset:3072 sc1 lds\ns_add_u32 m0, m0, -16384\ns_add_u32 %[sx], %[sx], -16384\n" \
  "buffer_load_dwordx4 %[vo], %[rs], %[sx] offen offset:0 sc1 lds\nbuffer_load_dwordx4 %[vo], %[rs], %[sx] offen offset:1024 sc1 lds\n" \
  "buffer_load_dwordx4 %[vo], %[rs], %[sx] offen offset:2048 sc1 lds\nbuffer_load_dwordx4 %[vo], %[rs], %[sx] offen offset:3072 sc1 lds\n" \
  "s_add_u32 m0, m0, 20480\ns_add_u32 %[sx], %[sx], 20480\nbuffer_load_dwordx4 %[vo], %[rs], %[sx] offen offset:0 sc1 lds\n" \
  "buffer_load_dwordx4 %[vo], %[rs], %[sx] offen offset:1024 sc1 lds\nbuffer_load_dwordx4 %[vo], %[rs], %[sx] offen offset:2048 sc1 lds\n" \
  "buffer_load_dwordx4 %[vo], %[rs], %[sx] offen offset:3072 sc1 lds\ns_add_u32 m0, m0, -16384\ns_add_u32 %[sx], %[sx], -16384\n" \
  "buffer_load_dwordx4 %[vo], %[rs], %[sx] offen offset:0 sc1 lds\nbuffer_load_dwordx4 %[vo], %[rs], %[sx] offen offset:1024 sc1 lds\n" \
  "buffer_load_dwordx4 %[vo], %[rs], %[sx] offen offset:2048 sc1 lds\nbuffer_load_dwordx4 %[vo], %[rs], %[sx] offen offset:3072 sc1 lds\n" \
  "s_add_u32 m0, m0, 20480\ns_add_u32 %[sx], %[sx], 20480\nbuffer_load_dwordx4 %[vo], %[rs], %[sx] offen offset:0 sc1 lds\n" \
  "buffer_load_dwordx4 %[vo], %[rs], %[sx] offen offset:1024 sc1 lds\nbuffer_load_dwordx4 %[vo], %[rs], %[sx] offen offset:2048 sc1 lds\n" \
  "buffer_load_dwordx4 %[vo], %[rs], %[sx] offen offset:3072 sc1 lds\ns_add_u32 m0, m0, -16384\ns_add_u32 %[sx], %[sx], -16384\n" \
  "buffer_load_dwordx4 %[vo], %[rs], %[sx] offen offset:0 sc1 lds\nbuffer_load_dwordx4 %[vo], %[rs], %[sx] offen offset:1024 sc1 lds\n" \
  "buffer_load_dwordx4 %[vo], %[rs], %[sx] offen offset:2048 sc1 lds\nbuffer_load_dwordx4 %[vo], %[rs], %[sx] offen offset:3072 sc1 lds\n" \
  "s_add_u32 m0, m0, 20480\ns_add_u32 %[sx], %[sx], 20480\nbuffer_load_dwordx4 %[vo], %[rs], %[sx] offen offset:0 sc1 lds\n" \
  "buffer_load_dwordx4 %[vo], %[rs], %[sx] offen offset:1024 sc1 lds\nbuffer_load_dwordx4 %[vo], %[rs], %[sx] offen offset:2048 sc1 lds\n" \
  "buffer_load_dwordx4 %[vo], %[rs], %[sx] offen offset:3072 sc1 lds\ns_add_u32 m0, m0, -16384\ns_add_u32 %[sx], %[sx], -16384\n" \
  "buffer_load_dwordx4 %[vo], %[rs], %[sx] offen offset:0 sc1 lds\nbuffer_load_dwordx4 %[vo], %[rs], %[sx] offen offset:1024 sc1 lds\n" \
  "buffer_load_dwordx4 %[vo], %[rs], %[sx] offen offset:2048 sc1 lds\nbuffer_load_dwordx4 %[vo], %[rs], %[sx] offen offset:3072 sc1 lds\n" \
  "s_add_u32 m0, m0, 20480\ns_add_u32 %[sx], %[sx], 20480\nbuffer_load_dwordx4 %[vo], %[rs], %[sx] offen offset:0 sc1 lds\n" \
  "buffer_load_dwordx4 %[vo], %[rs], %[sx] offen offset:1024 sc1 lds\nbuffer_load_dwordx4 %[vo], %[rs], %[sx] offen offset:2048 sc1 lds\n" \
  "buffer_load_dwordx4 %[vo], %[rs], %[sx] offen offset:3072 sc1 lds\ns_mov_b32 m0, %[k]\n"
#define XCLOB \
  "v25", "v26", "v27", "v28", "v29", "v30", "v31", "v32", "v33", "v34", "v35", "v36", "v37", "v38", "v39", "v40", "v41", "v42", "v43", "v44", "v45", \
  "v46", "v47", "v48", "v49", "v50", "v51", "v52", "v53", "v54", "v55", "v56", "v57", "v58", "v59", "v60", "v61", "v62", "v63", "v64", "v65", "v66", \
  "v67", "v68", "v69", "v70", "v71", "v72", "v73", "v74", "v75", "v76", "v77", "v78", "v79", "v80", "v81", "v82", "v83", "v84", "v85", "v86", "v87", \
  "v88", "v89", "v90", "v91", "s60", "s61", "s62", "s63", "s64", "s65", "s66", "s67", "s68"
#define ACLOB \
  "v32", "v33", "v34", "v35", "v36", "v37", "v38", "v39", "v40", "v41", "v42", "v43", "v44", "v45", "v46", "v47", "v48", "v49", "v50", "v51", "v52", \
  "v53", "v54", "v55", "v56", "v57", "v58", "v59", "v60", "v61", "v62", "v63", "v64", "v65", "v66", "v67", "v68", "v69", "v70", "v71", "v72", "v73", \
  "v74", "v75", "v76", "v77", "v78", "v79", "v80", "v81", "v82", "v83", "v84", "v85", "v86", "v87", "v88", "v89", "v90", "v91", "s60", "s61", "s62", \
  "s63", "s64", "s65", "s66", "s67", "s68", "s69"

__global__ TSATTR void __launch_bounds__(512, 2) k_fwd(Params p) {
  const int tid = threadIdx.x;
  const int w = rfl(tid >> 6);
  const int bid = blockIdx.x;
  asm volatile("" ::"s"(p.x), "s"(p.wqkv), "s"(p.wo), "s"(p.w1), "s"(p.b1), "s"(p.w2), "s"(p.b2), "s"(p.out), "s"(p.ws));
  const bool isY = bid & 1;
  const int xj = (bid >> 1) & 3, xm_ = bid >> 3;
  const int grp = isY ? 2 * xj + (xm_ >> 4) : xm_ >> 2, rk = isY ? xm_ & 15 : 4 * xj + (xm_ & 3);
  const rsrc_t rws = __builtin_amdgcn_make_buffer_rsrc(p.ws, 0, (int)WS_END, 0x00020000);
  const unsigned gb = grp * GRB;
  const unsigned row0 = (unsigned)(512 * (grp & 3) + (grp >> 2)) * 1280u;
  int budget = SPIN_CAP;
  TS(0);
  v2u e0;
  asm volatile("s_nop 4\n\tbuffer_load_dwordx2 %0, off, %1, %2 sc1" : "=&v"(e0) : "s"(rws), "s"(OFF_E) : "memory");
  if (tid < 64) *(volatile LAS unsigned *)(uintptr_t)(L_CTL + tid * 4) = (tid >= 4 && tid < 16 && (tid & 2)) ? 7u : 0u;
  v2u tag;
  const v4u NONE = {~0u, ~0u, ~0u, ~0u};
  float recS, recQ;
  auto stats = [&](const f32x4 (&y)[5]) AI {
    float s0 = 0.f, s1 = 0.f, q0 = 0.f, q1 = 0.f;
#pragma unroll
    for (int j = 0; j < 5; ++j) psum(s0, s1, y[j]);
    recS = row_sum(s0 + s1);
    const float mu = recS * (1.f / 80.f);
#pragma unroll
    for (int j = 0; j < 5; ++j) psq(q0, q1, y[j], mu);
    recQ = row_sum(q0 + q1);
  };
  auto third = [&](v2u c) AI {
    auto sx = __builtin_amdgcn_permlane16_swap(c.x, c.x, false, false);
    auto sw = __builtin_amdgcn_permlane16_swap(c.y, c.y, false, false);
    return v4u{sx[0], sw[0], sx[1], sw[1]};
  };
  auto rec_store = [&](unsigned vo, unsigned so) AI {
    const v2u tv = tg(tag);
    ast128c(v4u{f2u(recS), tv.x, f2u(recQ), tv.y}, rws, vo, so);
  };
  const unsigned ct = rk & 7, sy = rk >> 3;
  const unsigned xo = gb + OFF_XF + (14 + sy) * XT + ct * 2560, ro = gb + OFF_REC + (14 + sy) * 2048 + ct * 256;
  const rsrc_t rx0 = __builtin_amdgcn_make_buffer_rsrc((void *)p.x, 0, 2048 * 1280, 0x00020000);
  {
    const unsigned lane = tid & 63, g = lane >> 4, hv = lane * 16;
    auto piece = [&](unsigned t, v4u (&d)[4], bool load) AI {
      const unsigned mt = t < 600 ? 0 : t < 800 ? 3 : t < 1350 ? 2 : 1;
      const unsigned tn = t < 600 ? t : t < 800 ? t - 600 : t < 1350 ? t - 800 : t - 1350;
      const unsigned ncb = mt == 0 ? 30 : mt == 1 ? 27 : 10, kb = mt == 0 ? tn / 30 : mt == 1 ? tn / 27 : tn / 10, cb = tn - kb * ncb;
      const unsigned pitch = ncb * 128;
      if (load) {
        const bool isb = kb == 54, prm = mt == 3;
        const unsigned q5 = kb / 5, i5 = kb - 5 * q5;
        const rsrc_t rw = __builtin_amdgcn_make_buffer_rsrc((void *)(mt == 0 ? p.wqkv : mt == 1 ? p.w1 : prm ? p.wo : isb ? p.b2 : p.w2), 0,
                                                            (mt == 2 ? (isb ? 1 : 1728) : 640) * pitch, 0x00020000);
        const unsigned rb = prm ? 160 * q5 + (i5 < 4 ? 40 * i5 : 16) : isb ? 0 : 32 * kb, rs = prm ? (i5 < 4 ? 4 : 40) : 8;
        const unsigned l3 = lane >> 3, vo = (prm ? (l3 & 3) + 20 * (l3 >> 2) : l3) * pitch + cb * 128 + (lane & 7) * 16;
#pragma unroll
        for (int n = 0; n < 4; ++n) d[n] = ld128(rw, vo, (rb + n * rs) * pitch);
        return 0u;
      }
      const unsigned stg = !isY ? L_QKV + w * 4096 : w < 7 ? w * 5120 : L_PART + 8 * PSZ;
#pragma unroll
      for (int n = 0; n < 4; ++n) lds_st128(stg + n * 1024 + hv, d[n]);
      cfence();
      TS(102);
      const unsigned col = cb * 64 + lane, c80 = col / 80, r = col - 80 * c80;
      const unsigned pos = mt < 2 || r >= 64 ? col : 80 * c80 + (r & 32) + 4 * (r & 4) + ((r >> 1) & 12) + (r & 3);
      const unsigned tra = stg + ((lane >> 2) & 3) * 128 + g * 32 + (lane & 3) * 8;
      const unsigned wo = (mt == 0 ? OFF_W0 : mt == 1 ? OFF_W1 : mt == 2 ? OFF_W2 : OFF_W3) + kb * pitch * 32;
      float cs = 0.f;
#pragma unroll
      for (int n = 0; n < 4; ++n) {
        const v2u lo = ldstr(tra + n * 1024), up = ldstr(tra + n * 1024 + 512);
        st128c(v4u{lo.x, lo.y, up.x, up.y}, rws, pos * 16, wo + n * pitch * 8);
        cs = dot1(lo.x, dot1(lo.y, dot1(up.x, dot1(up.y, cs))));
      }
      __builtin_amdgcn_raw_buffer_store_b32(f2u(cs), rws, col * 4, mt < 2 ? OFF_CS + kb * CSP + mt * 7680 : OOB, 16);
      cfence();
      return OFF_WF + ((mt == 0 ? 0 : mt == 1 ? 30 : mt == 2 ? 57 : 67) + cb) * 512 + kb * 8;
    };
    const unsigned t = w * 128 + grp * 16 + rk;
    v4u d[4], d2[4];
    if (isY) {
      const unsigned r1 = (lane + 64) / 10, r2 = (lane + 128) / 10, xt = L_PART + w * PSZ;
      glds3(rx0, lane / 10 * 2400 + hv, r1 * 2400 + hv, lane < 32 ? r2 * 2400 + hv : OOB, row0 + (14 - 2 * w + sy) * 40960 + ct * 160, xt);
      const unsigned t6 = t - 768, t1 = t6 < 32 ? t : t6 < 98 ? 1024 + t : 1349;
      piece(t1, d, true);
      const unsigned co = gb + OFF_ACT + (14 - 2 * w + sy) * HT + ct * 2560;
      st128c(NONE, rws, hv, co); st128c(NONE, rws, hv + 1024, co); st128c(NONE, rws, hv < 512 ? hv + 2048 : OOB, co);
      const unsigned ch = gb + OFF_ACT + (28 + sy) * HT + ct * 6912, vh = w * 1024 + hv < 6912 ? w * 1024 + hv : OOB;
      st128c(NONE, rws, vh, ch); st128c(NONE, rws, vh, ch + 2 * HT);
      sbar();
      vmw<9>();
      const unsigned xa = xt + (lane & 15) * 160;
      const v4u a = lds128(xa + g * 16), b = lds128(xa + g * 16 + 64);
      const v2u c = lds64(xa + g * 8 + 128);
      const unsigned v2 = 2048 + (hv & 240) + ((hv >> 9) << 8) + ((hv & 256) << 22);
      st128c(a, rws, hv, xo - 2 * w * XT); st128c(b, rws, hv + 1024, xo - 2 * w * XT); st128c(third(c), rws, v2, xo - 2 * w * XT);
      TS(101);
      const f32x4 y[5] = {{bflo(a.x), bfhi(a.x), bflo(a.y), bfhi(a.y)}, {bflo(a.z), bfhi(a.z), bflo(a.w), bfhi(a.w)},
                          {bflo(b.x), bfhi(b.x), bflo(b.y), bfhi(b.y)}, {bflo(b.z), bfhi(b.z), bflo(b.w), bfhi(b.w)},
                          {bflo(c.x), bfhi(c.x), bflo(c.y), bfhi(c.y)}};
      stats(y);
      asm volatile("" : "+v"(e0), "+v"(recS), "+v"(recQ)::"memory");
      tag.x = rfl(e0.x) + 1u;
      tag.y = rfl(e0.y) + 1u;
      unsigned f1 = OOB;
      if (t6 < 98) f1 = piece(t1, d, false);
      piece(800 + t, d2, true);
      sbar();
      vmw<4>();
      asm volatile("" ::"v"(d[0]), "v"(d[1]), "v"(d[2]), "v"(d[3]));
      rec_store(hv < 256 ? hv : OOB, ro - w * 4096);
      st64c(tg(tag), rws, lane == 0 ? f1 : OOB, 0);
      TS(105);
      const unsigned f2 = piece(800 + t, d2, false);
      TS(103);
      drain();
      st64c(tg(tag), rws, lane == 0 ? f2 : OOB, 0);
    } else {
      unsigned f1 = OOB;
      if (w < 6) {
        piece(t, d, true);
        f1 = piece(t, d, false);
      }
      TS(103);
      drain();
      asm volatile("" : "+v"(e0)::"memory");
      tag.x = rfl(e0.x) + 1u;
      tag.y = rfl(e0.y) + 1u;
      st64c(tg(tag), rws, lane == 0 ? f1 : OOB, 0);
      TS(104);
      bar();
      if (w == 5) st64c(tg(tag), rws, lane == 0 ? OFF_ACK + bid * 8 : OOB, 0);
    }
    TS(1);
  }

  if (!isY) {
    const int c = rk, set = w >> 2, wq = w & 3;
    const int n1 = 4 * c + wq;
    const bool live = set == 0 || n1 < 54;
    const int step = c >= 14 ? 2 : 1;
    __builtin_amdgcn_s_setprio(3);
    f32x32 W0, W1, W2, W3, W4;
    v4u csf;
    auto colof = [&](int cw, bool ffn) AI {
      const int cc = 4 * wq + (cw >> 3);
      return ffn ? 32 * n1 + cw : 640 * (cc / 5) + 40 * c + 8 * (cc % 5) + (cw & 7);
    };
    auto w_load = [&](int lane, int P) AI {
      const unsigned vo = live ? *(volatile LAS unsigned *)(uintptr_t)(L_STG + 2048 + (wq & 1) * 256 + lane * 4) + 1024 : OOB;
      unsigned so = OFF_W1 + P * 16 * 1728 * 32;
#define WLI [so] "+s"(so) : [vo] "v"(vo), [rs] "s"(rws), [st] "s"(1728 * 32) : "memory", "scc"
      if (P == 0) asm volatile(XLOAD0 : "+{v[96:127]}"(W0), "+{v[128:159]}"(W1), WLI);
      if (P == 1) asm volatile(XLOAD1 : "+{v[160:191]}"(W2), "+{v[192:223]}"(W3), WLI);
      if (P == 2) asm volatile(XLOAD2 : "+{v[224:255]}"(W4), WLI);
    };
    auto flags_of = [&](int lane, bool ffn) AI {
      const unsigned cb = ffn ? 30 + 2 * c + (lane < 32 && c < 13) : umin(colof(8 * (lane >> 4), false) >> 6, 29u);
      return OFF_WF + cb * 512 + umin(lane & 15, 9) * 16;
    };
    auto is_tag = [&](v4u f, unsigned n) AI { return all_true(((f.x ^ (tag.x + n)) | (f.y ^ (tag.y + n)) | (f.z ^ (tag.x + n)) | (f.w ^ (tag.y + n))) == 0u); };
    auto poll128 = [&](unsigned vo, unsigned n) AI {
      for (;;) {
        cfence();
        const v4u f = ld128c(rws, vo, 0);
        if (is_tag(f, n) || budget <= 0) break;
        budget -= 8;
        __builtin_amdgcn_s_sleep(1);
      }
      cfence();
    };
    auto weights = [&](int lane, auto ns) AI {
      constexpr int NS = decltype(ns)::value;
      const int i = lane & 31, q4 = (i >> 2) & 3;
      const unsigned nc = NS == 1 ? 1920 : 1728, col0 = colof((i & 16) + 8 * (q4 & 1) + 4 * (q4 >> 1) + (i & 3), NS == 2);
      const rsrc_t rb = __builtin_amdgcn_make_buffer_rsrc((void *)p.b1, 0, 1728 * 2, 0x00020000);
      unsigned braw[NS];
      float cs[NS][10];
      const unsigned vo = col0 < nc ? ((lane >> 5) * nc + col0) * 16 : OOB;
#define CS1(k) "buffer_load_dword %" #k ", %[co], %[rs], %[so] offen sc1\n\ts_add_u32 %[so], %[so], %[cp]\n\t"
#define CSL "s_mov_b32 %[so], %[c0]\n\t" CS1(0) CS1(1) CS1(2) CS1(3) CS1(4) CS1(5) CS1(6) CS1(7) CS1(8) CS1(9)
#define CSO(s) "=&v"(s[0]), "=&v"(s[1]), "=&v"(s[2]), "=&v"(s[3]), "=&v"(s[4]), "=&v"(s[5]), "=&v"(s[6]), "=&v"(s[7]), "=&v"(s[8]), "=&v"(s[9])
      unsigned so;
      if constexpr (NS == 1) {
        asm volatile("s_cmp_lg_u32 %[set], 0\n\ts_cbranch_scc1 .LW%=\n\t" CSL "s_mov_b32 %[so], %[w0]\n\t" XLOAD ".LW%=:"
                     : CSO(cs[0]), "=&{v[96:127]}"(W0), "=&{v[128:159]}"(W1), "=&{v[160:191]}"(W2), "=&{v[192:223]}"(W3), "=&{v[224:255]}"(W4),
                       [so] "=&s"(so)
                     : [vo] "v"(vo), [rs] "s"(rws), [st] "s"(1920 * 32), [co] "v"(col0 < nc ? (lane >> 5) * CSP + col0 * 4 : OOB), [set] "s"(set),
                       [c0] "n"(OFF_CS), [cp] "n"(2 * CSP), [w0] "n"(OFF_W0)
                     : "memory", "scc");
        braw[0] = 0;
        signal(set == 0 ? C_WL + wq * 4 : L_DUMMY, 1u);
      } else {
        lds_st32(L_STG + 2048 + (wq & 1) * 256 + lane * 4, ((lane >> 5) * nc + col0 - 64) * 16);
        signal(C_W1 + (wq & 1) * 4, 1u);
#pragma unroll
        for (int n = 0; n < NS; ++n) {
          const unsigned col = col0 - 64 * n;
          asm volatile("s_nop 4\n\tbuffer_load_ushort %0, %1, %2, 0 offen" : "=&v"(braw[n]) : "v"(col * 2), "s"(rb) : "memory");
          asm volatile(CSL : CSO(cs[n]), [so] "=&s"(so)
                       : [rs] "s"(rws), [co] "v"(col < nc ? (lane >> 5) * CSP + 7680 + col * 4 : OOB), [c0] "n"(OFF_CS), [cp] "n"(2 * CSP)
                       : "memory", "scc");
        }
        w_load(lane, 0);
      }
#pragma unroll
      for (int n = 0; n < NS; ++n) {
        float(&s)[10] = cs[n];
        asm volatile("s_waitcnt vmcnt(%11)" : "+v"(braw[n]), "+v"(s[0]), "+v"(s[1]), "+v"(s[2]), "+v"(s[3]), "+v"(s[4]), "+v"(s[5]),
                     "+v"(s[6]), "+v"(s[7]), "+v"(s[8]), "+v"(s[9]) : "n"(NS == 1 ? 40 : 16) : "memory");
        float bias = bflo(braw[n]);
        if (NS == 1) bias = (wq == 3 && (lane & 20) == 20) ? 1.f : 0.f;
        const float c0 = half_sum((((s[0] + s[1]) + (s[2] + s[3])) + ((s[4] + s[5]) + (s[6] + s[7]))) + (s[8] + s[9]));
        const unsigned h2 = pk2(c0, c0);
        const float cl = c0 - bflo(h2);
        v4u r = v4u{h2, pk2(cl, bias), pk2(bias, 0.f), 0u};
        if (lane >= 32) r = v4u{0u, 0u, 0u, 0u};
        if (n == 0) csf = r;
        else lds_st128(L_STG + (wq & 1) * 1024 + lane * 16, r);
      }
    };
    {
      const int lane = lane_id();
      if (set == 0) poll128(flags_of(lane, false), 0);
      TS(98);
      weights(lane, IC<1>{});
      TS(99);
    }
    unsigned wc = w >> 1 == 2 && step == 1 ? 5 : 0, wo = OFF_W1;

    for (int ph = 0, base = 0; ph < 4; ph += step, base += 8) {
      const int layer = ph >> 1;
      const bool isM = set == (ph & 1);
      int lane = lane_id();
      const int og0 = base + (ph == 0 ? wq - 2 : isM ? 0 : 4 - wq), og1 = !isM ? base + 8 : ph == 0 ? 0 : base + 3 - wq;
      if (wq >= 2 && og0 < og1) {
        v4u pr[4], fl;
        asm volatile("" : "=v"(fl));
        auto rso = [&](int og) AI { return gb + OFF_REC + (ph * 16 + 2 * (7 - (og & 7))) * 2048; };
        const int ogw = ph == 0 && step == 1 ? wq + (WU - 4) : -9;
        const int ogh = w == 7 && !isM && (grp & 3) != 0 ? base + 7 : -9;
        const unsigned id = layer * 16 + c;
        pr[0] = pr[1] = pr[2] = pr[3] = v4u{0u, ~tag.x, 0u, 0u};
        for (int og = og0; og < og1; og += 2) {
          const int u = 7 - (og & 7);
          const unsigned slot = og % 3, xr = C_R + 16 * slot, dst = slot * 40960;
          const unsigned xfa = C_XF + ((og - 3 >= base ? ph : ph - step) & 1) * 16;
          if (og == ogh) {
            if (!is_tag(fl, 16 * ph)) poll128(gb - GRB + OFF_KVF + id * 128, 16 * ph);
            vmw<8>();
            glds4c(rws, lane * 16, gb - GRB + OFF_KV + id * KVP, L_HALO);
            glds(rws, lane * 16, gb - GRB + OFF_KV + id * KVP + 16 * QP - 1024, L_HALO + 16 * QP - 1024);
          }
          {
            unsigned sx = gb + OFF_XF + (ph * 16 + 2 * u) * XT, keep;
            asm volatile("s_nop 4\n" LPROG
                         : "+{v[72:75]}"(pr[0]), "+{v[76:79]}"(pr[1]), "+{v[80:83]}"(pr[2]), "+{v[84:87]}"(pr[3]), [bud] "+s"(budget),
                           [sx] "+s"(sx), [k] "=&s"(keep)
                         : [vo] "v"(lane * 16), [rs] "s"(rws), [ro] "s"(rso(og)), [dst] "s"(dst), [xa] "v"(og < 3 ? C_WL : xfa),
                           [need] "s"(og < 3 ? og != 0 : og - 2), [tx] "s"(tag.x), [ty] "s"(tag.y),
                           [tsb] "s"(L_TS + w * 256 + 68 + 4 * (8 * ph + 7 - u))
                         : "memory", "vcc", "scc", "v88", "v89", "v90", "v91");
          }
          const float S = row_sum2(u2f(pr[0].x) + u2f(pr[1].x), u2f(pr[2].x) + u2f(pr[3].x));
          auto sb = __builtin_amdgcn_permlane16_swap(f2u(S), f2u(S), false, false);
          float q[4];
#pragma unroll
          for (int k = 0; k < 4; ++k) {
            const float d = u2f(pr[k].x) * (1.f / 80.f) - u2f(sb[k >> 1]) * (1.f / 640.f);
            q[k] = u2f(pr[k].z) + 80.f * d * d;
          }
          const float Q = row_sum2(q[0] + q[1], q[2] + q[3]);
          const float nm = S * (-1.f / 640.f), var = Q * (1.f / 640.f) + 1e-5f;
          const float rstd = __builtin_amdgcn_rsqf(var), sd = var * rstd;
          const unsigned mh = pk2(nm, sd);
          lds_st128(L_HST + slot * 512 + (lane & 31) * 16,
                    v4u{pk2(nm, nm - bflo(mh)), mh, pk2(sd - bfhi(mh), 0.f), f2u(rstd) & ~0x80u});
          const unsigned fo = og == ogw ? flags_of(lane, true) : og + 2 == ogh ? gb - GRB + OFF_KVF + id * 128 : OOB;
          asm volatile("s_nop 4\n\t"
                       "buffer_load_dwordx4 %0, %5, %7, %8 offen sc1\n\t"
                       "buffer_load_dwordx4 %1, %5, %7, %8 offen offset:1024 sc1\n\t"
                       "buffer_load_dwordx4 %2, %5, %7, %8 offen offset:2048 sc1\n\t"
                       "buffer_load_dwordx4 %3, %5, %7, %8 offen offset:3072 sc1\n\t"
                       "buffer_load_dwordx4 %4, %6, %7, 0 offen sc1\n\t"
                       "s_waitcnt vmcnt(21)\n\tds_write_b32 %9, %10\n\t"
                       "s_waitcnt vmcnt(5)\n\tds_write_b32 %9, %11\n\t"
                       "s_waitcnt vmcnt(0)"
                       : "=&{v[72:75]}"(pr[0]), "=&{v[76:79]}"(pr[1]), "=&{v[80:83]}"(pr[2]), "=&{v[84:87]}"(pr[3]), "=&v"(fl)
                       : "v"(lane * 16), "v"(fo), "s"(rws), "s"(og + 2 < og1 ? rso(og + 2) : OOB), "v"(xr), "v"(2 * og + 1), "v"(2 * og + 2)
                       : "memory");
          TSD(35 + 2 * (8 * ph + 7 - u));
          if (og == ogw) {
            if (!is_tag(fl, 0)) poll128(fo, 0);
            TS(98);
            weights(lane, IC<2>{});
            TS(99);
          }
          if (og == ogw + 2) w_load(lane, 1);
          if (og == ogw + 4) w_load(lane, 2);
        }
      }
      if (ph == 1 && set == 1) {
        if (wq < 2) csf = lds128(L_STG + wq * 1024 + lane * 16);
        drain();
      }
      if (isM) {
        const unsigned xl = ((lane >> 4) & 1) * XT + (lane & 15) * 16 + (lane >> 5) * 256 - C_R * 2560u;
        const unsigned hl = L_HST + (lane & 31) * 16 - C_R * 32u, ra = C_R + 16 * (base % 3);
#define XIN "{v[96:127]}"(W0), "{v[128:159]}"(W1), "{v[160:191]}"(W2), "{v[192:223]}"(W3), "{v[224:255]}"(W4), "{v[92:95]}"(csf), [xl] "v"(xl), \
            [hl] "v"(hl), [mda] "v"(C_MD + 4 * w), [ra] "s"(ra), [base] "s"(base), [tsb] "s"(L_TS + w * 256 + 4 + 32 * layer)
        if (!live) {
          signal(C_XF + w * 4, base + 8);
        } else if (set == 0) {
          asm volatile(XPROG0 : [bud] "+s"(budget)
                       : XIN, [ql] "v"(L_QKV + (lane & 31) * QP + 64 * wq + (lane >> 5) * 16 - C_R * 544u), [dl] "v"(L_DUMMY + lane * 16)
                       : "memory", "vcc", "scc", XCLOB);
        } else {
          asm volatile("s_nop 4\n" XPROG1 : [bud] "+s"(budget)
                       : XIN, [ql] "v"(((lane >> 4) & 1) * HT + (lane & 15) * 16 + (lane >> 5) * 256), [rs] "s"(rws),
                         [mark] "s"(ph << 30 & 0x80000000u), [h1] "s"(gb + OFF_ACT + 32 * HT + n1 * 1024)
                       : "memory", "vcc", "scc", XCLOB);
        }
        TSP(17 + 16 * layer);
        if (bid == 126 && w == 2 && ph == 0) {
          for (;;) {
            cfence();
            bool ok = true;
#pragma unroll
            for (int k = 0; k < 2; ++k) {
              const v4u a = ld128c(rws, lane * 16 + k * 1024, OFF_ACK);
              ok = ok && a.x == tag.x && a.y == tag.y && a.z == tag.x && a.w == tag.y;
            }
            if (all_true(ok)) { if (lane == 0) st64c(tg(tag, 127), rws, 0, OFF_E); break; }
            if (budget <= 0) break;
            budget -= 8;
            __builtin_amdgcn_s_sleep(2);
          }
        }
      }
      if (wq >= 2 || (ph & 1)) continue;
      lane = lane_id();
      {
        const int g = lane >> 4, t = lane & 15, c4 = lane & 3;
        const bool hi = wq == 0;
        const bool has_halo = (grp & 3) != 0, give_halo = (grp & 3) != 3;
        const int m0 = set ? 0 : 7, m1 = set ? 7 : 8;
        f32x4 cP, cQ;
#pragma unroll
        for (int r = 0; r < 4; ++r) {
          const int j = 4 * g + r;
          cP[r] = j >= t + 9 ? 0.f : -INFINITY;
          cQ[r] = (j <= t && j >= t - 7) ? 0.f : -INFINITY;
        }
        const unsigned mp0 = (4 * g > t ? 0xffffu : 0u) | (4 * g + 1 > t ? 0xffff0000u : 0u);
        const unsigned mp1 = (4 * g + 2 > t ? 0xffffu : 0u) | (4 * g + 3 > t ? 0xffff0000u : 0u);
        const unsigned mqa = g == 2 ? 0u : ~0u, mqb = g == 0 ? 0u : ~0u;
        const unsigned kl = t * QP + 16 * g;
        const unsigned vrow = (4 * g + ((lane >> 2) & 3)) * QP;
        const unsigned v0 = vrow + 160 + c4 * 8, v1a = vrow + (c4 ? 240 : 192), v1b = vrow + (c4 ? 240 : 232);
        const unsigned so1 = lane < 16 ? 4096 + (c & 3) * 256 + lane * 16 : OOB;
        const bool pub = w == 4 && give_halo, late = w == 1;
        const unsigned id = layer * 16 + c, oc = L_QKV + (hi ? 16 * QP : 0), op = L_QKV + (hi ? 0 : 16 * QP), g0 = C_R + (hi ? 8 : 12);
        if (w == 1 && !has_halo) cP = f32x4{-INFINITY, -INFINITY, -INFINITY, -INFINITY};
        if (pub) {
          wait4(C_MD, base + 1, budget);
          v4u d[5];
#pragma unroll
          for (int k = 0; k < 5; ++k) d[k] = lds128(oc + base % 3 * QSL + lane * 16 + (k < 4 ? k * 1024 : 16 * QP - 1024));
          sbar();
#pragma unroll
          for (int k = 0; k < 5; ++k) st128c(d[k], rws, lane * 16 + (k < 4 ? k * 1024 : 16 * QP - 1024), gb + OFF_KV + id * KVP);
        }
        for (unsigned m = m0; m < m1;) {
          const unsigned og = base + m, s0 = og % 3, s1 = (og + 1) % 3, rc = oc + s0 * QSL;
          const unsigned rp = w == 1 ? (has_halo ? (unsigned)L_HALO : rc) : op + (hi ? s0 : s1) * QSL;
          const unsigned sb = step == 2 ? 9 : m < 7 ? 27 : 21;
          const bool first = pub && m < 2;
          asm volatile("s_nop 4\n" APROG
                       : [m] "+s"(m), [bud] "+s"(budget), [wc] "+s"(wc), [wo] "+s"(wo), "+{v[96:127]}"(W0), "+{v[128:159]}"(W1),
                         "+{v[160:191]}"(W2), "+{v[192:223]}"(W3), "+{v[224:255]}"(W4)
                       : [cp] "v"(cP), [cq] "v"(cQ), [mp0] "v"(mp0), [mp1] "v"(mp1), [mqa] "v"(mqa), [mqb] "v"(mqb), [kl] "v"(kl),
                         [kq] "v"(g == 3 ? C_ZERO : kl), [mz] "v"(g == 3 ? 0 : 1), [v0] "v"(v0), [v1] "v"(v1a), [v2] "v"(v1b),
                         [s0] "v"(lane * 16 + (c & 3) * 1024), [s1] "v"(so1), [md] "v"(C_MD), [go] "s"(wc ? C_W1 + wq * 4 : C_ZERO),
                         [va] "v"(L_STG + 2048 + wq * 256 + lane * 4), [rs] "s"(rws),
                         [rc] "s"(rc), [rp] "s"(rp), [sg] "s"(late ? L_DUMMY : g0 + 16 * s1), [c0] "s"(oc), [p0] "s"(op), [g0] "s"(g0),
                         [so] "s"(gb + OFF_ACT + (ph * 16 + 14 - 2 * m + hi) * HT + 5 * (c >> 2) * 1024),
                         [need] "s"(og + (hi || m == 7 ? 1 : 2)), [sv] "s"(2 * og), [sb] "s"(sb), [m1] "s"(first ? 2 : m1)
                         TSARG
                       : "memory", "vcc", "scc", ACLOB);
          if (first && m == 2) {
            vmw<4>();
            st64c(tg(tag, 16 * ph), rws, lane < 2 ? lane * 8 : OOB, gb + OFF_KVF + id * 128);
          }
          if (late) {
            wait1(C_R + 16 * s0 + 12, 2 * og + 7, budget);
            signal(g0 + 16 * s1, 2 * og + sb);
          }
        }
      }
    }
  } else {
    const unsigned lane = lane_id(), g = lane >> 4, hv = lane * 16;
    if (w == 7) {
      __builtin_amdgcn_s_setprio(3);
      f32x32 res[5];
      const unsigned rv = hv < 256 ? hv : OOB;
      unsigned v2 = 2048 + (hv & 240) + ((hv >> 9) << 8) + ((hv & 256) << 22);
      for (;;) {
        cfence();
        const v4u f = ld128c(rws, (7 - (lane >> 3)) * 4096 + (lane & 7) * 256, gb + OFF_REC + sy * 2048);
        if (all_true(f.y == tag.x && f.w == tag.y) || budget <= 0) break;
        budget -= 8;
        __builtin_amdgcn_s_sleep(2);
      }
      cfence();
#pragma unroll
      for (int i = 0; i < 8; ++i) {
        const unsigned xa = L_PART + i * PSZ + (lane & 15) * 160;
        const v4u a = lds128(xa + g * 16), b = lds128(xa + g * 16 + 64);
        const v2u c = lds64(xa + g * 8 + 128);
#pragma unroll
        for (int k = 0; k < 10; ++k) {
          const int e = 20 * i + 2 * k;
          const unsigned v = k < 4 ? a[k & 3] : k < 8 ? b[k & 3] : c[k & 1];
          res[e >> 5][e & 31] = bflo(v); res[e >> 5][(e & 31) + 1] = bfhi(v);
        }
      }
      asm volatile("" : "+{v[96:127]}"(res[0]), "+{v[128:159]}"(res[1]), "+{v[160:191]}"(res[2]), "+{v[192:223]}"(res[3]), "+{v[224:255]}"(res[4]));
      TS(2);
      bar();
      TS(3);
      st64c(tg(tag), rws, lane == 0 ? OFF_ACK + bid * 8 : OOB, 0);
      unsigned pa = L_PART + hv, v0 = hv;
      asm volatile("s_nop 4\n" FPROG
                   : "+{v[96:127]}"(res[0]), "+{v[128:159]}"(res[1]), "+{v[160:191]}"(res[2]), "+{v[192:223]}"(res[3]), "+{v[224:255]}"(res[4]),
                     [pa] "+v"(pa), [hv] "+v"(v0), [v2] "+v"(v2), [bud] "+s"(budget)
                   : [pf] "v"(C_PF), [rv] "v"(rv), [rs] "s"(rws),
                     [rout] "s"(__builtin_amdgcn_make_buffer_rsrc(p.out, 0, 2048 * 1280, 0x00020000)), [xo] "s"(xo + 2 * XT), [ro] "s"(ro + 4096),
                     [os] "s"(row0 + (14 + sy) * 40960 + ct * 160), [tx] "s"(tag.x), [ty] "s"(tag.y), [tsb] "s"(L_TS + w * 256)
                   : "memory", "vcc", "scc", FCLOB);
    } else {
      const bool bias = w == 6;
      const unsigned ones = gb + OFF_ONE + rk * 4096;
#pragma unroll
      for (int k = 0; k < 2; ++k)
        ast128c(v4u{k < 1 ? 0x3f803f80u : 0xbf803f80u, 0x3f803f80u, 0x3f803f80u, 0x3f803f80u}, rws, hv + k * 1024, bias ? ones : OOB);
      const unsigned cb = (80 * ct >> 6) + ((lane >> 3) & 1), l7 = lane & 7;
      poll64(rws, (67 + cb) * 512 + (w + 7 * l7 < 20 ? w + 7 * l7 : w) * 8, OFF_WF, tag, budget);
      TS(100);
      const unsigned vo = g * 10240 + (lane & 15) * 16;
      for (unsigned blk = w; blk < 20; blk += 7) {
#pragma unroll
        for (int j = 0; j < 5; ++j) glds(rws, vo, OFF_W3 + blk * 40960 + ct * 1280 + j * 256, blk * 5120 + j * 1024);
      }
      poll64(rws, (57 + cb) * 512 + umin(8 * w + l7, 54u) * 8, OFF_WF, tag, budget);
      TS(104);
      if (w >= 4) __builtin_amdgcn_s_setprio(1);
      const unsigned q = w & 3, k3 = 5 * q + (w < 3 ? 0 : 3);
      const unsigned wb = OFF_W2 + 8 * w * 40960 + ct * 1280, a5 = gb + OFF_ACT + (30 + sy) * HT + 8 * w * 1024;
      unsigned pw = L_PART + q * 2 * PSZ + hv;
      const unsigned wr0 = k3 * 5120 + hv;
      const unsigned cv = w * 1024 + hv;
      asm volatile("s_nop 4\n" YPROG
                   : [pw] "+v"(pw), [bud] "+s"(budget)
                   : [hv] "v"(hv), [vo] "v"(vo), [rs] "s"(rws), [wb] "s"(wb), [nb] "s"(bias ? 6 : 8), [ct] "s"(ct), [gd] "s"(w < 4 ? -1 : 1),
                     [a5] "s"(a5), [one] "s"(ones),
                     [a3] "s"(gb + OFF_ACT + (14 + sy) * HT + k3 * 1024), [d3] "s"(w < 3 ? 2048u : w == 3 ? OOB : (12 - 4 * q) * 1024),
                     [wr0] "v"(wr0), [wr2] "v"(w < 3 ? wr0 + 10240 : (15 + q) * 5120 + hv), [ga] "v"(w < 4 ? C_FD : C_PL + 4 * q),
                     [sa] "v"((w < 3 ? C_PL : C_PF) + 4 * q), [hi] "s"(w >> 2), [px] "s"((L_PART + q * 2 * PSZ) ^ (L_PART + q * 2 * PSZ + PSZ)),
                     [c0] "v"(cv < 6912 ? cv + ct * 6912 : OOB), [c1] "v"(cv < 2560 ? cv + ct * 2560 + 20 * HT : OOB),
                     [cl] "s"(gb + OFF_ACT + (26 + sy) * HT), [tsb] "s"(L_TS + w * 256)
                   : "memory", "vcc", "scc", YCLOB);
    }
  }
  drain();
}

extern "C" void kernel_launch(void *const *d_in, const int *in_sizes, int n_in, void *d_out, int out_size, void *d_ws,
                              size_t ws_size, hipStream_t stream) {
  Params p;
  p.x = d_in[0]; p.wqkv = d_in[1]; p.wo = d_in[2]; p.w1 = d_in[4]; p.b1 = d_in[5]; p.w2 = d_in[6]; p.b2 = d_in[7];
  p.out = d_out; p.ws = d_ws;
  (void)hipFuncSetAttribute((const void *)k_fwd, hipFuncAttributeMaxDynamicSharedMemorySize, L_TOTAL);
  k_fwd<<<256, 512, L_TOTAL, stream>>>(p);
}
```
